```python
import torch

def _softmax(x, axis=-1):
    return torch.softmax(x, dim=axis)
def _gelu(x):
    return torch.nn.functional.gelu(x, approximate="tanh")
def _silu(x):
    return torch.nn.functional.silu(x)
def _sigmoid(x):
    return torch.sigmoid(x)
def _layernorm(x, axis=-1, eps=1e-5):
    m = x.mean(dim=axis, keepdim=True)
    v = x.var(dim=axis, keepdim=True, unbiased=False)
    return (x - m) / torch.sqrt(v + eps)
def _scatter_add(tgt, idx, src, axis=0):
    return tgt.clone().index_add_(axis, idx, src)
def _segment_sum(src, seg, n):
    out = torch.zeros((n,) + src.shape[1:], dtype=src.dtype)
    return out.index_add_(0, seg, src)
def _pad(x, pw):
    flat = tuple(v for pair in reversed(pw) for v in pair)
    return torch.nn.functional.pad(x, flat)

kernel_name = 'parallel_sum__linear_kernel_hedgehog__dense_activation_relu__prompt'

def setup_inputs():

    _w = (lambda a: (lambda t: t.to(torch.bfloat16) if t.is_floating_point() else t)(torch.as_tensor(a)))
    def _A_setup():
        B, S, D, H = 1, 2048, 512, 32
        Dh = D // H
        return {
            "x": _w((lambda *s: torch.randn(*s, dtype=torch.bfloat16) * 0.05)(B, S, D)),
            "wqkv": _w((lambda *s: torch.randn(*s, dtype=torch.bfloat16) * 0.4)(D, 3 * D)),
            "w_phi": _w((lambda *s: torch.randn(*s, dtype=torch.bfloat16) * 0.4)(H, Dh, Dh) * (Dh ** -0.5)),
            "wo": _w((lambda *s: torch.randn(*s, dtype=torch.bfloat16) * 0.1)(D, D)),
        }
    _A_in = _A_setup()

    def _B_setup():
        B, S, D, Dff = 1, 2048, 512, 1792
        return {
            "x": _w((lambda *s: torch.randn(*s, dtype=torch.bfloat16) * 0.05)(B, S, D)),
            "w1": _w((lambda *s: torch.randn(*s, dtype=torch.bfloat16) * 0.05)(D, Dff)),
            "w2": _w((lambda *s: torch.randn(*s, dtype=torch.bfloat16) * 0.05)(Dff, D)),
        }
    _B_in = _B_setup()

    return {
        "A_x": _A_in["x"],
        "A_wqkv": _A_in["wqkv"],
        "A_w_phi": _A_in["w_phi"],
        "A_wo": _A_in["wo"],
        "B_x": _B_in["x"],
        "B_w1": _B_in["w1"],
        "B_w2": _B_in["w2"],
    }

def reference(A_x, A_wqkv, A_w_phi, A_wo, B_x, B_w1, B_w2):
    def _A_ref(x, wqkv, w_phi, wo):
        B, S, D = x.shape
        H = 32; Dh = D // H
        qkv = (lambda x, *d: x.permute(*d))((x @ wqkv).reshape(B, S, 3, H, Dh), 2, 0, 3, 1, 4)
        q, k, v = qkv[0], qkv[1], qkv[2]
        pq = torch.einsum("bhsd,hdf->bhsf", q, w_phi)
        pk = torch.einsum("bhsd,hdf->bhsf", k, w_phi)
        phi_q = torch.concatenate([_softmax(pq, axis=-1), _softmax(-pq, axis=-1)], axis=-1)
        phi_k = torch.concatenate([_softmax(pk, axis=-1), _softmax(-pk, axis=-1)], axis=-1)
        kv = torch.einsum("bhsf,bhsd->bhfd", phi_k, v)
        z  = torch.einsum("bhsf->bhf", phi_k)
        num = torch.einsum("bhqf,bhfd->bhqd", phi_q, kv)
        den = torch.einsum("bhqf,bhf->bhq", phi_q, z)[..., None] + 1e-6
        o = num / den
        return (lambda x, *d: x.permute(*d))(o, 0, 2, 1, 3).reshape(B, S, D) @ wo

    def _B_ref(x, w1, w2):
        h = torch.einsum("bsd,df->bsf", x, w1)
        zero = torch.zeros_like(h)
        h = torch.maximum(h, zero)
        return torch.einsum("bsf,fd->bsd", h, w2)

    x = A_x
    xn = _layernorm(x, axis=-1)
    return x + _A_ref(xn, A_wqkv, A_w_phi, A_wo) + _B_ref(xn, B_w1, B_w2)
```

```cpp
#include <hip/hip_runtime.h>
#include <hip/hip_bf16.h>
typedef unsigned short u16;
__device__ inline float bf2f(u16 b){ return __uint_as_float(((unsigned)b)<<16);}
__device__ inline u16 f2bf(float f){ unsigned u=__float_as_uint(f); u+=0x7fff+((u>>16)&1); return u>>16; }
constexpr int S=2048,D=512,H=32,DH=16,F=1792;
__global__ void k_ln(const u16* x, float* xn){
  int r=blockIdx.x; int t=threadIdx.x; __shared__ float red[512];
  float v=bf2f(x[r*D+t]); red[t]=v; __syncthreads();
  for(int s=256;s>0;s>>=1){ if(t<s) red[t]+=red[t+s]; __syncthreads(); }
  float m=red[0]/D; __syncthreads();
  red[t]=(v-m)*(v-m); __syncthreads();
  for(int s=256;s>0;s>>=1){ if(t<s) red[t]+=red[t+s]; __syncthreads(); }
  float var=red[0]/D;
  xn[r*D+t]=(v-m)/sqrtf(var+1e-5f);
}
__global__ void k_gemm(const float* A,const u16* B,float* C,int M,int N,int K,int relu){
  int n=blockIdx.x*64+threadIdx.x; int m=blockIdx.y;
  float acc=0; for(int k=0;k<K;k++) acc+=A[m*K+k]*bf2f(B[k*N+n]);
  if(relu) acc=fmaxf(acc,0.f);
  C[m*N+n]=acc;
}
__global__ void k_phi(const float* qkv,const u16* wphi,float* phi,int off){
  int s=blockIdx.x; int h=threadIdx.x;
  float p[16];
  for(int f=0;f<16;f++){ float a=0; for(int d=0;d<16;d++) a+=qkv[s*1536+off+h*16+d]*bf2f(wphi[h*256+d*16+f]); p[f]=a; }
  float mx=p[0],mn=p[0]; for(int f=1;f<16;f++){mx=fmaxf(mx,p[f]);mn=fminf(mn,p[f]);}
  float sp=0,sn=0; for(int f=0;f<16;f++){sp+=expf(p[f]-mx);sn+=expf(mn-p[f]);}
  for(int f=0;f<16;f++){ phi[(s*32+h)*32+f]=expf(p[f]-mx)/sp; phi[(s*32+h)*32+16+f]=expf(mn-p[f])/sn; }
}
__global__ void k_kv(const float* phik,const float* qkv,float* kv){
  int h=blockIdx.x; int f=threadIdx.x/17, d=threadIdx.x%17; if(f>=32) return;
  float a=0; for(int s=0;s<S;s++) a+=phik[(s*32+h)*32+f]*(d<16?qkv[s*1536+1024+h*16+d]:1.f);
  kv[(h*32+f)*17+d]=a;
}
__global__ void k_o(const float* phiq,const float* kv,float* o){
  int s=blockIdx.x; int c=threadIdx.x; int h=c/16,d=c%16;
  float num=0,den=0; for(int f=0;f<32;f++){ float p=phiq[(s*32+h)*32+f]; num+=p*kv[(h*32+f)*17+d]; den+=p*kv[(h*32+f)*17+16]; }
  o[s*D+c]=num/(den+1e-6f);
}
__global__ void k_fin(const u16* x,const float* a,const float* b,u16* out){
  int i=blockIdx.x*256+threadIdx.x; out[i]=f2bf(bf2f(x[i])+a[i]+b[i]);
}
extern "C" void kernel_launch(void* const* d_in, const int* in_sizes, int n_in, void* d_out, int out_size, void* d_ws, size_t ws_size, hipStream_t stream){
  const u16* x=(const u16*)d_in[0]; const u16* wqkv=(const u16*)d_in[1]; const u16* wphi=(const u16*)d_in[2]; const u16* wo=(const u16*)d_in[3];
  const u16* w1=(const u16*)d_in[5]; const u16* w2=(const u16*)d_in[6];
  float* ws=(float*)d_ws; float* xn=ws; float* qkv=xn+S*D; float* hh=qkv+S*1536; float* phiq=hh+S*F; float* phik=phiq+S*1024; float* kv=phik+S*1024; float* o=kv+32*32*17; float* a=o+S*D; float* b=a+S*D;
  k_ln<<<S,512,0,stream>>>(x,xn);
  k_gemm<<<dim3(1536/64,S),64,0,stream>>>(xn,wqkv,qkv,S,1536,512,0);
  k_gemm<<<dim3(F/64,S),64,0,stream>>>(xn,w1,hh,S,F,512,1);
  k_phi<<<S,32,0,stream>>>(qkv,wphi,phiq,0);
  k_phi<<<S,32,0,stream>>>(qkv,wphi,phik,512);
  k_kv<<<32,544,0,stream>>>(phik,qkv,kv);
  k_o<<<S,512,0,stream>>>(phiq,kv,o);
  k_gemm<<<dim3(512/64,S),64,0,stream>>>(o,wo,a,S,512,512,0);
  k_gemm<<<dim3(512/64,S),64,0,stream>>>(hh,w2,b,S,512,F,0);
  k_fin<<<S*D/256,256,0,stream>>>(x,a,b,(u16*)d_out);
}
```

```cpp
#include <hip/hip_runtime.h>
#pragma clang diagnostic ignored "-Wint-to-pointer-cast"
typedef unsigned short u16;
typedef unsigned u32;
typedef unsigned long long u64;
typedef u32 u32x2 __attribute__((ext_vector_type(2)));
typedef u32 u32x4 __attribute__((ext_vector_type(4)));
typedef short s16x2 __attribute__((ext_vector_type(2)));
typedef short s16x4 __attribute__((ext_vector_type(4)));
typedef float f32x2 __attribute__((ext_vector_type(2)));
typedef float f32x4 __attribute__((ext_vector_type(4)));
typedef float f32x16 __attribute__((ext_vector_type(16)));
typedef __bf16 b16x2 __attribute__((ext_vector_type(2)));
typedef __bf16 b16x8 __attribute__((ext_vector_type(8)));
#define LAS __attribute__((address_space(3)))
#define GAS __attribute__((address_space(1)))
#define DI __device__ __forceinline__
#define SB() __builtin_amdgcn_sched_barrier(0)
#define BAR() __builtin_amdgcn_s_barrier()
#define WAITV(n) asm volatile("s_waitcnt vmcnt(%0)" ::"i"(n) : "memory")
#define WAITL0() asm volatile("s_waitcnt lgkmcnt(0)" ::: "memory")
#define OPAQUE(x) asm volatile("" : "+v"(x))

#define PRIO_C 0
#define PRIO_H 1
constexpr u32 HF_OFF = 0;
constexpr u32 QF_OFF = 64u * 112u * 1024u;
constexpr u32 OF_OFF = QF_OFF + 64u * 32u * 1024u;
constexpr u32 KV_OFF = OF_OFF + 64u * 32u * 1024u;
constexpr u32 CS_OFF = KV_OFF + 32u * 16u * 2176u;
constexpr u32 RS_OFF = CS_OFF + 256u * 1024u;
constexpr u32 MB_OFF = RS_OFF + 2048u * 8u;
constexpr u32 EP_OFF = MB_OFF + 256u * 1024u;
constexpr u32 WS_BYTES = EP_OFF + 2048u;
constexpr u32 LX = 0, LWA = 49152, LWB = 98304, LBS = 135168, LSCR = 159744;
constexpr u32 lx(int s) { return LX + s * 16384; }
constexpr u32 lwa(int s) { return LWA + s * 16384; }
constexpr u32 lwb(int s) { return LWB + s * 12288; }
constexpr u32 L_WPHI = LSCR, L_WPA = LSCR + 2048, L_RS = LSCR + 3072, L_SEM = LSCR + 3584;
constexpr u32 L_SP0 = lwb(0) + 8192, L_SP1 = lwb(1) + 8192, L_SP2 = lwb(2) + 8192;
constexpr int AUX = 16;

DI f32x16 mfma(u32x4 a, u32x4 b, f32x16 c) {
  return __builtin_amdgcn_mfma_f32_32x32x16_bf16(__builtin_bit_cast(b16x8, a), __builtin_bit_cast(b16x8, b), c, 0, 0, 0);
}
DI void mma(f32x16& d, u32x4 a, u32x4 b) { asm volatile("v_mfma_f32_32x32x16_bf16 %0, %1, %2, %0" : "+v"(d) : "v"(a), "v"(b)); }
DI void mma0(f32x16& d, u32x4 a, u32x4 b) { asm volatile("v_mfma_f32_32x32x16_bf16 %0, %1, %2, 0" : "=&v"(d) : "v"(a), "v"(b)); }
DI u32x2 trd(u32 addr) { return __builtin_bit_cast(u32x2, __builtin_amdgcn_ds_read_tr16_b64_v4i16((LAS s16x4*)addr)); }
DI u32x4 trd2(u32 a0, u32 a1) {
  u32x2 lo = trd(a0), hi = trd(a1);
  return __builtin_shufflevector(lo, hi, 0, 1, 2, 3);
}
DI u32x4 lds128(u32 addr) { return *(LAS u32x4*)addr; }
DI f32x4 ldsf4(u32 addr) { return *(LAS f32x4*)addr; }
DI u32 pk(float a, float b) {
  f32x2 v = {a, b};
  return __builtin_bit_cast(u32, __builtin_convertvector(v, b16x2));
}
DI u32 pk2(f32x2 v) { return __builtin_bit_cast(u32, __builtin_convertvector(v, b16x2)); }
DI u32 relu2(u32 v) { return __builtin_bit_cast(u32, __builtin_elementwise_max(__builtin_bit_cast(s16x2, v), s16x2{0, 0})); }
extern "C" __device__ float fmul_legacy_(float, float) __asm("llvm.amdgcn.fmul.legacy");
DI float mul1(float a, float b) { return fmul_legacy_(a, b); }
DI float mx(float a, float b) { return __builtin_elementwise_maximum(a, b); }
DI float dot2(u32 a, u32 b, float c) {
  return __builtin_amdgcn_fdot2_f32_bf16(__builtin_bit_cast(b16x2, a), __builtin_bit_cast(b16x2, b), c, false);
}
DI void swap32(float& a, float& b) {
  auto r = __builtin_amdgcn_permlane32_swap(__float_as_uint(a), __float_as_uint(b), false, false);
  a = __uint_as_float(r[0]);
  b = __uint_as_float(r[1]);
}
template <int I> DI f32x2 pr(const f32x16& a) { return __builtin_shufflevector(a, a, I, I + 1); }
template <int I> DI f32x4 q4(const f32x16& a) { return __builtin_shufflevector(a, a, I, I + 1, I + 2, I + 3); }
DI u32x4 make_srd(const void* p, u32 bytes) {
  u64 a = (u64)p;
  u32x4 r = {(u32)a, (u32)(a >> 32) & 0xffffu, bytes, 0x00020000u};
  return r;
}
template <u32 LI, u32 SI>
DI void dma(u32x4 srd, u32 voff, u32 sbase, u32 lbase) {
  u32 t;
  asm volatile("s_add_u32 m0, %4, %6\n\ts_add_u32 %0, %3, %5\n\tbuffer_load_dwordx4 %1, %2, %0 offen lds"
               : "=&s"(t)
               : "v"(voff), "s"(srd), "s"(sbase), "s"(lbase), "i"(SI), "i"(LI)
               : "memory", "scc");
}
DI void st128(u32x4 v, __amdgpu_buffer_rsrc_t rs, u32 voff, u32 soff) {
  __builtin_amdgcn_raw_buffer_store_b128(v, rs, voff, soff, AUX);
  asm volatile("s_nop 1" ::"v"(v) : "memory");
}
DI u32x4 ld128(__amdgpu_buffer_rsrc_t rs, u32 voff, u32 soff) { return __builtin_amdgcn_raw_buffer_load_b128(rs, voff, soff, AUX); }
template <int OFF> DI void gld32(float& d, u32 voff, const char* base) {
  asm volatile("global_load_dword %0, %1, %2 offset:%3 sc1" : "=v"(d) : "v"(voff), "s"(base), "i"(OFF) : "memory");
}
template <int N> struct IC { static constexpr int v = N; };
template <int B, int E, class F> DI void sfor(F&& f) {
  if constexpr (B < E) {
    f(IC<B>{});
    sfor<B + 1, E>(f);
  }
}
constexpr u32 ONES2 = 0x3f803f80u;
constexpr float LOG2E = 1.4426950408889634f;

DI void dual_softmax(const f32x16& P, u32x4& o0, u32x4& o1) {
  float m0 = mx(mx(mx(mx(mx(mx(mx(P[0], P[1]), P[2]), P[3]), P[4]), P[5]), P[6]), P[7]);
  float m1 = mx(mx(mx(mx(mx(mx(mx(P[8], P[9]), P[10]), P[11]), P[12]), P[13]), P[14]), P[15]);
  swap32(m0, m1);
  m0 = mx(m0, m1) * -LOG2E;
  m1 = m0;
  swap32(m0, m1);
  f32x2 e[8];
  sfor<0, 8>([&](auto II) {
    constexpr int I = decltype(II)::v;
    const float m = I < 4 ? m0 : m1;
    const f32x2 t = __builtin_elementwise_fma(pr<2 * I>(P), f32x2{LOG2E, LOG2E}, f32x2{m, m});
    e[I] = f32x2{__builtin_amdgcn_exp2f(t[0]), __builtin_amdgcn_exp2f(t[1])};
  });
  const f32x2 s0 = (e[0] + e[1]) + (e[2] + e[3]), s1 = (e[4] + e[5]) + (e[6] + e[7]);
  m0 = s0[0] + s0[1];
  OPAQUE(m0);
  m1 = s1[0] + s1[1];
  swap32(m0, m1);
  m0 = __builtin_amdgcn_rcpf(m0 + m1);
  m1 = m0;
  swap32(m0, m1);
#pragma unroll
  for (int i = 0; i < 4; i++) {
    o0[i] = pk2(e[i] * m0);
    o1[i] = pk2(e[4 + i] * m1);
  }
}
DI u32x4 wphi_frag(u32 a, u32 sg) {
  u32x4 f = trd2(a, a + 256);
  f[0] ^= sg; f[1] ^= sg; f[2] ^= sg; f[3] ^= sg;
  return f;
}

constexpr u32 slot_loc(int s, int k) { return k < 2 ? lx(s) + k * 8192 : k < 4 ? lwa(s) + (k - 2) * 8192 : lwb(s); }
constexpr u32 bloc(int n) {
  return n < 8 ? LBS + (n & 1) * 8192 : n < 13 ? slot_loc(2, n - 8) : n < 18 ? slot_loc(0, n - 13) : n < 23 ? slot_loc(1, n - 18)
       : n < 26 ? LBS + (n - 23) * 8192 : n < 31 ? slot_loc(2, n - 26) : slot_loc(0, n - 31);
}
constexpr u32 L_ST = LBS + 16384;

#define STAMP(c, i) do {} while (0)
struct Ctx {
  mutable u32 dbg;
  __amdgpu_buffer_rsrc_t ws;
  char* wsp;
  u32 m, lane;
  u32 ab[3][2];
  u32 seg[4];
  u64 tag;
  u32 fl;
};
template <int N> DI u32x4 rdb(const Ctx& c, int cb) {
  constexpr u32 W = bloc(N) / 57344;
  const u32 a = c.ab[W][cb] + (bloc(N) - W * 57344);
  return trd2(a, a + 1024);
}
DI void init_ab(Ctx& c) {
  const u32 lane = c.lane, m = c.m, hh = lane >> 5, blk = (lane >> 4) & 1, q = (lane & 15) >> 2, p = lane & 3;
#pragma unroll
  for (int cb = 0; cb < 2; cb++) {
    const u32 t = m * 2048 + (4 * hh + q) * 128 + (((4 * (cb ^ (m & 1)) + 2 * blk + (p >> 1)) ^ ((q >> 1) << 2)) << 4) + 8 * (p & 1);
#pragma unroll
    for (int k = 0; k < 3; k++) {
      c.ab[k][cb] = t + k * 57344;
      OPAQUE(c.ab[k][cb]);
    }
  }
}
DI void publish(const Ctx& c, u32 first, u32 n = 8) {
  if (c.lane - first < n) asm volatile("global_atomic_swap_x2 %0, %1, %2 sc1" ::"v"(c.fl), "v"(c.tag), "s"(c.wsp) : "memory");
}
DI u64 own_tag(const Ctx& c) {
  return ((u64)(u32)__builtin_amdgcn_readlane((u32)(c.tag >> 32), 63) << 32) | (u32)__builtin_amdgcn_readlane((u32)c.tag, 63);
}
DI u64 peek(const Ctx& c, u32 off, u32 n) {
  u64 v = own_tag(c);
  const u64 z = 0;
  if (c.lane < n) asm volatile("global_atomic_or_x2 %0, %1, %2, %3 sc0 sc1" : "+v"(v) : "v"(off + c.lane * 8), "v"(z), "s"(c.wsp) : "memory");
  return v;
}
DI void await(const Ctx& c, u32 off, u32 n, u64 v) {
  const u64 want = own_tag(c);
#pragma unroll 1
  for (int it = 0; it < (1 << 16); it++) {
    asm volatile("s_waitcnt vmcnt(0)" : "+v"(v)::"memory");
    if (__all(v == want)) break;
    __builtin_amdgcn_s_sleep(2);
    v = peek(c, off, n);
  }
}
DI void sem_post(const Ctx& c, u32 idx) {
  WAITL0();
  if (c.lane == 0) asm volatile("ds_add_u32 %0, %1" ::"v"(L_SEM + idx * 4), "v"(1u) : "memory");
}
DI void sem_wait(u32 idx, u32 n) {
#pragma unroll 1
  for (int it = 0; it < (1 << 20); it++) {
    if (__builtin_amdgcn_readfirstlane(*(volatile LAS u32*)(L_SEM + idx * 4)) >= n) break;
    __builtin_amdgcn_s_sleep(1);
  }
  asm volatile("" ::: "memory");
}
constexpr u32 SEM_B = 0, SEM_FREE = 4, SEM_A = 8, SEM_KV = 9, SEM_ST = 10;

struct Tile {
  int rt, j, ct;
  u32 c0, c1, c2, cc;
};
template <bool FFN> DI Tile make_tile(int rt, int j) {
  Tile t;
  t.rt = rt;
  t.j = j;
  t.ct = 2 * j + (FFN ? 1 : 0);
  if (FFN) {
    t.c0 = 448 * j + ((j & 1) ? 64 : 0);
    t.c1 = t.c0 + 128;
    t.c2 = t.c0 + 256;
    t.cc = 448 * j + ((j & 1) ? 0 : 384);
  } else { t.c0 = 1024 + 128 * j; t.c1 = 128 * j; t.c2 = 2048 + 128 * j; t.cc = 0; }
  return t;
}
DI void load_tag(Ctx& c, u32 tgt, u32 slot) {
  if (c.lane >= 32) tgt = blockIdx.x;
  c.fl = MB_OFF + tgt * 1024 + slot * 8;
  asm volatile("global_load_dwordx2 %0, %1, %2 sc1" : "=v"(c.tag) : "v"(EP_OFF + tgt * 8), "s"(c.wsp) : "memory");
}
DI u32 blk_of(u32 rt, u32 ct) { return (((rt & 7) * 4 + (ct & 3)) << 3) + (rt >> 3) * 4 + (ct >> 2); }
DI void phi_store(u32 apw, const f32x16& P) {
  u32x4 o0, o1;
  dual_softmax(P, o0, o1);
  *(LAS u32x2*)(apw) = u32x2{o0[0], o0[1]};
  *(LAS u32x2*)(apw ^ 16) = u32x2{o0[2], o0[3]};
  *(LAS u32x2*)(apw ^ 32) = u32x2{o1[0], o1[1]};
  *(LAS u32x2*)(apw ^ 48) = u32x2{o1[2], o1[3]};
}

DI u32 finish_idx(const Ctx& c, int H) {
  const int b = blockIdx.x, i = (b & 7) * 4 + (b >> 6), jc = (b >> 3) & 7;
  return (64 * i + 32 * (c.m >> 1) + (c.lane & 31)) * 512 + 64 * jc + 32 * (c.m & 1) + 16 * H + 8 * (c.lane >> 5);
}
template <int H> DI void finish(const Ctx& c, f32x4 v0, f32x4 v1, u32x4 xr, u16* dst) {
  sfor<0, 4>([&](auto KK) {
    constexpr int k = decltype(KK)::v;
    const u32 a = (c.m ^ k) * 2048 + c.lane * 16;
    f32x4 s0 = ldsf4(a + bloc(18 + 2 * k + H)), s1 = ldsf4(a + bloc(18 + 2 * k + H) + 1024);
    if constexpr (k > 0 || H == 1) {
      s0 += ldsf4(a + bloc(25 + 2 * k + H));
      s1 += ldsf4(a + bloc(25 + 2 * k + H) + 1024);
    }
    v0 += s0;
    v1 += s1;
  });
  float e[8] = {v0[0], v0[1], v0[2], v0[3], v1[0], v1[1], v1[2], v1[3]};
#pragma unroll
  for (int k = 0; k < 4; k++) swap32(e[k], e[4 + k]);
  u32x4 o;
#pragma unroll
  for (int d = 0; d < 4; d++) o[d] = pk(e[2 * d] + __uint_as_float(xr[d] << 16), e[2 * d + 1] + __uint_as_float(xr[d] & 0xffff0000u));
  *(GAS u32x4*)dst = o;
}

template <bool FFN>
DI void helper(Ctx& c, const Tile t, const u16* x, const u16* wt, const u16* wphi, const u16* wo, const u16* w2, u16* out) {
  constexpr u32 LDW = FFN ? 3584 : 3072;
  constexpr int NBK = FFN ? 7 : 6, ND = FFN ? 11 : 10;
  const u32 lane = c.lane, m = c.m;
  const int b = blockIdx.x, jc = (b >> 3) & 7, i = (b & 7) * 4 + (b >> 6);
  const u32x4 srd_x = make_srd(x, 2048 * 512 * 2), srd_w = make_srd(wt, 512 * LDW), srd_w2 = make_srd(w2, 1792 * 1024), srd_wo = make_srd(wo, 512 * 1024);
  const u32 vx0 = (lane >> 3) * 1024 + (((lane & 7) ^ (lane >> 4)) << 4);
  const u32 vx1 = vx0 ^ 64;
  const u32 sw = ((lane & 7) ^ (((lane >> 4) & 1) << 2)) << 4;
  const u32 vw = (lane >> 3) * LDW + sw, vb = (lane >> 3) * 1024 + sw;
  const u32 vc = (lane >> 2) * LDW + ((lane & 3) << 4);
  const u32 sx = (t.rt * 128 + m * 32) * 1024;
  const u32 s0 = m * 16 * LDW + t.c0, s1 = m * 16 * LDW + t.c1, s2 = m * 16 * LDW + t.c2, sc = m * 16 * LDW + t.cc;
  const u32 d4 = m * 4096, d2 = m * 2048, d1 = m * 1024;
  auto ring1 = [&](auto GG, auto II) {
    constexpr int G = decltype(GG)::v, S = G % 3, I = decltype(II)::v;
    if constexpr (I < 4) dma<lx(S) + I * 1024, I * 8192 + G * 128>(srd_x, (I & 1) ? vx1 : vx0, sx, d4);
    else if constexpr (I < 6) dma<lwa(S) + (I & 1) * 1024, (G * 64 + (I & 1) * 8) * LDW>(srd_w, vw, s0, d2);
    else if constexpr (I < 8) dma<lwa(S) + 8192 + (I & 1) * 1024, (G * 64 + (I & 1) * 8) * LDW>(srd_w, vw, s1, d2);
    else if constexpr (I < 10) dma<lwb(S) + (I & 1) * 1024, (G * 64 + (I & 1) * 8) * LDW>(srd_w, vw, s2, d2);
    else dma<lwb(S) + 8192, G * 64 * LDW>(srd_w, vc, sc, d1);
  };
  constexpr int PRE = 5;
  sfor<0, PRE>([&](auto II) { ring1(IC<1>{}, II); });
  u32 sb[4];
#pragma unroll
  for (int k = 0; k < 4; k++) sb[k] = (m * 448 + c.seg[k] * 16) * 1024 + jc * 128;
  const u32 sbo = m * 128 * 1024 + jc * 128;
  auto issue_h = [&](auto HH) {
    constexpr int N = decltype(HH)::v >> 1, H = decltype(HH)::v & 1;
    if constexpr (N < 28) dma<bloc(N) + H * 1024, (N % 7) * 16384 + H * 8192>(srd_w2, vb, sb[N / 7], d2);
    else dma<bloc(N) + H * 1024, (N - 28) * 16384 + H * 8192>(srd_wo, vb, sbo, d2);
  };
  auto issue_b = [&](auto NN) {
    issue_h(IC<2 * decltype(NN)::v>{});
    issue_h(IC<2 * decltype(NN)::v + 1>{});
  };
  const u32 hh = lane >> 5, blk = (lane >> 4) & 1, q = (lane & 15) >> 2, p = lane & 3, r = lane & 31;
  init_ab(c);
  u32x4 bf[18][2];
  auto ldb = [&](auto NN) {
    constexpr int N = decltype(NN)::v;
    bf[N][0] = rdb<N>(c, 0);
    bf[N][1] = rdb<N>(c, 1);
  };
  u64 ps = 0, grs = 0;
  float gc[16];
  u32x4 xa, xb;
  __builtin_amdgcn_s_setprio(1);
  STAMP(c, 1);
  WAITV(PRE);
  BAR();
  STAMP(c, 2);
  sfor<PRE, ND>([&](auto II) { ring1(IC<1>{}, II); });
  {
    const u32 xo = (t.rt * 128 + t.ct * 8 + m * 2) * 1024 + lane * 16;
    asm volatile("global_load_dwordx4 %0, %1, %2" : "=v"(xa) : "v"(xo), "s"(x) : "memory");
    asm volatile("global_load_dwordx4 %0, %1, %2 offset:1024" : "=v"(xb) : "v"(xo), "s"(x) : "memory");
    const u32 so = (t.rt * 32 + m * 8) * LDW;
    dma<LBS, 0>(srd_w, vw, so + t.c0, d2);
    dma<LBS + 1024, 0>(srd_w, vw, so + t.c1, d2);
    dma<LBS + 8192, 0>(srd_w, vw, so + t.c2, d2);
    if constexpr (FFN) dma<LBS + 9216, 0>(srd_w, vc, so + t.cc, d2);
    load_tag(c, lane < 16 ? blk_of(t.rt, lane) : blk_of(lane & 15, t.ct), 64 + (lane < 16 ? t.ct : 16 + t.rt));
    const u32x4 srd_p = make_srd(wphi, 32 * 512);
    dma<L_WPHI, 0>(srd_p, lane * 16, m < 2 ? (u32)(t.j * 2048 + m * 1024) : (u32)(((b & 7) * 2 + ((b >> 4) & 1)) * 1024), (m < 2 ? m : 2) * 1024);
  }
  constexpr int NSL = FFN ? 8 : 7;
  sfor<0, 8>([&](auto GG) {
    constexpr int G = decltype(GG)::v;
    constexpr int NR = G + 2 < 8 ? ND : 10;
    if constexpr (G >= 3) ldb(IC<G - 3>{});
    if constexpr (G == 2) {
      const f32x4 u0 = ldsf4(L_ST + (m * 64 + lane) * 32), u1 = ldsf4(L_ST + (m * 64 + lane) * 32 + 16);
      float a = u0[0];
#pragma unroll
      for (int k = 1; k < 8; k++) {
        a += k < 4 ? u0[k & 3] : u1[k & 3];
        OPAQUE(a);
      }
      __builtin_amdgcn_raw_buffer_store_b32(__float_as_uint(a), c.ws, lane * 4, CS_OFF + (t.ct * 16 + t.rt) * 1024 + m * 256, AUX);
    }
    if constexpr (G == 3) {
      if (m == 0) publish(c, 0, 32);
    }
    if constexpr (G == 5) ps = peek(c, MB_OFF + b * 1024 + 512, 32);
    if constexpr (G == 6) {
      OPAQUE(ps);
      if (!__all(ps == own_tag(c))) await(c, MB_OFF + b * 1024 + 512, 32, ps);
      asm volatile("global_load_dwordx2 %0, %1, %2 sc1" : "=v"(grs) : "v"(RS_OFF + (t.rt * 128 + m * 32 + r) * 8), "s"(c.wsp) : "memory");
      const u32 bc = CS_OFF + t.ct * 16384 + m * 256 + lane * 4;
      sfor<0, 16>([&](auto KK) {
        constexpr int k = decltype(KK)::v;
        gld32<(k & 3) * 1024>(gc[k], bc + (k >> 2) * 4096, c.wsp);
      });
    }
    if constexpr (G == 7) {
      float cs = 0.f;
#pragma unroll
      for (int k = 0; k < 16; k++) {
        OPAQUE(gc[k]);
        cs += gc[k];
      }
      OPAQUE(grs);
      *(LAS u32*)(L_SP0 + 2048 + m * 128 + r * 4) = (u32)grs;
      *(LAS u32*)(L_RS + m * 128 + r * 4) = (u32)(grs >> 32);
      const u32 h2 = pk(cs, cs);
      const float lo = cs - __uint_as_float(h2 & 0xffff0000u);
      *(LAS u32x2*)(L_SP0 + (m * 64 + lane) * 8) = u32x2{h2, pk(lo, lo)};
    }
    if constexpr (G + 2 < 8) sfor<0, ND>([&](auto II) { ring1(IC<G + 2>{}, II); });
    else sfor<8 + (G - 6) * 5, 13 + (G - 6) * 5>(issue_b);
    SB();
    if constexpr (G == 1) {
      WAITV(2 * ND);
      OPAQUE(xa);
      OPAQUE(xb);
      OPAQUE(c.tag);
      c.tag += 1;
      const u32 a = LBS + m * 2048 + (4 * hh + q) * 128 + (((2 * blk + (p >> 1)) ^ ((q >> 1) << 2)) << 4) + 8 * (p & 1);
      u32x2 wv[NBK];
#pragma unroll
      for (int k = 0; k < NBK; k++)
        wv[k] = trd(k < 6 ? (a ^ ((k & 1) * 64)) + (k < 4 ? (k >> 1) * 1024 : 8192) : LBS + 9216 + m * 2048 + (4 * hh + q) * 64 + 32 * blk + 8 * p);
      float st[4] = {0.f, 0.f, 0.f, 0.f};
#pragma unroll
      for (int e = 0; e < 16; e++) {
        const u32 d = (e & 8) ? xb[(e >> 1) & 3] : xa[(e >> 1) & 3];
        const float v = __uint_as_float((e & 1) ? d & 0xffff0000u : d << 16);
        st[(e >> 3) * 2] += v;
        OPAQUE(st[(e >> 3) * 2]);
        st[(e >> 3) * 2 + 1] = __builtin_fmaf(v, v, st[(e >> 3) * 2 + 1]);
      }
      swap32(st[0], st[1]);
      swap32(st[2], st[3]);
      float ta = st[0] + st[1], tb = st[2] + st[3];
      OPAQUE(ta);
      auto sw16 = __builtin_amdgcn_permlane16_swap(__float_as_uint(ta), __float_as_uint(tb), false, false);
      float u = __uint_as_float(sw16[0]) + __uint_as_float(sw16[1]);
      u += __int_as_float(__builtin_amdgcn_update_dpp(0, __float_as_int(u), 0x128, 0xf, 0xf, false));
      u += __int_as_float(__builtin_amdgcn_update_dpp(0, __float_as_int(u), 0x124, 0xf, 0xf, false));
      u += __int_as_float(__builtin_amdgcn_update_dpp(0, __float_as_int(u), 0x122, 0xf, 0xf, false));
      u += __int_as_float(__builtin_amdgcn_update_dpp(0, __float_as_int(u), 0x121, 0xf, 0xf, false));
      float mean = u * (1.f / 512.f), rstd = mean;
      swap32(mean, rstd);
      rstd = __builtin_amdgcn_rsqf(mx(__builtin_fmaf(-mean, mean, rstd), 0.f) + 1e-5f);
      const u32 mh = pk(-mean, -mean);
      const float ml = -mean - __uint_as_float(mh & 0xffff0000u);
      __builtin_amdgcn_raw_buffer_store_b64(u32x2{(mh & 0xffffu) | (pk(ml, ml) & 0xffff0000u), __float_as_uint(rstd)}, c.ws, blk * 8,
                                            RS_OFF + (t.rt * 128 + t.ct * 8 + m * 2) * 8, AUX);
#pragma unroll
      for (int k = 0; k < NBK; k++) {
        float a0 = __uint_as_float(wv[k][0] << 16) + __uint_as_float(wv[k][0] & 0xffff0000u);
        OPAQUE(a0);
        a0 += __uint_as_float(wv[k][1] << 16);
        OPAQUE(a0);
        *(LAS float*)(L_ST + (k * 32 + r) * 32 + (m * 2 + hh) * 4) = a0 + __uint_as_float(wv[k][1] & 0xffff0000u);
      }
    }
    WAITL0();
    if constexpr (G >= 1) issue_b(IC<G - 1>{});
    STAMP(c, 3 + 2 * G);
    WAITV(G == 0 ? NSL + ND : G == 1 ? ND + 3 : NR + 2);
    BAR();
    STAMP(c, 4 + 2 * G);
  });
  __builtin_amdgcn_s_setprio(0);
  if constexpr (!FFN) {
    BAR();
    const u32 awp = L_WPHI + 1024 + (4 * hh + q) * 32 + p * 8, sg = blk ? 0x80008000u : 0u;
    const f32x16 P0 = mfma(wphi_frag(awp, sg), lds128(lwb(1) + m * 2048 + lane * 16), f32x16{});
    const f32x16 P1 = mfma(wphi_frag(awp + 512, sg), lds128(lwb(1) + m * 2048 + 1024 + lane * 16), f32x16{});
    const u32 apw = lwa(1) + m * 2048 + r * 64 + ((hh ^ ((r >> 1) & 7)) << 3);
    phi_store(apw, P0);
    phi_store(apw + 8192, P1);
    WAITL0();
    BAR();
  }
  ldb(IC<5>{});
  sfor<8, 13>(ldb);
  WAITL0();
  issue_b(IC<7>{});
  WAITV(2);
  ldb(IC<6>{});
  sfor<13, 18>(ldb);
  sem_wait(FFN ? SEM_ST : SEM_KV, 4);
  WAITL0();
  STAMP(c, 19);
  sfor<18, 24>(issue_b);
  issue_b(IC<25>{});
  WAITV(14);
  ldb(IC<7>{});
  sfor<26, 36>(issue_b);
  WAITL0();
  issue_b(IC<24>{});
  STAMP(c, 20);
  const u32 fh = MB_OFF + b * 1024 + 32 * m;
  await(c, fh, 4, peek(c, fh, 4));
  sem_post(c, SEM_B + m);
  STAMP(c, 24);
  u32 ah[3];
#pragma unroll
  for (int k = 0; k < 3; k++) ah[k] = HF_OFF + ((2 * i) * 112 + 28 * m + c.seg[k]) * 1024;
  const u32 va[2] = {lane * 16 + (m >> 1) * 114688, lane * 16 + ((m >> 1) ^ 1) * 114688};
  u32x4 af[18][2];
  auto lda = [&](auto NN) {
    constexpr int N = decltype(NN)::v;
    af[N][0] = ld128(c.ws, va[0], ah[N / 7] + (N % 7) * 1024);
    af[N][1] = ld128(c.ws, va[1], ah[N / 7] + (N % 7) * 1024);
  };
  sfor<0, 3>(lda);
  SB();
  f32x16 acc[4];
  sfor<0, 18>([&](auto NN) {
    constexpr int N = decltype(NN)::v;
    if constexpr (N == 0) {
      mma0(acc[0], bf[N][0], af[N][0]); mma0(acc[1], bf[N][1], af[N][0]); mma0(acc[2], bf[N][0], af[N][1]); mma0(acc[3], bf[N][1], af[N][1]);
    } else {
      mma(acc[0], bf[N][0], af[N][0]); mma(acc[1], bf[N][1], af[N][0]); mma(acc[2], bf[N][0], af[N][1]); mma(acc[3], bf[N][1], af[N][1]);
    }
    SB();
    constexpr int lo = N < 5 ? 3 + 2 * N : N + 8, hi = N < 5 ? 5 + 2 * N : N + 9;
    sfor<(lo < 18 ? lo : 18), (hi < 18 ? hi : 18)>(lda);
    SB();
  });
  STAMP(c, 29);
  sem_wait(SEM_FREE + m, 1);
  asm volatile("s_nop 15" : "+v"(acc[0]), "+v"(acc[1]), "+v"(acc[2]), "+v"(acc[3]));
  STAMP(c, 30);
  {
    const u32 a = m * 2048 + lane * 16;
    sfor<0, 8>([&](auto PP) {
      constexpr int P = decltype(PP)::v;
      *(LAS f32x4*)(a + bloc(18 + P)) = q4<(P & 1) * 8>(acc[P >> 1]);
      *(LAS f32x4*)(a + bloc(18 + P) + 1024) = q4<(P & 1) * 8 + 4>(acc[P >> 1]);
    });
  }
  u32x4 xr;
  const u32 oidx = finish_idx(c, 1);
  xr = *(const GAS u32x4*)(x + oidx);
  WAITL0();
  STAMP(c, 32);
  BAR();
  finish<1>(c, f32x4{}, f32x4{}, xr, out + oidx);
}

template <bool FFN>
DI void compute(Ctx& c, const Tile t, const u16* x, u16* out) {
  constexpr int NBK = FFN ? 7 : 6;
  const u32 lane = c.lane, m = c.m;
  const int b = blockIdx.x, i = (b & 7) * 4 + (b >> 6), jc = (b >> 3) & 7, rt = t.rt, j = t.j;
  const int hd = (b & 7) * 4 + ((b >> 3) & 3), ga = b >> 5;
  {
    const u32 ii = 2 * rt + (m >> 1), l7 = lane & 7;
    const u32 t1 = FFN ? (ii & 3) * 64 + (ii >> 2) + l7 * 8 : l7 * 32 + m * 8 + j, f1 = FFN ? j * 2 + (m & 1) : 16 + rt;
    load_tag(c, lane < 8 ? t1 : lane < 16 ? m * 64 + l7 * 8 + ga : (rt >> 1) * 32 + (lane & 3) * 8 + j,
             lane < 8 ? f1 : lane < 16 ? 32 + hd : 96 + (rt & 1) * 4 + m);
  }
  if (threadIdx.x < 16) *(LAS u32*)(L_SEM + threadIdx.x * 4) = 0;
  const u32 hh = lane >> 5, blk = (lane >> 4) & 1, q = (lane & 15) >> 2, p = lane & 3, r = lane & 31;
  u32 ax[4], aw[2][2], ac;
#pragma unroll
  for (int s = 0; s < 4; s++) ax[s] = m * 4096 + r * 128 + (((2 * s + hh) ^ ((r >> 1) & 7)) << 4);
  {
    const u32 a = (8 * hh + q) * 128 + (((2 * blk + (p >> 1)) ^ ((q >> 1) << 2)) << 4) + 8 * (p & 1);
    aw[0][0] = a + LWA; aw[0][1] = (a ^ 64) + LWA; aw[1][0] = a + LWB; aw[1][1] = (a ^ 64) + LWB;
    ac = (8 * hh + q) * 64 + 32 * blk + 8 * p + LWB + 8192;
    OPAQUE(aw[0][0]); OPAQUE(aw[0][1]); OPAQUE(aw[1][0]); OPAQUE(aw[1][1]); OPAQUE(ac);
  }
  auto rdw = [&](auto TT, auto MM) -> u32x4 {
    constexpr int T = decltype(TT)::v, M = decltype(MM)::v, S = (T >> 2) % 3, s = T & 3;
    if constexpr (M < 6) {
      const u32 a = (M < 4 ? aw[0][M & 1] + S * 16384 + (M >> 1) * 8192 : aw[1][M & 1] + S * 12288) + s * 2048;
      return trd2(a, a + 512);
    } else {
      const u32 a = ac + S * 12288 + s * 1024;
      return trd2(a, a + 256);
    }
  };
  u32x4 xf[2], wf[2][NBK];
  f32x16 acc[NBK];
  u32x2 csv[NBK];
  u32 fmd;
  float rstd;
  WAITL0();
  STAMP(c, 1);
  WAITV(1);
  BAR();
  STAMP(c, 2);
  xf[0] = lds128(ax[0]);
  sfor<0, NBK>([&](auto MM) { wf[0][decltype(MM)::v] = rdw(IC<0>{}, MM); });
  SB();
  sfor<0, 32>([&](auto TT) {
    constexpr int T = decltype(TT)::v, G = T >> 2, S = T & 3, cur = T & 1;
    sfor<0, NBK>([&](auto MM) {
      constexpr int M = decltype(MM)::v;
      constexpr bool TR = !FFN && M >= 4;
      if constexpr (T == 0) mma0(acc[M], TR ? xf[cur] : wf[cur][M], TR ? wf[cur][M] : xf[cur]);
      else mma(acc[M], TR ? xf[cur] : wf[cur][M], TR ? wf[cur][M] : xf[cur]);
      if constexpr (S == 3 && M == 1) {
        WAITL0();
        STAMP(c, 3 + 2 * G);
        BAR();
        STAMP(c, 4 + 2 * G);
      }
      if constexpr (T < 31) {
        constexpr int F = S == 3 ? 1 : 0;
        if constexpr (M == F) {
          xf[cur ^ 1] = lds128(ax[(S + 1) & 3] + (((T + 1) >> 2) % 3) * 16384);
          if constexpr (F == 1) wf[cur ^ 1][0] = rdw(IC<T + 1>{}, IC<0>{});
        }
        if constexpr (M >= F) wf[cur ^ 1][M] = rdw(IC<T + 1>{}, MM);
      }
      if constexpr (T == 31 && M == 1) {
        fmd = *(LAS u32*)(L_SP0 + 2048 + m * 128 + r * 4);
#pragma unroll
        for (int k = 0; k < NBK; k++) csv[k] = *(LAS u32x2*)(L_SP0 + (k * 32 + r) * 8);
        rstd = *(LAS float*)(L_RS + m * 128 + r * 4);
      }
      SB();
    });
  });
  __builtin_amdgcn_s_setprio(2);
  WAITV(0);
  OPAQUE(c.tag);
  c.tag += 1;
  fmd = hh ? 0u : fmd;
  const u32x4 fm = {fmd, fmd, 0u, 0u};
#pragma unroll
  for (int k = 0; k < NBK; k++) {
    const u32x4 fc = {csv[k][0], csv[k][1], 0u, 0u};
    acc[k] = (!FFN && k >= 4) ? mfma(fm, fc, acc[k]) : mfma(fc, fm, acc[k]);
  }
  const u32 tb = rt * 4 + m;
  auto pack8 = [&](const f32x16& a, auto SS) -> u32x4 {
    constexpr int s = decltype(SS)::v;
    return u32x4{pk2(pr<8 * s>(a) * rstd), pk2(pr<8 * s + 2>(a) * rstd), pk2(pr<8 * s + 4>(a) * rstd), pk2(pr<8 * s + 6>(a) * rstd)};
  };
  if constexpr (FFN) {
    const u32 hbase = HF_OFF + tb * 112 * 1024;
    sfor<0, 14>([&](auto UU) {
      constexpr int k = decltype(UU)::v >> 1, s = decltype(UU)::v & 1;
      u32x4 o = pack8(acc[k], IC<s>{});
#pragma unroll
      for (int d = 0; d < 4; d++) o[d] = relu2(o[d]);
      st128(o, c.ws, lane * 16, hbase + (((k < 6 ? t.c0 + k * 64 : t.cc) >> 5) + s) * 1024);
    });
    sem_post(c, SEM_ST);
  } else {
    *(LAS u32x4*)(lwb(1) + m * 2048 + lane * 16) = pack8(acc[1], IC<0>{});
    *(LAS u32x4*)(lwb(1) + m * 2048 + 1024 + lane * 16) = pack8(acc[1], IC<1>{});
    WAITL0();
    BAR();
    const u32 awp = L_WPHI + (4 * hh + q) * 32 + p * 8, sg = blk ? 0x80008000u : 0u;
    const f32x16 P0 = mfma(wphi_frag(awp, sg), pack8(acc[0], IC<0>{}), f32x16{});
    const f32x16 P1 = mfma(wphi_frag(awp + 512, sg), pack8(acc[0], IC<1>{}), f32x16{});
    sfor<0, 4>([&](auto UU) {
      constexpr int u = decltype(UU)::v;
      st128(pack8(acc[2 + (u >> 1)], IC<(u & 1)>{}), c.ws, lane * 16, QF_OFF + (tb * 32 + 4 * j + u) * 1024);
    });
    const u32 av0 = LBS + 16384 + m * 2048 + lane * 16, av1 = ((m >> 1) ? L_SP2 : L_SP1) + (m & 1) * 2048 + lane * 16;
    sfor<0, 2>([&](auto TT) {
      constexpr int tt = decltype(TT)::v;
      const f32x4 ra = ldsf4(L_RS + m * 128 + (16 * tt + 4 * hh) * 4), rb = ldsf4(L_RS + m * 128 + (16 * tt + 8 + 4 * hh) * 4);
      const f32x4 a0 = q4<8 * tt>(acc[4]) * ra, b0 = q4<8 * tt + 4>(acc[4]) * rb, a1 = q4<8 * tt>(acc[5]) * ra, b1 = q4<8 * tt + 4>(acc[5]) * rb;
      *(LAS u32x4*)(av0 + tt * 1024) = u32x4{pk(a0[0], a0[1]), pk(a0[2], a0[3]), pk(b0[0], b0[1]), pk(b0[2], b0[3])};
      *(LAS u32x4*)(av1 + tt * 1024) = u32x4{pk(a1[0], a1[1]), pk(a1[2], a1[3]), pk(b1[0], b1[1]), pk(b1[2], b1[3])};
    });
    const u32 apw = lx(1) + m * 2048 + r * 64 + ((hh ^ ((r >> 1) & 7)) << 3);
    phi_store(apw, P0);
    phi_store(apw + 8192, P1);
    WAITL0();
    STAMP(c, 19);
    BAR();
    WAITV(0);
    publish(c, 16, 4);
    const u32 t0 = 4 * hh + q;
    const u32 apr = ((m >> 1) ? lwa(1) : lx(1)) + (m & 1) * 8192 + t0 * 64 + (((4 * blk + p) ^ (t0 >> 1)) << 3);
    const u32 avl = ((m >> 1) ? L_SP1 : LBS + 16384) + lane * 16, avh = ((m >> 1) ? L_SP2 : LBS + 16384 + 4096) + lane * 16;
    const bool isv = blk == (m & 1);
    u32x4 pa[8], vb[8];
#pragma unroll
    for (int k = 0; k < 8; k++) {
      pa[k] = trd2(apr + k * 1024, (apr ^ 32) + k * 1024 + 512);
      vb[k] = lds128((k < 4 ? avl : avh) + (k & 3) * 1024);
    }
    sem_post(c, SEM_KV);
    f32x16 X = f32x16{};
#pragma unroll
    for (int k = 0; k < 8; k++) {
#pragma unroll
      for (int d = 0; d < 4; d++) vb[k][d] = isv ? vb[k][d] : ONES2;
      X = mfma(pa[k], vb[k], X);
    }
    const u32 e = isv ? hh * 16 + (lane & 15) : 32 + hh;
    const u32 kb = KV_OFF + ((4 * j + m) * 16 + rt) * 2176;
    st128(__builtin_bit_cast(u32x4, q4<0>(X)), c.ws, e * 16, kb);
    st128(__builtin_bit_cast(u32x4, q4<4>(X)), c.ws, e * 16, kb + 544);
    st128(__builtin_bit_cast(u32x4, q4<8>(X)), c.ws, e * 16, kb + 1088);
    st128(__builtin_bit_cast(u32x4, q4<12>(X)), c.ws, e * 16, kb + 1632);
  }
  STAMP(c, 20);
  init_ab(c);
  WAITV(0);
  publish(c, 0);
  STAMP(c, 23);
  const u32 mb = MB_OFF + b * 1024;
  const u32 fk = mb + 128, fo = mb + 256 + 64 * m, fq = mb + 768 + 16 * m;
  await(c, fq, 2, peek(c, fq, 2));
  STAMP(c, 24);
  u32x4 p00, p01, p10, p11;
  u64 pkv;
  {
    u32x4 qf[2];
#pragma unroll
    for (int u = 0; u < 2; u++) qf[u] = ld128(c.ws, lane * 16, QF_OFF + ((8 * ga + 2 * m + u) * 32 + hd) * 1024);
    const u32x4 wp = wphi_frag(L_WPA + (hd & 1) * 512 + (4 * hh + q) * 32 + p * 8, blk ? 0x80008000u : 0u);
    const f32x16 P0 = mfma(wp, qf[0], f32x16{}), P1 = mfma(wp, qf[1], f32x16{});
    dual_softmax(P0, p00, p01);
    SB();
    pkv = peek(c, fk, 16);
    dual_softmax(P1, p10, p11);
    OPAQUE(p00); OPAQUE(p01); OPAQUE(p10); OPAQUE(p11);
  }
  STAMP(c, 21);
  await(c, fk, 16, pkv);
  STAMP(c, 25);
  const u32 ah2 = HF_OFF + ((2 * i) * 112 + 28 * m + c.seg[2]) * 1024, ah3 = HF_OFF + ((2 * i) * 112 + 28 * m + c.seg[3]) * 1024;
  const u32 ao = OF_OFF + ((2 * i) * 32 + 8 * m) * 1024;
  const u32 va[2] = {lane * 16 + (m >> 1) * 114688, lane * 16 + ((m >> 1) ^ 1) * 114688};
  const u32 vo[2] = {lane * 16 + (m >> 1) * 32768, lane * 16 + ((m >> 1) ^ 1) * 32768};
  u32x4 af[18][2], bf[18][2];
  auto lda = [&](auto NN) {
    constexpr int N = decltype(NN)::v;
#pragma unroll
    for (int u = 0; u < 2; u++)
      af[N - 18][u] = N < 28 ? ld128(c.ws, va[u], (N < 21 ? ah2 : ah3) + (N % 7) * 1024) : ld128(c.ws, vo[u], ao + (N - 28) * 1024);
  };
  {
    const u32 e = (blk == (u32)(hd & 1)) ? hh * 16 + (lane & 15) : 32 + hh;
    f32x4 part[16];
#pragma unroll
    for (int s = 0; s < 16; s++) part[s] = __builtin_bit_cast(f32x4, ld128(c.ws, e * 16, KV_OFF + (hd * 16 + s) * 2176 + m * 544));
    SB();
    float v[4] = {part[0][0], part[0][1], part[0][2], part[0][3]};
#pragma unroll
    for (int s = 1; s < 16; s++)
#pragma unroll
      for (int k = 0; k < 4; k++) {
        v[k] += part[s][k];
        OPAQUE(v[k]);
      }
    *(LAS u32x2*)(L_WPHI + e * 32 + m * 8) = u32x2{pk(v[0], v[1]), pk(v[2], v[3])};
    sem_post(c, SEM_A);
    sem_wait(SEM_A, 4);
    const u32x4 k0 = lds128(L_WPHI + e * 32), k1 = lds128(L_WPHI + e * 32 + 16);
    f32x16 Z0 = mfma(k0, p00, f32x16{}), Z1 = mfma(k0, p10, f32x16{});
    Z0 = mfma(k1, p01, Z0);
    Z1 = mfma(k1, p11, Z1);
    constexpr int NO = FFN ? 8 : 0;
    const float r0 = __builtin_amdgcn_rcpf(Z0[8 - NO] + 1e-6f), r1 = __builtin_amdgcn_rcpf(Z1[8 - NO] + 1e-6f);
    u32x4 o0, o1;
#pragma unroll
    for (int d = 0; d < 4; d++) {
      o0[d] = pk(mul1(Z0[NO + 2 * d], r0), mul1(Z0[NO + 2 * d + 1], r0));
      o1[d] = pk(mul1(Z1[NO + 2 * d], r1), mul1(Z1[NO + 2 * d + 1], r1));
    }
    st128(o0, c.ws, lane * 16, OF_OFF + ((8 * ga + 2 * m) * 32 + hd) * 1024);
    st128(o1, c.ws, lane * 16, OF_OFF + ((8 * ga + 2 * m + 1) * 32 + hd) * 1024);
  }
  STAMP(c, 26);
  sem_wait(SEM_B + m, 1);
  auto ldb = [&](auto NN) {
    constexpr int N = decltype(NN)::v;
    bf[N - 18][0] = rdb<N>(c, 0);
    bf[N - 18][1] = rdb<N>(c, 1);
  };
  sfor<18, 22>(lda);
  sfor<18, 26>(ldb);
  sem_post(c, SEM_FREE + m);
  WAITV(8);
  publish(c, 8);
  STAMP(c, 27);
  sfor<22, 28>(lda);
  const u32 oidx = finish_idx(c, 0);
  const u32x4 xr = *(const GAS u32x4*)(x + oidx);
  SB();
  STAMP(c, 28);
  f32x16 ac2[4];
  u64 po = 0;
  sfor<18, 36>([&](auto NN) {
    constexpr int N = decltype(NN)::v, n = N - 18;
    if constexpr (n == 0) {
      mma0(ac2[0], bf[n][0], af[n][0]); mma0(ac2[1], bf[n][1], af[n][0]); mma0(ac2[2], bf[n][0], af[n][1]); mma0(ac2[3], bf[n][1], af[n][1]);
    } else {
      mma(ac2[0], bf[n][0], af[n][0]); mma(ac2[1], bf[n][1], af[n][0]); mma(ac2[2], bf[n][0], af[n][1]); mma(ac2[3], bf[n][1], af[n][1]);
    }
    SB();
    if constexpr (N + 8 < 36) ldb(IC<N + 8>{});
    if constexpr (N == 22) po = peek(c, fo, 8);
    if constexpr (N == 27) {
      STAMP(c, 29);
      await(c, fo, 8, po);
      STAMP(c, 30);
      sfor<28, 36>(lda);
    }
    SB();
  });
  STAMP(c, 31);
  asm volatile("s_nop 15" : "+v"(ac2[0]), "+v"(ac2[1]), "+v"(ac2[2]), "+v"(ac2[3]));
  {
    const u32 a = m * 2048 + lane * 16;
    sfor<1, 8>([&](auto PP) {
      constexpr int P = decltype(PP)::v;
      *(LAS f32x4*)(a + bloc(25 + P)) = q4<(P & 1) * 8>(ac2[P >> 1]);
      *(LAS f32x4*)(a + bloc(25 + P) + 1024) = q4<(P & 1) * 8 + 4>(ac2[P >> 1]);
    });
  }
  WAITL0();
  STAMP(c, 32);
  BAR();
  STAMP(c, 33);
  if (threadIdx.x == 63) __hip_atomic_store((GAS u64*)(c.wsp + EP_OFF + b * 8), c.tag, __ATOMIC_RELAXED, __HIP_MEMORY_SCOPE_AGENT);
  finish<0>(c, q4<0>(ac2[0]), q4<4>(ac2[0]), xr, out + oidx);
}

#define ROTB 1
__global__ __launch_bounds__(512, 2) void fused_k(const u16* x, const u16* wqkv, const u16* wphi, const u16* wo, const u16* w1, const u16* w2, u16* out,
                                                  char* ws) {
  __shared__ __attribute__((aligned(16))) char smem[163840];
  asm volatile("" ::"v"((LAS char*)smem));
  const int b = blockIdx.x, xcd = b & 7, r = b >> 3;
  Ctx c;
  c.dbg = 0;
  STAMP(c, 0);
  c.lane = threadIdx.x & 63;
  const u32 w = __builtin_amdgcn_readfirstlane(threadIdx.x >> 6);
  c.m = w & 3;
  c.ws = __builtin_amdgcn_make_buffer_rsrc(ws, 0, WS_BYTES, 0x00020000);
  c.wsp = ws;
#pragma unroll
  for (int k = 0; k < 4; k++) c.seg[k] = ((k + (ROTB ? (b >> 6) : 0)) & 3) * 7;
  const int rt = (xcd >> 2) * 8 + (r >> 2), ct = (xcd & 3) * 4 + (r & 3), j = ct >> 1;
  {
    const bool ffn = ct & 1;
    const u32 lane = c.lane, ldw = ffn ? 3584 : 3072;
    const u32x4 srd_x = make_srd(x, 2048 * 512 * 2), srd_w = make_srd(ffn ? w1 : wqkv, 512 * ldw);
    const u32 vx0 = (lane >> 3) * 1024 + (((lane & 7) ^ (lane >> 4)) << 4);
    const u32 sx = (rt * 128 + c.m * 32 + (w >> 2) * 16) * 1024, dx = c.m * 4096 + (w >> 2) * 2048;
    dma<LX, 0>(srd_x, vx0, sx, dx);
    dma<LX + 1024, 8192>(srd_x, vx0 ^ 64, sx, dx);
    const u32 vw = (lane >> 3) * ldw + (((lane & 7) ^ (((lane >> 4) & 1) << 2)) << 4);
    const u32 c0 = ffn ? 448 * j + (j & 1) * 64 : 1024 + 128 * j, c1 = ffn ? c0 + 128 : 128 * j, c2 = ffn ? c0 + 256 : 2048 + 128 * j;
    dma<LWA, 0>(srd_w, vw, w * 8 * ldw + c0, w * 1024);
    dma<LWA + 8192, 0>(srd_w, vw, w * 8 * ldw + c1, w * 1024);
    dma<LWB, 0>(srd_w, vw, w * 8 * ldw + c2, w * 1024);
    if (ffn && w < 4) dma<LWB + 8192, 0>(srd_w, (lane >> 2) * 3584 + ((lane & 3) << 4), w * 16 * 3584 + 448 * j + ((j & 1) ? 0 : 384), w * 1024);
    SB();
  }
  if (ct & 1) {
    const Tile t = make_tile<true>(rt, j);
    if (w < 4) compute<true>(c, t, x, out);
    else helper<true>(c, t, x, w1, wphi, wo, w2, out);
  } else {
    const Tile t = make_tile<false>(rt, j);
    if (w < 4) compute<false>(c, t, x, out);
    else helper<false>(c, t, x, wqkv, wphi, wo, w2, out);
  }
}

extern "C" void kernel_launch(void* const* d_in, const int* in_sizes, int n_in, void* d_out, int out_size, void* d_ws, size_t ws_size,
                              hipStream_t stream) {
  const u16 *x = (const u16*)d_in[0], *wqkv = (const u16*)d_in[1], *wphi = (const u16*)d_in[2], *wo = (const u16*)d_in[3];
  const u16 *w1 = (const u16*)d_in[5], *w2 = (const u16*)d_in[6];
  fused_k<<<256, 512, 0, stream>>>(x, wqkv, wphi, wo, w1, w2, (u16*)d_out, (char*)d_ws);
}
```

```cpp
#include <hip/hip_runtime.h>
#pragma clang diagnostic ignored "-Wint-to-pointer-cast"
typedef unsigned short u16;
typedef unsigned u32;
typedef unsigned long long u64;
typedef u32 u32x2 __attribute__((ext_vector_type(2)));
typedef u32 u32x4 __attribute__((ext_vector_type(4)));
typedef short s16x2 __attribute__((ext_vector_type(2)));
typedef short s16x4 __attribute__((ext_vector_type(4)));
typedef float f32x2 __attribute__((ext_vector_type(2)));
typedef float f32x4 __attribute__((ext_vector_type(4)));
typedef float f32x16 __attribute__((ext_vector_type(16)));
typedef __bf16 b16x2 __attribute__((ext_vector_type(2)));
typedef __bf16 b16x8 __attribute__((ext_vector_type(8)));
#define LAS __attribute__((address_space(3)))
#define GAS __attribute__((address_space(1)))
#define DI __device__ __forceinline__
#define SB() __builtin_amdgcn_sched_barrier(0)
#define BAR() __builtin_amdgcn_s_barrier()
#define WAITV(n) do { asm volatile("s_waitcnt vmcnt(%0)" ::"i"(n) : "memory"); SB(); } while (0)
#define WAITL0() asm volatile("s_waitcnt lgkmcnt(0)" ::: "memory")
#define OPAQUE(x) asm volatile("" : "+v"(x))

constexpr u32 HF_OFF = 0;
constexpr u32 QF_OFF = 64u * 112u * 1024u;
constexpr u32 OF_OFF = QF_OFF + 64u * 32u * 1024u;
constexpr u32 KV_OFF = OF_OFF + 64u * 32u * 1024u;
constexpr u32 KZ_OFF = KV_OFF + 32u * 16u * 2048u;
constexpr u32 CS_OFF = KZ_OFF + 32u * 16u * 128u;
constexpr u32 RS_OFF = CS_OFF + 256u * 1024u;
constexpr u32 MB_OFF = RS_OFF + 2048u * 8u;
constexpr u32 EP_OFF = MB_OFF + 256u * 1024u;
constexpr u32 WS_BYTES = EP_OFF + 2048u;
constexpr u32 LX = 0, LWA = 49152, LWB = 98304, LBS = 135168, LSCR = 159744;
constexpr u32 lx(int s) { return LX + s * 16384; }
constexpr u32 lwa(int s) { return LWA + s * 16384; }
constexpr u32 lwb(int s) { return LWB + s * 12288; }
constexpr u32 L_WPHI = LSCR, L_WPA = LSCR + 2048, L_RS = LSCR + 3072, L_SEM = LSCR + 3584, L_ZERO = LSCR + 3648;
constexpr u32 L_SP1 = lwb(1) + 8192, L_SP2 = lwb(2) + 8192;
constexpr u32 L_ST = LBS + 16384;
constexpr int AUX = 16;

DI f32x16 mfma(u32x4 a, u32x4 b, f32x16 c) {
  return __builtin_amdgcn_mfma_f32_32x32x16_bf16(__builtin_bit_cast(b16x8, a), __builtin_bit_cast(b16x8, b), c, 0, 0, 0);
}
DI void mma(f32x16& d, u32x4 a, u32x4 b) { asm volatile("v_mfma_f32_32x32x16_bf16 %0, %1, %2, %0" : "+v"(d) : "v"(a), "v"(b)); }
DI void mma0(f32x16& d, u32x4 a, u32x4 b) { asm volatile("v_mfma_f32_32x32x16_bf16 %0, %1, %2, 0" : "=&v"(d) : "v"(a), "v"(b)); }
DI u32x2 trd(u32 addr) { return __builtin_bit_cast(u32x2, __builtin_amdgcn_ds_read_tr16_b64_v4i16((LAS s16x4*)addr)); }
DI u32x4 trd2(u32 a0, u32 a1) {
  u32x2 lo = trd(a0), hi = trd(a1);
  return __builtin_shufflevector(lo, hi, 0, 1, 2, 3);
}
DI u32x4 lds128(u32 addr) { return *(LAS u32x4*)addr; }
DI f32x4 ldsf4(u32 addr) { return *(LAS f32x4*)addr; }
DI u32 pk(float a, float b) {
  f32x2 v = {a, b};
  return __builtin_bit_cast(u32, __builtin_convertvector(v, b16x2));
}
DI u32 pk2(f32x2 v) { return __builtin_bit_cast(u32, __builtin_convertvector(v, b16x2)); }
DI u32 relu2(u32 v) { return __builtin_bit_cast(u32, __builtin_elementwise_max(__builtin_bit_cast(s16x2, v), s16x2{0, 0})); }
extern "C" __device__ float fmul_legacy_(float, float) __asm("llvm.amdgcn.fmul.legacy");
DI float mul1(float a, float b) { return fmul_legacy_(a, b); }
DI float mx(float a, float b) { return __builtin_elementwise_maximum(a, b); }
DI float dot2(u32 a, u32 b, float c) {
  return __builtin_amdgcn_fdot2_f32_bf16(__builtin_bit_cast(b16x2, a), __builtin_bit_cast(b16x2, b), c, false);
}
DI void swap32(float& a, float& b) {
  auto r = __builtin_amdgcn_permlane32_swap(__float_as_uint(a), __float_as_uint(b), false, false);
  a = __uint_as_float(r[0]);
  b = __uint_as_float(r[1]);
}
template <int I> DI f32x2 pr(const f32x16& a) { return __builtin_shufflevector(a, a, I, I + 1); }
template <int I> DI f32x4 q4(const f32x16& a) { return __builtin_shufflevector(a, a, I, I + 1, I + 2, I + 3); }
DI u32x4 make_srd(const void* p, u32 bytes) {
  u64 a = (u64)p;
  u32x4 r = {(u32)a, (u32)(a >> 32) & 0xffffu, bytes, 0x00020000u};
  return r;
}
template <u32 LI, u32 SI>
DI void dma(u32x4 srd, u32 voff, u32 sbase, u32 lbase) {
  u32 t;
  asm volatile("s_add_u32 m0, %4, %6\n\ts_add_u32 %0, %3, %5\n\tbuffer_load_dwordx4 %1, %2, %0 offen lds"
               : "=&s"(t)
               : "v"(voff), "s"(srd), "s"(sbase), "s"(lbase), "i"(SI), "i"(LI)
               : "memory", "scc");
}
DI void st128(u32x4 v, __amdgpu_buffer_rsrc_t rs, u32 voff, u32 soff) {
  __builtin_amdgcn_raw_buffer_store_b128(v, rs, voff, soff, AUX);
  asm volatile("s_nop 1" ::"v"(v) : "memory");
}
#define LDAUX AUX
DI u32x4 ld128(__amdgpu_buffer_rsrc_t rs, u32 voff, u32 soff) { return __builtin_amdgcn_raw_buffer_load_b128(rs, voff, soff, LDAUX); }
template <int OFF> DI void gld64(u64& d, u32 voff, const char* base) {
  asm volatile("global_load_dwordx2 %0, %1, %2 offset:%3 sc1" : "=v"(d) : "v"(voff), "s"(base), "i"(OFF) : "memory");
}
template <int OFF> DI void gld32(float& d, u32 voff, const char* base) {
  asm volatile("global_load_dword %0, %1, %2 offset:%3 sc1" : "=v"(d) : "v"(voff), "s"(base), "i"(OFF) : "memory");
}
template <int OFF, class T> DI void gld128(T& d, u32 voff, const char* base) {
  asm volatile("global_load_dwordx4 %0, %1, %2 offset:%3 sc1" : "=v"(d) : "v"(voff), "s"(base), "i"(OFF) : "memory");
}
template <int L0, int N> DI void warm(u32& d, const void* base) {
  if constexpr (N > 0) {
    asm volatile("s_load_dword %0, %1, %2" : "+s"(d) : "s"(base), "i"(L0 * 128));
    warm<L0 + 1, N - 1>(d, base);
  }
}
template <int N> struct IC { static constexpr int v = N; };
template <int B, int E, class F> DI void sfor(F&& f) {
  if constexpr (B < E) {
    f(IC<B>{});
    sfor<B + 1, E>(f);
  }
}
constexpr u32 ONES2 = 0x3f803f80u;
constexpr float LOG2E = 1.4426950408889634f;

DI void dual_softmax(const f32x16& P, float sl, u32x4& o0, u32x4& o1) {
  float m0 = mx(mx(mx(mx(mx(mx(mx(P[0], P[1]), P[2]), P[3]), P[4]), P[5]), P[6]), P[7]);
  float m1 = mx(mx(mx(mx(mx(mx(mx(P[8], P[9]), P[10]), P[11]), P[12]), P[13]), P[14]), P[15]);
  swap32(m0, m1);
  m0 = mx(m0, m1) * -sl;
  m1 = m0;
  swap32(m0, m1);
  f32x2 e[8];
  sfor<0, 8>([&](auto II) {
    constexpr int I = decltype(II)::v;
    const float m = I < 4 ? m0 : m1;
    e[I] = __builtin_elementwise_fma(pr<2 * I>(P), f32x2{sl, sl}, f32x2{m, m});
  });
#pragma unroll
  for (int i = 0; i < 8; i++) e[i] = f32x2{__builtin_amdgcn_exp2f(e[i][0]), __builtin_amdgcn_exp2f(e[i][1])};
  const f32x2 s0 = (e[0] + e[1]) + (e[2] + e[3]), s1 = (e[4] + e[5]) + (e[6] + e[7]);
  m0 = s0[0] + s0[1];
  OPAQUE(m0);
  m1 = s1[0] + s1[1];
  swap32(m0, m1);
  m0 = __builtin_amdgcn_rcpf(m0 + m1);
  m1 = m0;
  swap32(m0, m1);
#pragma unroll
  for (int i = 0; i < 4; i++) {
    o0[i] = pk2(e[i] * m0);
    o1[i] = pk2(e[4 + i] * m1);
  }
}
struct Smx {
  f32x2 e[8];
  float m0, m1;
  u32x4 o0, o1;
  template <int I> DI void step(const f32x16& P, float sl) {
    if constexpr (I == 0) {
      m0 = mx(mx(mx(mx(mx(mx(mx(P[0], P[1]), P[2]), P[3]), P[4]), P[5]), P[6]), P[7]);
      m1 = mx(mx(mx(mx(mx(mx(mx(P[8], P[9]), P[10]), P[11]), P[12]), P[13]), P[14]), P[15]);
      swap32(m0, m1);
      m0 = mx(m0, m1) * -sl;
      m1 = m0;
      swap32(m0, m1);
      OPAQUE(m0); OPAQUE(m1);
    } else if constexpr (I == 1 || I == 2) {
      sfor<4 * (I - 1), 4 * I>([&](auto KK) {
        constexpr int K = decltype(KK)::v;
        const float m = K < 4 ? m0 : m1;
        e[K] = __builtin_elementwise_fma(pr<2 * K>(P), f32x2{sl, sl}, f32x2{m, m});
      });
#pragma unroll
      for (int i = 4 * (I - 1); i < 4 * I; i++) {
        e[i] = f32x2{__builtin_amdgcn_exp2f(e[i][0]), __builtin_amdgcn_exp2f(e[i][1])};
        OPAQUE(e[i]);
      }
    } else if constexpr (I == 3) {
      const f32x2 s0 = (e[0] + e[1]) + (e[2] + e[3]), s1 = (e[4] + e[5]) + (e[6] + e[7]);
      m0 = s0[0] + s0[1];
      OPAQUE(m0);
      m1 = s1[0] + s1[1];
      swap32(m0, m1);
      m0 = __builtin_amdgcn_rcpf(m0 + m1);
      m1 = m0;
      swap32(m0, m1);
      OPAQUE(m0); OPAQUE(m1);
    } else {
#pragma unroll
      for (int i = 0; i < 4; i++) {
        o0[i] = pk2(e[i] * m0);
        o1[i] = pk2(e[4 + i] * m1);
      }
      OPAQUE(o0); OPAQUE(o1);
    }
  }
};
DI u32x4 wphi_frag(u32 a, u32 sg) {
  u32x4 f = trd2(a, a + 256);
  f[0] ^= sg; f[1] ^= sg; f[2] ^= sg; f[3] ^= sg;
  return f;
}
DI void phi_store(u32 apw, const f32x16& P, float sl) {
  u32x4 o0, o1;
  dual_softmax(P, sl, o0, o1);
  *(LAS u32x2*)(apw) = u32x2{o0[0], o0[1]};
  *(LAS u32x2*)(apw ^ 16) = u32x2{o0[2], o0[3]};
  *(LAS u32x2*)(apw ^ 32) = u32x2{o1[0], o1[1]};
  *(LAS u32x2*)(apw ^ 48) = u32x2{o1[2], o1[3]};
}
template <int S> DI u32x4 pack8(const f32x16& a) { return u32x4{pk2(pr<8 * S>(a)), pk2(pr<8 * S + 2>(a)), pk2(pr<8 * S + 4>(a)), pk2(pr<8 * S + 6>(a))}; }

constexpr u32 slot_loc(int s, int k) { return k < 2 ? lx(s) + k * 8192 : k < 4 ? lwa(s) + (k - 2) * 8192 : lwb(s); }
constexpr u32 bloc(int n) {
  return n < 5 ? slot_loc(2, n) : n < 10 ? slot_loc(0, n - 5) : n < 15 ? slot_loc(1, n - 10) : n == 15 ? LBS + 16384
       : n < 28 ? bloc(n - 16) : LBS + (n & 1) * 8192;
}

#define STAMP(c, i) do {} while (0)
struct Ctx {
  mutable u32 dbg;
  __amdgpu_buffer_rsrc_t ws;
  char* wsp;
  u32 m, lane;
  u32 ab[3][2];
  u32 rot;
  u64 tag;
  u64 own;
  u32 fl;
};
template <int N> DI u32x4 rdb(const Ctx& c, int cb) {
  constexpr u32 W = bloc(N) / 57344;
  const u32 a = c.ab[W][cb] + (bloc(N) - W * 57344);
  return trd2(a, a + 1024);
}
DI void init_ab(Ctx& c) {
  const u32 lane = c.lane, m = c.m, hh = lane >> 5, blk = (lane >> 4) & 1, q = (lane & 15) >> 2, p = lane & 3;
#pragma unroll
  for (int cb = 0; cb < 2; cb++) {
    const u32 t = m * 2048 + (4 * hh + q) * 128 + (((4 * (cb ^ (m & 1)) + 2 * blk + (p >> 1)) ^ ((q >> 1) << 2)) << 4) + 8 * (p & 1);
#pragma unroll
    for (int k = 0; k < 3; k++) {
      c.ab[k][cb] = t + k * 57344;
      OPAQUE(c.ab[k][cb]);
    }
  }
}
DI void load_tag(Ctx& c, u32 tgt, u32 slot) {
  c.fl = MB_OFF + tgt * 1024 + slot * 8;
  asm volatile("global_load_dwordx2 %0, %1, %2 sc1" : "=v"(c.tag) : "v"(EP_OFF + tgt * 8), "s"(c.wsp) : "memory");
}
DI void set_tag(Ctx& c) {
  OPAQUE(c.tag);
  c.tag += 1;
  c.own = ((u64)(u32)__builtin_amdgcn_readlane((u32)(c.tag >> 32), 63) << 32) | (u32)__builtin_amdgcn_readlane((u32)c.tag, 63);
}
DI void publish(const Ctx& c, u64 mask) {
  asm volatile("s_mov_b64 exec, %3\n\tglobal_atomic_swap_x2 %0, %1, %2 sc1\n\ts_mov_b64 exec, -1" ::"v"(c.fl), "v"(c.tag), "s"(c.wsp), "s"(mask) : "memory");
}
DI u64 peek(const Ctx& c, u32 off, int n) {
  u64 v = c.own;
  const u64 z = 0;
  asm volatile("s_mov_b64 exec, %4\n\tglobal_atomic_or_x2 %0, %1, %2, %3 sc0 sc1\n\ts_mov_b64 exec, -1"
               : "+v"(v)
               : "v"(off + c.lane * 8), "v"(z), "s"(c.wsp), "s"((1ull << n) - 1)
               : "memory");
  return v;
}
DI void await(const Ctx& c, u32 off, int n, u64 v) {
  OPAQUE(v);
#pragma unroll 1
  for (int it = 0; it < (1 << 16) && !__all(v == c.own); it++) {
    __builtin_amdgcn_s_sleep(2);
    v = peek(c, off, n);
    asm volatile("s_waitcnt vmcnt(0)" : "+v"(v)::"memory");
  }
}
DI void sem_post(u32 idx) {
  asm volatile("s_mov_b64 exec, 1\n\tds_add_u32 %0, %1\n\ts_mov_b64 exec, -1" ::"v"(L_SEM + idx * 4), "v"(1u) : "memory");
}
DI void sem_wait(u32 idx, u32 n) {
#pragma unroll 1
  for (int it = 0; it < (1 << 20); it++) {
    if (__builtin_amdgcn_readfirstlane(*(volatile LAS u32*)(L_SEM + idx * 4)) >= n) break;
    __builtin_amdgcn_s_sleep(1);
  }
  asm volatile("" ::: "memory");
}
constexpr u32 SEM_B = 0, SEM_F = 4, SEM_K = 8, SEM_A = 12, SEM_KV = 13, SEM_C = 14, SEM_H = 15;
template <int N> DI u32 hstep(const Ctx& c) {
  const u32 h = c.rot + N;
  return h >= 28 ? h - 28 : h;
}
struct BSrc {
  u32x4 srd;
  u32 vb, sb, d2;
  DI void init(const Ctx& c, const u16* w, u32 rows, u32 rows_per_q) {
    srd = make_srd(w, rows * 1024);
    vb = (c.lane >> 3) * 1024 + (((c.lane & 7) ^ (((c.lane >> 4) & 1) << 2)) << 4);
    sb = c.m * rows_per_q * 1024 + ((blockIdx.x >> 3) & 7) * 128;
    d2 = c.m * 2048;
  }
  template <int N> DI void issue(const Ctx& c) const {
    const u32 so = sb + (N < 28 ? hstep<N>(c) : N - 28) * 16384;
    dma<bloc(N), 0>(srd, vb, so, d2);
    dma<bloc(N) + 1024, 8192>(srd, vb, so, d2);
  }
};

struct Tile {
  int rt, j, ct;
  bool ffn;
  u32 ldw;
  u32 c0, c1, c2, cc;
  u32x4 srd_x, srd_w;
  u32 vx0, vw;
};
DI u32 blk_of(u32 rt, u32 ct) { return (((rt & 7) * 4 + (ct & 3)) << 3) + (rt >> 3) * 4 + (ct >> 2); }

template <int N> DI void put_block(const Ctx& c, const f32x16& a) {
  const u32 p = c.m * 2048 + c.lane * 16;
  *(LAS f32x4*)(p + bloc(N)) = q4<0>(a);
  *(LAS f32x4*)(p + bloc(N) + 1024) = q4<4>(a);
  *(LAS f32x4*)(p + bloc(N + 1)) = q4<8>(a);
  *(LAS f32x4*)(p + bloc(N + 1) + 1024) = q4<12>(a);
}
template <int N> DI void add_block(const Ctx& c, u32 src, f32x4 (&v)[4]) {
  const u32 p = src * 2048 + c.lane * 16;
  v[0] += ldsf4(p + bloc(N));
  v[1] += ldsf4(p + bloc(N) + 1024);
  v[2] += ldsf4(p + bloc(N + 1));
  v[3] += ldsf4(p + bloc(N + 1) + 1024);
}

#define PFCODE 34816
#define TOUCH 0
#define WARM 0
#define CW 6
#define NB7 4
constexpr int NCP = 9;
constexpr u32 CODE_BYTES = PFCODE;
DI void helper(Ctx& c, const Tile& t, const u16* x, const u16* wo, const u16* w2, u16* out) {
  constexpr int ND = 10;
  const u32 lane = c.lane, m = c.m;
  const int b = blockIdx.x, i = (b & 7) * 4 + (b >> 6);
  const int hd = (b & 7) * 4 + ((b >> 3) & 3), ga = b >> 5;
  const u32 vx1 = t.vx0 ^ 64, vw1 = t.vw + 8 * t.ldw;
  const u32 vc = (lane >> 2) * 3584 + ((lane & 3) << 4);
  const u32 sx = (t.rt * 128 + m * 32) * 1024, sc = m * 16 * 3584 + t.cc;
  const u32 d4 = m * 4096, d2 = m * 2048, d1 = m * 1024;
  auto ringf = [&](auto FF) {
    constexpr int G = decltype(FF)::v / ND, S = G % 3, I = decltype(FF)::v % ND;
    const u32 sg = (m * 16 + G * 64) * t.ldw;
    if constexpr (I == 4) {
      if (t.ffn) dma<lwb(S) + 8192, G * 64 * 3584>(t.srd_w, vc, sc, d1);
    }
    if constexpr (I < 4) dma<lx(S) + I * 1024, I * 8192 + G * 128>(t.srd_x, (I & 1) ? vx1 : t.vx0, sx, d4);
    else if constexpr (I < 6) dma<lwa(S) + (I & 1) * 1024, 0>(t.srd_w, (I & 1) ? vw1 : t.vw, sg + t.c0, d2);
    else if constexpr (I < 8) dma<lwa(S) + 8192 + (I & 1) * 1024, 0>(t.srd_w, (I & 1) ? vw1 : t.vw, sg + t.c1, d2);
    else dma<lwb(S) + (I & 1) * 1024, 0>(t.srd_w, (I & 1) ? vw1 : t.vw, sg + t.c2, d2);
  };
  constexpr auto rend = [](int G) { return G < 0 ? ND + 3 : G == 0 ? 2 * ND + 5 : G == 1 ? 3 * ND + 6 : G == 2 ? 4 * ND + 8 : G < 6 ? (G + 3) * ND : 8 * ND; };
  sfor<ND, rend(-1)>(ringf);
  load_tag(c, lane < 16 ? blk_of(t.rt, lane) : lane < 32 ? blk_of(lane & 15, t.ct) : lane < 40 ? m * 64 + (lane & 7) * 8 + ga : b,
           lane < 16 ? 64 + t.ct : lane < 32 ? 80 + t.rt : 32 + hd);
  BSrc b2, bo;
  b2.init(c, w2, 1792, 448);
  bo.init(c, wo, 512, 128);
  const u32 hh = lane >> 5, blk = (lane >> 4) & 1, q = (lane & 15) >> 2, p = lane & 3, r = lane & 31;
  init_ab(c);
  u32x4 bf[8][2];
  auto ldb = [&](auto NN) {
    constexpr int N = decltype(NN)::v;
    bf[N - 28][0] = rdb<N>(c, 0);
    bf[N - 28][1] = rdb<N>(c, 1);
  };
  u64 ps = 0, grs = 0;
  f32x4 gc[4];
  __builtin_amdgcn_s_setprio(1);
  STAMP(c, 1);
  WAITV(4);
  BAR();
  STAMP(c, 2);
  float sv[4], scs;
  u32 wd = 0;
  const char *pw2 = (const char*)w2 + ((b >> 3) * 448 + m * 112) * 128, *pwo = (const char*)wo + ((b >> 3) * 128 + m * 32) * 128;
  sfor<0, 8>([&](auto GG) {
    constexpr int G = decltype(GG)::v;
    if constexpr (G == 1) {
      const u32 cv = *(LAS u32*)(L_ST + (m * 64 + lane) * 4), rv = *(LAS u32*)(L_ST + 1024 + (lane & 15) * 4);
      __builtin_amdgcn_raw_buffer_store_b32(cv, c.ws, lane * 4, CS_OFF + (t.ct * 16 + t.rt) * 1024 + m * 256, AUX);
      asm volatile("s_mov_b64 exec, %3\n\tglobal_store_dword %0, %1, %2 sc1\n\ts_mov_b64 exec, -1" ::"v"(RS_OFF + (t.rt * 128 + t.ct * 8) * 8 + (lane & 15) * 4),
                   "v"(rv), "s"(c.wsp), "s"(m == 0 ? 0xffffull : 0ull)
                   : "memory");
      set_tag(c);
    }
    if constexpr (G == 2) publish(c, 0xffull << (8 * m));
    if constexpr (G == 4) ps = peek(c, MB_OFF + b * 1024 + 512, 32);
    if constexpr (G == 5) {
      await(c, MB_OFF + b * 1024 + 512, 32, ps);
      gld64<0>(grs, RS_OFF + (t.rt * 128 + m * 32 + r) * 8, c.wsp);
      const u32 bc = CS_OFF + t.ct * 16384 + (lane >> 4) * 4096 + m * 256 + (lane & 15) * 16;
      gld128<0>(gc[0], bc, c.wsp); gld128<1024>(gc[1], bc, c.wsp); gld128<2048>(gc[2], bc, c.wsp); gld128<3072>(gc[3], bc, c.wsp);
    }
    if constexpr (WARM == 1 && G < 6) warm<G * 15, 15>(wd, pw2);
    if constexpr (WARM == 2 && G < 6) warm<G * 19, 12>(wd, pw2);
    sfor<rend(G - 1), rend(G)>(ringf);
    if constexpr (WARM == 2 && G < 6) { warm<G * 19 + 12, (G < 5 ? 7 : 5)>(wd, pw2); warm<G * 5, 5>(wd, pwo); }
    if constexpr (G == 6)
      sfor<0, 5>([&](auto NN) {
        constexpr int N = decltype(NN)::v;
        b2.issue<N>(c);
        SB();
        if constexpr (N == 0) {
#pragma unroll
          for (int k = 0; k < 4; k++) OPAQUE(gc[k]);
#pragma unroll
          for (int k = 0; k < 4; k++) {
            sv[k] = gc[0][k];
#pragma unroll
            for (int u = 1; u < 4; u++) {
              sv[k] += gc[u][k];
              OPAQUE(sv[k]);
            }
          }
        } else if constexpr (N == 1) {
          swap32(sv[0], sv[2]);
          swap32(sv[1], sv[3]);
          sv[0] += sv[2];
          OPAQUE(sv[0]);
          sv[1] += sv[3];
          auto sw16 = __builtin_amdgcn_permlane16_swap(__float_as_uint(sv[0]), __float_as_uint(sv[1]), false, false);
          scs = __uint_as_float(sw16[0]) + __uint_as_float(sw16[1]);
          OPAQUE(scs);
        } else if constexpr (N == 2) {
          const u32 h2 = pk(scs, scs);
          const float lo = scs - __uint_as_float(h2 & 0xffff0000u);
          *(LAS u32x4*)(L_ST + (m * 64 + (lane & 15) * 4 + (lane >> 4)) * 16) = u32x4{h2, pk(lo, lo), 0u, 0u};
        } else if constexpr (N == 3) {
          OPAQUE(grs);
          *(LAS u32x4*)(L_ST + 4096 + (m * 32 + r) * 16) = u32x4{(u32)grs, (u32)grs, 0u, 0u};
          *(LAS u32*)(L_RS + m * 128 + r * 4) = (u32)(grs >> 32);
        }
      });
    if constexpr (G == 6) {
      WAITL0();
      asm volatile("" : "+s"(wd));
    }
    if constexpr (G == 7) {
      bo.issue<28>(c);
      bo.issue<29>(c);
    }
    STAMP(c, 3 + 2 * G);
    if constexpr (G < 7) WAITV(G < 6 ? rend(G) - (G + 2) * ND : 10);
    if (G == 7 && !t.ffn) {
      const u32 awp = L_WPHI + 1024 + (4 * hh + q) * 32 + p * 8, sg = blk ? 0x80008000u : 0u;
      const u32x4 w0 = wphi_frag(awp, sg), w1 = wphi_frag(awp + 512, sg);
      const float sl = __uint_as_float((u32)(grs >> 32)) * LOG2E;
      sem_wait(SEM_K + m, 1);
      const u32 ak = ((m >> 1) ? L_SP2 : L_SP1) + (m & 1) * 2048 + lane * 16;
      const f32x16 P0 = mfma(w0, lds128(ak), f32x16{}), P1 = mfma(w1, lds128(ak + 1024), f32x16{});
      BAR();
      const u32 apw = lwa(1) + m * 2048 + r * 64 + ((hh ^ ((r >> 1) & 7)) << 3);
      phi_store(apw, P0, sl);
      phi_store(apw + 8192, P1, sl);
      WAITL0();
    }
    BAR();
    STAMP(c, 4 + 2 * G);
  });
  __builtin_amdgcn_s_setprio(2);
  const u32 mb = MB_OFF + b * 1024;
  const u32 fk = mb + 128, fo = mb + 256 + 64 * m, fq = mb + 768 + 16 * m;
  u64 pq, pkv;
  u32x4 qf[2];
  float rs0, rs1;
  f32x16 Q0, Q1;
  Smx s0, s1;
  auto B = [&](auto NN) {
    constexpr int N = decltype(NN)::v;
    if constexpr (N < 28) b2.issue<N>(c);
    else bo.issue<N>(c);
    SB();
  };
  auto W2 = [&](auto NN) {
    constexpr int N = decltype(NN)::v;
    ldb(IC<N>{});
    ldb(IC<N + 1>{});
    WAITL0();
    if constexpr (N + 2 < 36) sfor<N + 2, N + 4>(B);
  };
  pq = peek(c, fq, 2);
  sfor<5, 8>(B);
  STAMP(c, 36);
  WAITV(7);
  STAMP(c, 37);
  sem_post(SEM_B + m);
  W2(IC<28>{});
  STAMP(c, 38);
  WAITV(10);
  STAMP(c, 20);
  await(c, fq, 2, pq);
  STAMP(c, 24);
  {
    const u32 qo = QF_OFF + ((8 * ga + 2 * m) * 32 + hd) * 1024 + lane * 16;
    gld128<0>(qf[0], qo, c.wsp);
    gld128<0>(qf[1], qo + 32768, c.wsp);
    gld32<4>(rs0, RS_OFF + (8 * ga + 2 * m) * 256 + lane * 8, c.wsp);
  }
  sfor<8, 10>(B);
  if (!t.ffn) sem_wait(SEM_KV, 4);
  sfor<10, 14>(B);
  STAMP(c, 39);
  WAITV(12);
  STAMP(c, 40);
  {
    OPAQUE(qf[0]); OPAQUE(qf[1]); OPAQUE(rs0);
    const u32x4 wp = wphi_frag(L_WPA + (hd & 1) * 512 + (4 * hh + q) * 32 + p * 8, blk ? 0x80008000u : 0u);
    Q0 = mfma(wp, qf[0], f32x16{});
    Q1 = mfma(wp, qf[1], f32x16{});
    rs1 = rs0;
    swap32(rs0, rs1);
  }
  W2(IC<30>{});
  STAMP(c, 41);
  const float sl0 = rs0 * LOG2E, sl1 = rs1 * LOG2E;
  s0.step<0>(Q0, sl0);
  B(IC<14>{});
  s0.step<1>(Q0, sl0);
  B(IC<15>{});
  s0.step<2>(Q0, sl0);
  STAMP(c, 42);
  WAITV(12);
  STAMP(c, 43);
  sem_post(SEM_B + m);
  sem_wait(SEM_F + m, 1);
  STAMP(c, 44);
  B(IC<16>{});
  s0.step<3>(Q0, sl0);
  B(IC<17>{});
  s0.step<4>(Q0, sl0);
  B(IC<18>{});
  s1.step<0>(Q1, sl1);
  B(IC<19>{});
  s1.step<1>(Q1, sl1);
  B(IC<20>{});
  s1.step<2>(Q1, sl1);
  STAMP(c, 45);
  WAITV(14);
  STAMP(c, 46);
  W2(IC<32>{});
  s1.step<3>(Q1, sl1);
  STAMP(c, 47);
  sem_wait(SEM_F + m, 2);
  STAMP(c, 48);
  B(IC<21>{});
  s1.step<4>(Q1, sl1);
  sfor<22, 24>(B);
  pkv = peek(c, fk, 16);
  sfor<24, 28>(B);
  STAMP(c, 21);
  WAITV(8);
  W2(IC<34>{});
  OPAQUE(pkv);
  if (!__all(pkv == c.own)) {
    WAITV(0);
    sem_post(SEM_B + m);
    await(c, fk, 16, pkv);
  }
  STAMP(c, 25);
  {
    f32x4 part[8];
#pragma unroll
    for (int s = 0; s < 8; s++) part[s] = __builtin_bit_cast(f32x4, ld128(c.ws, hh * 2048 + r * 16, KV_OFF + (hd * 16 + 2 * s) * 2048 + m * 512));
    f32x4 z = __builtin_bit_cast(f32x4, ld128(c.ws, (lane & 15) * 128 + blk * 16, KZ_OFF + hd * 2048 + m * 32));
    STAMP(c, 49);
    WAITV(9);
    STAMP(c, 50);
    sem_post(SEM_B + m);
    float v[4] = {part[0][0], part[0][1], part[0][2], part[0][3]};
#pragma unroll
    for (int s = 1; s < 8; s++)
#pragma unroll
      for (int k = 0; k < 4; k++) {
        v[k] += part[s][k];
        OPAQUE(v[k]);
      }
    swap32(v[0], v[2]);
    swap32(v[1], v[3]);
    v[0] += v[2];
    OPAQUE(v[0]);
    *(LAS u32*)(L_WPHI + r * 32 + m * 8 + hh * 4) = pk(v[0], v[1] + v[3]);
    float za = hh ? z[2] : z[0], zb = hh ? z[3] : z[1];
    sfor<0, 4>([&](auto KK) {
      za += __int_as_float(__builtin_amdgcn_update_dpp(0, __float_as_int(za), 0x120 + (8 >> decltype(KK)::v), 0xf, 0xf, false));
      zb += __int_as_float(__builtin_amdgcn_update_dpp(0, __float_as_int(zb), 0x120 + (8 >> decltype(KK)::v), 0xf, 0xf, false));
    });
    *(LAS u32*)(L_WPHI + (32 + blk) * 32 + m * 8 + hh * 4) = pk(za, zb);
    STAMP(c, 51);
    sem_post(SEM_A);
    sem_wait(SEM_A, 4);
    STAMP(c, 52);
    const u32 e = blk == 0 ? hh * 16 + (lane & 15) : 32 + hh;
    const u32x4 k0 = lds128(L_WPHI + e * 32), k1 = lds128(L_WPHI + e * 32 + 16);
    f32x16 Z0 = mfma(k0, s0.o0, f32x16{}), Z1 = mfma(k0, s1.o0, f32x16{});
    Z0 = mfma(k1, s0.o1, Z0);
    Z1 = mfma(k1, s1.o1, Z1);
    constexpr int NO = 0;
    const float r0 = __builtin_amdgcn_rcpf(mul1(Z0[8 - NO] + 1e-6f, rs0)), r1 = __builtin_amdgcn_rcpf(mul1(Z1[8 - NO] + 1e-6f, rs1));
    u32x4 o0, o1;
#pragma unroll
    for (int d = 0; d < 4; d++) {
      o0[d] = pk(mul1(Z0[NO + 2 * d], r0), mul1(Z0[NO + 2 * d + 1], r0));
      o1[d] = pk(mul1(Z1[NO + 2 * d], r1), mul1(Z1[NO + 2 * d + 1], r1));
    }
    st128(o0, c.ws, lane * 16, OF_OFF + ((8 * ga + 2 * m) * 32 + hd) * 1024);
    st128(o1, c.ws, lane * 16, OF_OFF + ((8 * ga + 2 * m + 1) * 32 + hd) * 1024);
  }
  STAMP(c, 26);
  WAITV(0);
  publish(c, 0xff00000000ull);
  STAMP(c, 27);
  const u32 orow = 64 * i + 32 * (m >> 1) + r, oidx = orow * 512 + 64 * ((b >> 3) & 7) + 32 * (m & 1) + 8 * hh;
  u32x4 xr[2] = {*(const GAS u32x4*)(x + oidx), *(const GAS u32x4*)(x + oidx + 16)};
  float frs = __uint_as_float(__builtin_amdgcn_raw_buffer_load_b32(c.ws, orow * 8 + 4, RS_OFF, AUX));
  u64 po = peek(c, fo, 8);
  WAITV(0);
  OPAQUE(xr[0]); OPAQUE(xr[1]); OPAQUE(frs);
  await(c, fo, 8, po);
  STAMP(c, 30);
  u32x4 af[8][2];
#pragma unroll
  for (int n = 0; n < 8; n++)
#pragma unroll
    for (int u = 0; u < 2; u++) af[n][u] = ld128(c.ws, lane * 16, OF_OFF + ((2 * i + ((m >> 1) ^ u)) * 32 + 8 * m + n) * 1024);
  SB();
  f32x16 acc[4];
  constexpr int NSM = 4;
  sfor<0, NSM>([&](auto NN) {
    constexpr int N = decltype(NN)::v;
    if constexpr (N == 0) {
      mma0(acc[0], bf[N][0], af[N][0]); mma0(acc[1], bf[N][1], af[N][0]); mma0(acc[2], bf[N][0], af[N][1]); mma0(acc[3], bf[N][1], af[N][1]);
    } else {
      mma(acc[0], bf[N][0], af[N][0]); mma(acc[1], bf[N][1], af[N][0]); mma(acc[2], bf[N][0], af[N][1]); mma(acc[3], bf[N][1], af[N][1]);
    }
  });
  sem_wait(SEM_F + m, 3);
  SB();
  sfor<0, 4>([&](auto KK) {
    constexpr int k = 3 - decltype(KK)::v;
    sfor<NSM, 8>([&](auto NN) {
      constexpr int N = decltype(NN)::v;
      mma(acc[k], bf[N][k & 1], af[N][k >> 1]);
      if constexpr (N == NSM + 1 && k < 3) {
        SB();
        put_block<12 + 2 * k>(c, acc[k + 1]);
        if constexpr (k == 0) sem_post(SEM_H);
        SB();
      }
    });
  });
  STAMP(c, 31);
  sem_wait(SEM_H, 4);
  STAMP(c, 32);
  asm volatile("s_nop 7" : "+v"(acc[0]));
  f32x4 v[4] = {q4<0>(acc[0]), q4<4>(acc[0]), q4<8>(acc[0]), q4<12>(acc[0])};
  add_block<12>(c, m ^ 1, v);
  add_block<14>(c, m ^ 2, v);
  add_block<16>(c, m ^ 3, v);
  sem_wait(SEM_C, 4);
  STAMP(c, 33);
  add_block<2>(c, m, v);
  add_block<4>(c, m ^ 1, v);
  add_block<6>(c, m ^ 2, v);
  add_block<8>(c, m ^ 3, v);
  if (threadIdx.x == 319) __hip_atomic_store((GAS u64*)(c.wsp + EP_OFF + b * 8), c.tag, __ATOMIC_RELAXED, __HIP_MEMORY_SCOPE_AGENT);
#pragma unroll
  for (int h = 0; h < 2; h++) {
    float e[8] = {v[2 * h][0], v[2 * h][1], v[2 * h][2], v[2 * h][3], v[2 * h + 1][0], v[2 * h + 1][1], v[2 * h + 1][2], v[2 * h + 1][3]};
#pragma unroll
    for (int k = 0; k < 4; k++) swap32(e[k], e[4 + k]);
    u32x4 o;
#pragma unroll
    for (int d = 0; d < 4; d++)
      o[d] = pk(__builtin_fmaf(e[2 * d], frs, __uint_as_float(xr[h][d] << 16)), __builtin_fmaf(e[2 * d + 1], frs, __uint_as_float(xr[h][d] & 0xffff0000u)));
    *(GAS u32x4*)(out + oidx + 16 * h) = o;
  }
}

template <bool FFN> struct Slots {
  static constexpr int NBK = FFN ? 7 : 6, N7 = 28 * NBK, NS = N7 + 5 * NBK;
  static constexpr int blk(int n) {
    if (n < N7) return n % NBK;
    const int k = (n - N7) / 5;
    return (!FFN && k < 2) ? 1 - k : k;
  }
  static constexpr int step(int n) { return n < N7 ? n / NBK : ((n - N7) % 5 == 0 ? -1 : 27 + (n - N7) % 5); }
  static constexpr int bar(int G) { return (G < 7 ? (4 * G + 3) * NBK : NS - NBK) + 1; }
};
template <bool FFN>
DI void compute1(Ctx& c, const Tile& t, u32x4 xa, u32x4 xb) {
  using SL = Slots<FFN>;
  constexpr int NBK = SL::NBK;
  const u32 lane = c.lane, m = c.m;
  const int rt = t.rt, j = t.j;
  const u32 hh = lane >> 5, blk = (lane >> 4) & 1, q = (lane & 15) >> 2, p = lane & 3, r = lane & 31;
  u32 ax[4], aw[2][2], ac;
#pragma unroll
  for (int s = 0; s < 4; s++) ax[s] = m * 4096 + r * 128 + (((2 * s + hh) ^ ((r >> 1) & 7)) << 4);
  const u32 at = (((2 * blk + (p >> 1)) ^ ((q >> 1) << 2)) << 4) + 8 * (p & 1);
  {
    const u32 a = (8 * hh + q) * 128 + at;
    aw[0][0] = a + LWA; aw[0][1] = (a ^ 64) + LWA; aw[1][0] = a + LWB; aw[1][1] = (a ^ 64) + LWB;
    ac = (8 * hh + q) * 64 + 32 * blk + 8 * p + LWB + 8192;
    OPAQUE(aw[0][0]); OPAQUE(aw[0][1]); OPAQUE(aw[1][0]); OPAQUE(aw[1][1]); OPAQUE(ac);
  }
  u32 acs = L_ST + r * 16;
  OPAQUE(acs);
  auto rdw = [&](auto NN) -> u32x4 {
    constexpr int T = SL::step(decltype(NN)::v), M = SL::blk(decltype(NN)::v);
    if constexpr (T < 0) return lds128(acs + M * 512);
    else {
      constexpr int S = (T >> 2) % 3, s = T & 3;
      if constexpr (M < 6) {
        const u32 a = (M < 4 ? aw[0][M & 1] + S * 16384 + (M >> 1) * 8192 : aw[1][M & 1] + S * 12288) + s * 2048;
        return trd2(a, a + 512);
      } else {
        const u32 a = ac + S * 12288 + s * 1024;
        return trd2(a, a + 256);
      }
    }
  };
  u32x4 xf[4], fm, wf[2 * NBK];
  f32x16 acc[NBK], P0, P1;
  float rstd;
  u32x2 sv[8];
  u32x4 eo;
  float sa;
  const u32 tb = rt * 4 + m;
  const u32 obase = FFN ? HF_OFF + tb * 112 * 1024 : QF_OFF + (tb * 32 + 4 * j) * 1024;
  WAITL0();
  STAMP(c, 1);
  WAITV(NCP);
  BAR();
  STAMP(c, 2);
  xf[0] = lds128(ax[0]);
  sfor<0, NBK>([&](auto NN) { wf[decltype(NN)::v] = rdw(NN); });
  SB();
  auto filler = [&](auto NN) {
    constexpr int n = decltype(NN)::v;
    if constexpr (n == SL::bar(0) - 5) {
      WAITV(0);
      OPAQUE(xa);
      OPAQUE(xb);
      const u32 a = LBS + m * 4096 + (4 * hh + q) * 128 + at;
#pragma unroll
      for (int k = 0; k < 8; k++) sv[k] = trd((a ^ ((k >> 2) * 64)) + (k & 3) * 1024);
      set_tag(c);
    }
    if constexpr (n == SL::bar(0) - 2) {
      u32 ones = ONES2;
      asm volatile("" : "+s"(ones));
      float st[4] = {0.f, 0.f, 0.f, 0.f}, cs[2] = {0.f, 0.f};
#pragma unroll
      for (int d = 0; d < 4; d++) {
        st[0] = dot2(ones, xa[d], st[0]);
        st[1] = dot2(xa[d], xa[d], st[1]);
        st[2] = dot2(ones, xb[d], st[2]);
        st[3] = dot2(xb[d], xb[d], st[3]);
      }
#pragma unroll
      for (int d = 0; d < 8; d++) {
        cs[0] = dot2(ones, sv[d >> 1][d & 1], cs[0]);
        cs[1] = dot2(ones, sv[4 + (d >> 1)][d & 1], cs[1]);
      }
      swap32(st[0], st[1]);
      swap32(st[2], st[3]);
      swap32(cs[0], cs[1]);
      *(LAS float*)(L_ST + (m * 64 + lane) * 4) = cs[0] + cs[1];
      st[0] += st[1];
      OPAQUE(st[0]);
      st[2] += st[3];
      auto sw16 = __builtin_amdgcn_permlane16_swap(__float_as_uint(st[0]), __float_as_uint(st[2]), false, false);
      sa = __uint_as_float(sw16[0]) + __uint_as_float(sw16[1]);
      OPAQUE(sa);
    }
    if constexpr (n == SL::bar(0) - 1) {
      sfor<0, 4>([&](auto KK) { sa += __int_as_float(__builtin_amdgcn_update_dpp(0, __float_as_int(sa), 0x120 + (8 >> decltype(KK)::v), 0xf, 0xf, false)); });
      float mean = sa * (1.f / 512.f), ms = mean;
      swap32(mean, ms);
      ms = __builtin_amdgcn_rsqf(mx(__builtin_fmaf(-mean, mean, ms), 0.f) + 1e-5f);
      const u32 mh = pk(-mean, -mean);
      const float ml = -mean - __uint_as_float(mh & 0xffff0000u);
      *(LAS u32x2*)(L_ST + ((lane & 47) == 0 ? 1024 + m * 16 + blk * 8 : 2048 + lane * 8)) = u32x2{(mh & 0xffffu) | (pk(ml, ml) & 0xffff0000u), __float_as_uint(ms)};
    }
    if constexpr (n >= SL::N7 + 5) {
      constexpr int g = (n - SL::N7) % 5, M = SL::blk(n - 5);
      if constexpr (g >= 1) {
        constexpr int s = (g - 1) >> 1, h = (g - 1) & 1;
        if constexpr (FFN || M < 4) {
          eo[2 * h] = pk2(pr<8 * s + 4 * h>(acc[M]));
          eo[2 * h + 1] = pk2(pr<8 * s + 4 * h + 2>(acc[M]));
          if constexpr (FFN) {
            eo[2 * h] = relu2(eo[2 * h]);
            eo[2 * h + 1] = relu2(eo[2 * h + 1]);
          }
          if constexpr (h == 0) OPAQUE(eo);
        }
        if constexpr (h == 1) {
          if constexpr (FFN) st128(eo, c.ws, lane * 16, obase + ((t.c0 >> 5) + 2 * M + s) * 1024);
          else if constexpr (M == 1) {
            *(LAS u32x4*)(((m >> 1) ? L_SP2 : L_SP1) + (m & 1) * 2048 + s * 1024 + lane * 16) = eo;
            if constexpr (s == 1) sem_post(SEM_K + m);
          } else if constexpr (M == 0) {
            const u32x4 wp = wphi_frag(L_WPHI + s * 512 + (4 * hh + q) * 32 + p * 8, blk ? 0x80008000u : 0u);
            asm volatile("s_nop 1\n\tv_mfma_f32_32x32x16_bf16 %0, %1, %2, 0" : "=&v"(s ? P1 : P0) : "v"(wp), "v"(eo));
          } else if constexpr (M < 4) st128(eo, c.ws, lane * 16, obase + (2 * (M - 2) + s) * 1024);
        }
      }
    }
  };
  sfor<0, SL::NS>([&](auto NN) {
    constexpr int n = decltype(NN)::v, T = SL::step(n), M = SL::blk(n);
    constexpr bool TR = !FFN && M >= 4;
    const u32x4 xo = T < 0 ? fm : xf[T & 3], wo_ = wf[n % (2 * NBK)];
    if constexpr (T == 0) mma0(acc[M], TR ? xo : wo_, TR ? wo_ : xo);
    else mma(acc[M], TR ? xo : wo_, TR ? wo_ : xo);
    constexpr int G = n < SL::N7 ? n / (4 * NBK) : 7;
    if constexpr (n == SL::bar(G)) {
      WAITL0();
      STAMP(c, 3 + 2 * G);
      BAR();
      STAMP(c, 4 + 2 * G);
    }
    if constexpr (n + NBK < SL::NS && n != SL::bar(G) - 1) {
      if constexpr (n < SL::N7 && (n == SL::bar(G) || (M == 0 && (T & 3) != 3))) {
        if constexpr (T == 27) {
          fm = lds128(hh ? L_ZERO : L_ST + 4096 + (m * 32 + r) * 16);
          xf[0] = lds128(ax[0] + 16384); xf[1] = lds128(ax[1] + 16384); xf[2] = lds128(ax[2] + 16384);
        } else xf[(T + 1) & 3] = lds128(ax[(T + 1) & 3] + (((T + 1) >> 2) % 3) * 16384);
      }
      if constexpr (n == SL::bar(G)) wf[(n - 1 + NBK) % (2 * NBK)] = rdw(IC<n - 1 + NBK>{});
      wf[(n + NBK) % (2 * NBK)] = rdw(IC<n + NBK>{});
      if constexpr (n == SL::N7) {
        xf[3] = lds128(ax[3] + 16384);
        if constexpr (!FFN) rstd = *(LAS float*)(L_RS + m * 128 + r * 4);
      }
    }
    filler(NN);
    SB();
  });
  __builtin_amdgcn_s_setprio(3);
  if constexpr (FFN) {
    asm volatile("s_nop 15" : "+v"(acc[6]));
    sfor<0, 2>([&](auto SS) {
      u32x4 o = pack8<decltype(SS)::v>(acc[6]);
#pragma unroll
      for (int d = 0; d < 4; d++) o[d] = relu2(o[d]);
      st128(o, c.ws, lane * 16, obase + ((t.cc >> 5) + decltype(SS)::v) * 1024);
    });
  } else {
    const u32 apw = lx(1) + m * 2048 + r * 64 + ((hh ^ ((r >> 1) & 7)) << 3);
    phi_store(apw, P0, rstd * LOG2E);
    SB();
    WAITV(0);
    publish(c, 0xf00ull);
    const u32 av0 = L_ST + m * 2048 + lane * 16, av1 = lwb(1) + m * 2048 + lane * 16;
    sfor<0, 2>([&](auto TT) {
      constexpr int tt = decltype(TT)::v;
      const f32x4 ra = ldsf4(L_RS + m * 128 + (16 * tt + 4 * hh) * 4), rb = ldsf4(L_RS + m * 128 + (16 * tt + 8 + 4 * hh) * 4);
      const f32x4 a0 = q4<8 * tt>(acc[4]) * ra, b0 = q4<8 * tt + 4>(acc[4]) * rb, a1 = q4<8 * tt>(acc[5]) * ra, b1 = q4<8 * tt + 4>(acc[5]) * rb;
      *(LAS u32x4*)(av0 + tt * 1024) = u32x4{pk(a0[0], a0[1]), pk(a0[2], a0[3]), pk(b0[0], b0[1]), pk(b0[2], b0[3])};
      *(LAS u32x4*)(av1 + tt * 1024) = u32x4{pk(a1[0], a1[1]), pk(a1[2], a1[3]), pk(b1[0], b1[1]), pk(b1[2], b1[3])};
    });
    phi_store(apw + 8192, P1, rstd * LOG2E);
    WAITL0();
    STAMP(c, 19);
    BAR();
    const u32 t0 = 4 * hh + q;
    const u32 apr = ((m >> 1) ? lwa(1) : lx(1)) + (m & 1) * 8192 + t0 * 64 + (((4 * blk + p) ^ (t0 >> 1)) << 3);
    const u32 avr = ((m >> 1) ? lwb(1) : L_ST) + lane * 16;
    const bool isv = blk == (m & 1);
    u32x4 pa[8], vb[8];
#pragma unroll
    for (int k = 0; k < 8; k++) {
      pa[k] = trd2(apr + k * 1024, (apr ^ 32) + k * 1024 + 512);
      vb[k] = lds128(avr + k * 1024);
    }
    sem_post(SEM_KV);
    f32x16 X = f32x16{};
#pragma unroll
    for (int k = 0; k < 8; k++) {
#pragma unroll
      for (int d = 0; d < 4; d++) vb[k][d] = isv ? vb[k][d] : ONES2;
      X = mfma(pa[k], vb[k], X);
    }
    const u32 pi = (4 * j + m) * 16 + rt;
    const u32 ve = isv ? KV_OFF + pi * 2048 + (hh * 16 + (lane & 15)) * 16 : KZ_OFF + pi * 128 + hh * 16, vs = isv ? 512 : 32;
    st128(__builtin_bit_cast(u32x4, q4<0>(X)), c.ws, ve, 0);
    st128(__builtin_bit_cast(u32x4, q4<4>(X)), c.ws, ve + vs, 0);
    st128(__builtin_bit_cast(u32x4, q4<8>(X)), c.ws, ve + 2 * vs, 0);
    st128(__builtin_bit_cast(u32x4, q4<12>(X)), c.ws, ve + 3 * vs, 0);
  }
  STAMP(c, 20);
  __builtin_amdgcn_s_setprio(0);
}
DI void compute2(Ctx& c) {
  const u32 lane = c.lane, m = c.m;
  const int b = blockIdx.x, i = (b & 7) * 4 + (b >> 6);
  init_ab(c);
  u32x4 af[28][2], bf[28][2];
  auto ldb = [&](auto NN) {
    constexpr int N = decltype(NN)::v;
    bf[N][0] = rdb<N>(c, 0);
    bf[N][1] = rdb<N>(c, 1);
  };
  sem_wait(SEM_B + m, 1);
  sfor<0, 5>(ldb);
  sem_post(SEM_F + m);
  WAITV(0);
  publish(c, 0xffull);
  STAMP(c, 23);
  sem_wait(SEM_B + m, 2);
  sfor<5, 12>(ldb);
  sem_post(SEM_F + m);
  const u32 fh = MB_OFF + b * 1024 + 32 * m;
  u64 ph = peek(c, fh, 4);
  STAMP(c, 24);
  WAITV(0);
  await(c, fh, 4, ph);
  STAMP(c, 25);
  const u32 ah = HF_OFF + ((2 * i) * 112 + 28 * m) * 1024;
  const u32 va[2] = {lane * 16 + (m >> 1) * 114688, lane * 16 + ((m >> 1) ^ 1) * 114688};
  auto lda = [&](auto NN, int u) {
    constexpr int N = decltype(NN)::v;
    if constexpr (N < 28) af[N][u] = ld128(c.ws, va[u], ah + hstep<N>(c) * 1024);
  };
  sfor<0, CW>([&](auto NN) { lda(NN, 0); lda(NN, 1); });
  SB();
  STAMP(c, 27);
  f32x16 ac2[4];
  sfor<0, 24>([&](auto NN) {
    constexpr int n = decltype(NN)::v;
    sfor<0, 4>([&](auto KK) {
      constexpr int k = decltype(KK)::v;
      if constexpr (n == 0) mma0(ac2[k], bf[n][k & 1], af[n][k >> 1]);
      else mma(ac2[k], bf[n][k & 1], af[n][k >> 1]);
      SB();
      if constexpr ((k & 1) == 1 && n + CW < 28) lda(IC<n + CW>{}, k >> 1);
      if constexpr ((k & 1) == 0 && n >= 8) bf[n + 4][k >> 1] = rdb<n + 4>(c, k >> 1);
      if constexpr (k == 3 && n == 7) sem_wait(SEM_B + m, 3);
      if constexpr (k == 3 && n == 13) sem_post(SEM_F + m);
      SB();
    });
  });
  STAMP(c, 31);
  sfor<0, 4>([&](auto KK) {
    constexpr int k = decltype(KK)::v;
    sfor<24, 28>([&](auto NN) {
      constexpr int n = decltype(NN)::v;
      mma(ac2[k], bf[n][k & 1], af[n][k >> 1]);
      if constexpr (n == 25 && k > 0) {
        SB();
        put_block<2 * k>(c, ac2[k - 1]);
        SB();
      }
    });
  });
  asm volatile("s_nop 15" : "+v"(ac2[3]));
  put_block<8>(c, ac2[3]);
  sem_post(SEM_C);
  STAMP(c, 32);
}

#define ROTB 1
__global__ __launch_bounds__(512, 2) void fused_k(const u16* x, const u16* wqkv, const u16* wphi, const u16* wo, const u16* w1, const u16* w2, u16* out,
                                                  char* ws) {
  __shared__ __attribute__((aligned(16))) char smem[163840];
  asm volatile("" ::"v"((LAS char*)smem));
  const int b = blockIdx.x, xcd = b & 7, r = b >> 3;
  u64 pc0;
  asm volatile("s_getpc_b64 %0" : "=s"(pc0));
  Ctx c;
  c.dbg = 0;
  STAMP(c, 0);
  c.lane = threadIdx.x & 63;
  const u32 w = __builtin_amdgcn_readfirstlane(threadIdx.x >> 6);
  c.m = w & 3;
  c.ws = __builtin_amdgcn_make_buffer_rsrc(ws, 0, WS_BYTES, 0x00020000);
  c.wsp = ws;
  c.rot = ROTB == 0 ? 0 : ROTB == 1 ? (b >> 6) * 7 : ((b >> 6) * 7 + (((b >> 3) & 7) * 7 >> 1)) % 28;
  const int rt = (xcd >> 2) * 8 + (r >> 2), ct = (xcd & 3) * 4 + (r & 3), j = ct >> 1;
  Tile t;
  t.rt = rt;
  t.j = j;
  t.ct = ct;
  t.ffn = ct & 1;
  t.ldw = t.ffn ? 3584 : 3072;
  const u32 lane = c.lane, m = c.m;
  t.srd_x = make_srd(x, 2048 * 512 * 2);
  t.srd_w = make_srd(t.ffn ? w1 : wqkv, 512 * t.ldw);
  t.vx0 = (lane >> 3) * 1024 + (((lane & 7) ^ (lane >> 4)) << 4);
  {
    const u32 sx = (rt * 128 + m * 32 + (w >> 2) * 16) * 1024, dx = m * 4096 + (w >> 2) * 2048;
    dma<LX, 0>(t.srd_x, t.vx0, sx, dx);
    dma<LX + 1024, 8192>(t.srd_x, t.vx0 ^ 64, sx, dx);
    t.vw = (lane >> 3) * t.ldw + (((lane & 7) ^ (((lane >> 4) & 1) << 2)) << 4);
    t.c0 = t.ffn ? 448 * j + (j & 1) * 64 : 1024 + 128 * j;
    t.c1 = t.ffn ? t.c0 + 128 : 128 * j;
    t.c2 = t.ffn ? t.c0 + 256 : 2048 + 128 * j;
    t.cc = t.ffn ? 448 * j + ((j & 1) ? 0 : 384) : 0;
    dma<LWA, 0>(t.srd_w, t.vw, w * 8 * t.ldw + t.c0, w * 1024);
    dma<LWA + 8192, 0>(t.srd_w, t.vw, w * 8 * t.ldw + t.c1, w * 1024);
    dma<LWB, 0>(t.srd_w, t.vw, w * 8 * t.ldw + t.c2, w * 1024);
    if (t.ffn && w >= 4) dma<LWB + 8192, 0>(t.srd_w, (lane >> 2) * 3584 + ((lane & 3) << 4), m * 16 * 3584 + t.cc, m * 1024);
    BAR();
    SB();
  }
  if (__builtin_expect(w >= 4, 1)) {
    helper(c, t, x, wo, w2, out);
  } else {
    u32x4 xa, xb;
    {
      u32 off = ((r * 4 + m) * 3 + (lane & 3)) * 128;
      off = off < CODE_BYTES ? off : 0;
      asm volatile("s_mov_b32 m0, %2\n\ts_mov_b64 exec, 7\n\tglobal_load_lds_dword %0, %1\n\ts_mov_b64 exec, -1" ::"v"(off), "s"(pc0), "i"(L_ST + 4096) : "memory");
      const u32 vs = m < 3 ? t.vw : (lane >> 3) * t.ldw + ((lane & 3) << 4);
      const u32 so = rt * 32 * t.ldw + (m == 0 ? t.c0 : m == 1 ? t.c1 : m == 2 ? t.c2 : t.cc);
      dma<LBS, 0>(t.srd_w, vs, so, m * 4096);
      dma<LBS + 1024, 0>(t.srd_w, vs, so + 8 * t.ldw, m * 4096);
      dma<LBS + 2048, 0>(t.srd_w, vs, so + 16 * t.ldw, m * 4096);
      dma<LBS + 3072, 0>(t.srd_w, vs, so + 24 * t.ldw, m * 4096);
      const u32 xo = (rt * 128 + ct * 8 + m * 2) * 1024 + lane * 16;
      asm volatile("global_load_dwordx4 %0, %1, %2" : "=v"(xa) : "v"(xo), "s"(x) : "memory");
      asm volatile("global_load_dwordx4 %0, %1, %2 offset:1024" : "=v"(xb) : "v"(xo), "s"(x) : "memory");
      dma<L_WPHI, 0>(make_srd(wphi, 32 * 512), lane * 16, m < 2 ? (u32)(j * 2048 + m * 1024) : (u32)(((b & 7) * 2 + ((b >> 4) & 1)) * 1024), (m < 2 ? m : 2) * 1024);
      const u32 ii = 2 * rt + (m >> 1), l7 = lane & 7;
      const u32 t1 = t.ffn ? (ii & 3) * 64 + (ii >> 2) + l7 * 8 : l7 * 32 + m * 8 + j, f1 = t.ffn ? j * 2 + (m & 1) : 16 + rt;
      load_tag(c, lane < 8 ? t1 : lane < 12 ? (rt >> 1) * 32 + (lane & 3) * 8 + j : b, lane < 8 ? f1 : 96 + (rt & 1) * 4 + m);
      *(LAS u32*)(L_SEM + (lane & 31) * 4) = 0;
    }
    if (t.ffn) compute1<true>(c, t, xa, xb);
    else compute1<false>(c, t, xa, xb);
    compute2(c);
  }
}

extern "C" void kernel_launch(void* const* d_in, const int* in_sizes, int n_in, void* d_out, int out_size, void* d_ws, size_t ws_size,
                              hipStream_t stream) {
  const u16 *x = (const u16*)d_in[0], *wqkv = (const u16*)d_in[1], *wphi = (const u16*)d_in[2], *wo = (const u16*)d_in[3];
  const u16 *w1 = (const u16*)d_in[5], *w2 = (const u16*)d_in[6];
  fused_k<<<256, 512, 0, stream>>>(x, wqkv, wphi, wo, w1, w2, (u16*)d_out, (char*)d_ws);
}
```

```cpp
#define PROBE 0
#include <hip/hip_runtime.h>
#pragma clang diagnostic ignored "-Wint-to-pointer-cast"
typedef unsigned short u16;
typedef unsigned u32;
typedef unsigned long long u64;
typedef u32 u32x2 __attribute__((ext_vector_type(2)));
typedef u32 u32x4 __attribute__((ext_vector_type(4)));
typedef short s16x2 __attribute__((ext_vector_type(2)));
typedef short s16x4 __attribute__((ext_vector_type(4)));
typedef float f32x2 __attribute__((ext_vector_type(2)));
typedef float f32x4 __attribute__((ext_vector_type(4)));
typedef float f32x16 __attribute__((ext_vector_type(16)));
typedef __bf16 b16x2 __attribute__((ext_vector_type(2)));
typedef __bf16 b16x8 __attribute__((ext_vector_type(8)));
#define LAS __attribute__((address_space(3)))
#define GAS __attribute__((address_space(1)))
#define DI __device__ __forceinline__
#define SB() __builtin_amdgcn_sched_barrier(0)
#define BAR() __builtin_amdgcn_s_barrier()
#define WAITV(n) do { asm volatile("s_waitcnt vmcnt(%0)" ::"i"(n) : "memory"); SB(); } while (0)
#define WAITL0() asm volatile("s_waitcnt lgkmcnt(0)" ::: "memory")
#define OPAQUE(x) asm volatile("" : "+v"(x))

constexpr u32 HF_OFF = 0;
constexpr u32 QF_OFF = 64u * 112u * 1024u;
constexpr u32 OF_OFF = QF_OFF + 64u * 32u * 1024u;
constexpr u32 KV_OFF = OF_OFF + 64u * 32u * 1024u;
constexpr u32 KZ_OFF = KV_OFF + 32u * 16u * 2048u;
constexpr u32 CS_OFF = KZ_OFF + 32u * 16u * 128u;
constexpr u32 RS_OFF = CS_OFF + 256u * 1024u;
constexpr u32 MB_OFF = RS_OFF + 2048u * 8u;
constexpr u32 EP_OFF = MB_OFF + 256u * 1024u;
constexpr u32 WS_BYTES = EP_OFF + 2048u;
constexpr u32 LX = 0, LWA = 49152, LWB = 98304, LBS = 135168, LSCR = 159744;
constexpr u32 lx(int s) { return LX + s * 16384; }
constexpr u32 lwa(int s) { return LWA + s * 16384; }
constexpr u32 lwb(int s) { return LWB + s * 12288; }
constexpr u32 L_WPHI = LSCR, L_WPA = LSCR + 2048, L_RS = LSCR + 3072, L_SEM = LSCR + 3584, L_ZERO = LSCR + 3648;
constexpr u32 L_SP1 = lwb(1) + 8192, L_SP2 = lwb(2) + 8192;
constexpr u32 L_ST = LBS + 16384;
constexpr int AUX = 16;

DI f32x16 mfma(u32x4 a, u32x4 b, f32x16 c) {
  return __builtin_amdgcn_mfma_f32_32x32x16_bf16(__builtin_bit_cast(b16x8, a), __builtin_bit_cast(b16x8, b), c, 0, 0, 0);
}
DI void mma(f32x16& d, u32x4 a, u32x4 b) { asm volatile("v_mfma_f32_32x32x16_bf16 %0, %1, %2, %0" : "+v"(d) : "v"(a), "v"(b)); }
DI void mma0(f32x16& d, u32x4 a, u32x4 b) { asm volatile("v_mfma_f32_32x32x16_bf16 %0, %1, %2, 0" : "=&v"(d) : "v"(a), "v"(b)); }
DI u32x2 trd(u32 addr) { return __builtin_bit_cast(u32x2, __builtin_amdgcn_ds_read_tr16_b64_v4i16((LAS s16x4*)addr)); }
DI u32x4 trd2(u32 a0, u32 a1) {
  u32x2 lo = trd(a0), hi = trd(a1);
  return __builtin_shufflevector(lo, hi, 0, 1, 2, 3);
}
DI u32x4 lds128(u32 addr) { return *(LAS u32x4*)addr; }
DI f32x4 ldsf4(u32 addr) { return *(LAS f32x4*)addr; }
DI u32 pk(float a, float b) {
  f32x2 v = {a, b};
  return __builtin_bit_cast(u32, __builtin_convertvector(v, b16x2));
}
DI u32 pk2(f32x2 v) { return __builtin_bit_cast(u32, __builtin_convertvector(v, b16x2)); }
DI u32 relu2(u32 v) { return __builtin_bit_cast(u32, __builtin_elementwise_max(__builtin_bit_cast(s16x2, v), s16x2{0, 0})); }
extern "C" __device__ float fmul_legacy_(float, float) __asm("llvm.amdgcn.fmul.legacy");
DI float mul1(float a, float b) { return fmul_legacy_(a, b); }
DI float mx(float a, float b) { return __builtin_elementwise_maximum(a, b); }
DI float dot2(u32 a, u32 b, float c) {
  return __builtin_amdgcn_fdot2_f32_bf16(__builtin_bit_cast(b16x2, a), __builtin_bit_cast(b16x2, b), c, false);
}
DI void swap32(float& a, float& b) {
  auto r = __builtin_amdgcn_permlane32_swap(__float_as_uint(a), __float_as_uint(b), false, false);
  a = __uint_as_float(r[0]);
  b = __uint_as_float(r[1]);
}
template <int I> DI f32x2 pr(const f32x16& a) { return __builtin_shufflevector(a, a, I, I + 1); }
template <int I> DI f32x4 q4(const f32x16& a) { return __builtin_shufflevector(a, a, I, I + 1, I + 2, I + 3); }
DI u32x4 make_srd(const void* p, u32 bytes) {
  u64 a = (u64)p;
  u32x4 r = {(u32)a, (u32)(a >> 32) & 0xffffu, bytes, 0x00020000u};
  return r;
}
template <u32 LI, u32 SI>
DI void dma(u32x4 srd, u32 voff, u32 sbase, u32 lbase) {
  u32 t;
  asm volatile("s_add_u32 m0, %4, %6\n\ts_add_u32 %0, %3, %5\n\tbuffer_load_dwordx4 %1, %2, %0 offen lds"
               : "=&s"(t)
               : "v"(voff), "s"(srd), "s"(sbase), "s"(lbase), "i"(SI), "i"(LI)
               : "memory", "scc");
}
DI void st128(u32x4 v, __amdgpu_buffer_rsrc_t rs, u32 voff, u32 soff) {
  __builtin_amdgcn_raw_buffer_store_b128(v, rs, voff, soff, AUX);
  asm volatile("s_nop 1" ::"v"(v) : "memory");
}
#define LDAUX AUX
DI u32x4 ld128(__amdgpu_buffer_rsrc_t rs, u32 voff, u32 soff) { return __builtin_amdgcn_raw_buffer_load_b128(rs, voff, soff, LDAUX); }
template <int OFF> DI void gld64(u64& d, u32 voff, const char* base) {
  asm volatile("global_load_dwordx2 %0, %1, %2 offset:%3 sc1" : "=v"(d) : "v"(voff), "s"(base), "i"(OFF) : "memory");
}
template <int OFF> DI void gld128(f32x4& d, u32 voff, const char* base) {
  asm volatile("global_load_dwordx4 %0, %1, %2 offset:%3 sc1" : "=v"(d) : "v"(voff), "s"(base), "i"(OFF) : "memory");
}
template <int N> struct IC { static constexpr int v = N; };
template <int B, int E, class F> DI void sfor(F&& f) {
  if constexpr (B < E) {
    f(IC<B>{});
    sfor<B + 1, E>(f);
  }
}
constexpr u32 ONES2 = 0x3f803f80u;
constexpr float LOG2E = 1.4426950408889634f;

DI void dual_softmax(const f32x16& P, float sl, u32x4& o0, u32x4& o1) {
  float m0 = mx(mx(mx(mx(mx(mx(mx(P[0], P[1]), P[2]), P[3]), P[4]), P[5]), P[6]), P[7]);
  float m1 = mx(mx(mx(mx(mx(mx(mx(P[8], P[9]), P[10]), P[11]), P[12]), P[13]), P[14]), P[15]);
  swap32(m0, m1);
  m0 = mx(m0, m1) * -sl;
  m1 = m0;
  swap32(m0, m1);
  f32x2 e[8];
  sfor<0, 8>([&](auto II) {
    constexpr int I = decltype(II)::v;
    const float m = I < 4 ? m0 : m1;
    e[I] = __builtin_elementwise_fma(pr<2 * I>(P), f32x2{sl, sl}, f32x2{m, m});
  });
#pragma unroll
  for (int i = 0; i < 8; i++) e[i] = f32x2{__builtin_amdgcn_exp2f(e[i][0]), __builtin_amdgcn_exp2f(e[i][1])};
  const f32x2 s0 = (e[0] + e[1]) + (e[2] + e[3]), s1 = (e[4] + e[5]) + (e[6] + e[7]);
  m0 = s0[0] + s0[1];
  OPAQUE(m0);
  m1 = s1[0] + s1[1];
  swap32(m0, m1);
  m0 = __builtin_amdgcn_rcpf(m0 + m1);
  m1 = m0;
  swap32(m0, m1);
#pragma unroll
  for (int i = 0; i < 4; i++) {
    o0[i] = pk2(e[i] * m0);
    o1[i] = pk2(e[4 + i] * m1);
  }
}
DI u32x4 wphi_frag(u32 a, u32 sg) {
  u32x4 f = trd2(a, a + 256);
  f[0] ^= sg; f[1] ^= sg; f[2] ^= sg; f[3] ^= sg;
  return f;
}
DI void phi_store(u32 apw, const f32x16& P, float sl) {
  u32x4 o0, o1;
  dual_softmax(P, sl, o0, o1);
  *(LAS u32x2*)(apw) = u32x2{o0[0], o0[1]};
  *(LAS u32x2*)(apw ^ 16) = u32x2{o0[2], o0[3]};
  *(LAS u32x2*)(apw ^ 32) = u32x2{o1[0], o1[1]};
  *(LAS u32x2*)(apw ^ 48) = u32x2{o1[2], o1[3]};
}
template <int S> DI u32x4 pack8(const f32x16& a) { return u32x4{pk2(pr<8 * S>(a)), pk2(pr<8 * S + 2>(a)), pk2(pr<8 * S + 4>(a)), pk2(pr<8 * S + 6>(a))}; }

constexpr u32 slot_loc(int s, int k) { return k < 2 ? lx(s) + k * 8192 : k < 4 ? lwa(s) + (k - 2) * 8192 : lwb(s); }
constexpr u32 bloc(int n) {
  return n < 5 ? slot_loc(2, n) : n < 10 ? slot_loc(0, n - 5) : n < 15 ? slot_loc(1, n - 10) : n == 15 ? LBS + 16384
       : n < 26 ? bloc(n - 16) : LBS + (n & 1) * 8192;
}

#define STAMP(c, i) do { if ((i) == PROBE || (i) == 0) { asm volatile("s_memrealtime %0\n\ts_waitcnt lgkmcnt(0)" : "=s"((c).tp)::"memory"); } } while (0)
struct Ctx {
  mutable u32 dbg;
  mutable u64 tp;
  __amdgpu_buffer_rsrc_t ws;
  char* wsp;
  u32 m, lane;
  u32 ab[3][2];
  u32 rot;
  u64 tag;
  u64 own;
  u32 fl;
};
template <int N> DI u32x4 rdb(const Ctx& c, int cb) {
  constexpr u32 W = bloc(N) / 57344;
  const u32 a = c.ab[W][cb] + (bloc(N) - W * 57344);
  return trd2(a, a + 1024);
}
DI void init_ab(Ctx& c) {
  const u32 lane = c.lane, m = c.m, hh = lane >> 5, blk = (lane >> 4) & 1, q = (lane & 15) >> 2, p = lane & 3;
#pragma unroll
  for (int cb = 0; cb < 2; cb++) {
    const u32 t = m * 2048 + (4 * hh + q) * 128 + (((4 * (cb ^ (m & 1)) + 2 * blk + (p >> 1)) ^ ((q >> 1) << 2)) << 4) + 8 * (p & 1);
#pragma unroll
    for (int k = 0; k < 3; k++) {
      c.ab[k][cb] = t + k * 57344;
      OPAQUE(c.ab[k][cb]);
    }
  }
}
DI void load_tag(Ctx& c, u32 tgt, u32 slot) {
  c.fl = MB_OFF + tgt * 1024 + slot * 8;
  asm volatile("global_load_dwordx2 %0, %1, %2 sc1" : "=v"(c.tag) : "v"(EP_OFF + tgt * 8), "s"(c.wsp) : "memory");
}
DI void set_tag(Ctx& c) {
  OPAQUE(c.tag);
  c.tag += 1;
  c.own = ((u64)(u32)__builtin_amdgcn_readlane((u32)(c.tag >> 32), 63) << 32) | (u32)__builtin_amdgcn_readlane((u32)c.tag, 63);
}
DI void publish(const Ctx& c, u64 mask) {
  asm volatile("s_mov_b64 exec, %3\n\tglobal_atomic_swap_x2 %0, %1, %2 sc1\n\ts_mov_b64 exec, -1" ::"v"(c.fl), "v"(c.tag), "s"(c.wsp), "s"(mask) : "memory");
}
DI u64 peek(const Ctx& c, u32 off, int n) {
  u64 v = c.own;
  const u64 z = 0;
  asm volatile("s_mov_b64 exec, %4\n\tglobal_atomic_or_x2 %0, %1, %2, %3 sc0 sc1\n\ts_mov_b64 exec, -1"
               : "+v"(v)
               : "v"(off + c.lane * 8), "v"(z), "s"(c.wsp), "s"((1ull << n) - 1)
               : "memory");
  return v;
}
DI void await(const Ctx& c, u32 off, int n, u64 v) {
  OPAQUE(v);
#pragma unroll 1
  for (int it = 0; it < (1 << 16) && !__all(v == c.own); it++) {
    __builtin_amdgcn_s_sleep(2);
    v = peek(c, off, n);
    asm volatile("s_waitcnt vmcnt(0)" : "+v"(v)::"memory");
  }
}
DI void sem_post(u32 idx) {
  asm volatile("s_mov_b64 exec, 1\n\tds_add_u32 %0, %1\n\ts_mov_b64 exec, -1" ::"v"(L_SEM + idx * 4), "v"(1u) : "memory");
}
DI void sem_wait(u32 idx, u32 n) {
#pragma unroll 1
  for (int it = 0; it < (1 << 20); it++) {
    if (__builtin_amdgcn_readfirstlane(*(volatile LAS u32*)(L_SEM + idx * 4)) >= n) break;
    __builtin_amdgcn_s_sleep(1);
  }
  asm volatile("" ::: "memory");
}
constexpr u32 SEM_B = 0, SEM_K = 8, SEM_A = 12, SEM_KV = 13, SEM_C = 14, SEM_H = 15;
template <int N> DI u32 hstep(const Ctx& c) {
  const u32 h = c.rot + N;
  return h >= 28 ? h - 28 : h;
}
struct BSrc {
  u32x4 srd;
  u32 vb, sb, d2;
  DI void init(const Ctx& c, const u16* w, u32 rows, u32 rows_per_q) {
    srd = make_srd(w, rows * 1024);
    vb = (c.lane >> 3) * 1024 + (((c.lane & 7) ^ (((c.lane >> 4) & 1) << 2)) << 4);
    sb = c.m * rows_per_q * 1024 + ((blockIdx.x >> 3) & 7) * 128;
    d2 = c.m * 2048;
  }
  template <int N> DI void issue(const Ctx& c) const {
    const u32 so = sb + (N < 28 ? hstep<N>(c) : N - 28) * 16384;
    dma<bloc(N), 0>(srd, vb, so, d2);
    dma<bloc(N) + 1024, 8192>(srd, vb, so, d2);
  }
};

struct Tile {
  int rt, j, ct;
  bool ffn;
  u32 ldw;
  u32 c0, c1, c2, cc;
  u32x4 srd_x, srd_w;
  u32 vx0, vw;
};
DI u32 blk_of(u32 rt, u32 ct) { return (((rt & 7) * 4 + (ct & 3)) << 3) + (rt >> 3) * 4 + (ct >> 2); }

template <int N> DI void put_block(const Ctx& c, const f32x16& a) {
  const u32 p = c.m * 2048 + c.lane * 16;
  *(LAS f32x4*)(p + bloc(N)) = q4<0>(a);
  *(LAS f32x4*)(p + bloc(N) + 1024) = q4<4>(a);
  *(LAS f32x4*)(p + bloc(N + 1)) = q4<8>(a);
  *(LAS f32x4*)(p + bloc(N + 1) + 1024) = q4<12>(a);
}
template <int N> DI void add_block(const Ctx& c, u32 src, f32x4 (&v)[4]) {
  const u32 p = src * 2048 + c.lane * 16;
  v[0] += ldsf4(p + bloc(N));
  v[1] += ldsf4(p + bloc(N) + 1024);
  v[2] += ldsf4(p + bloc(N + 1));
  v[3] += ldsf4(p + bloc(N + 1) + 1024);
}

#define PFCODE 34816
#define TOUCH 0
#define CW 8
#define NB7 4
constexpr int NCP = 9;
constexpr u32 CODE_BYTES = PFCODE;
DI void helper(Ctx& c, const Tile& t, const u16* x, const u16* wo, const u16* w2, u16* out) {
  constexpr int ND = 10;
  const u32 lane = c.lane, m = c.m;
  const int b = blockIdx.x, i = (b & 7) * 4 + (b >> 6);
  const int hd = (b & 7) * 4 + ((b >> 3) & 3), ga = b >> 5;
  const u32 vx1 = t.vx0 ^ 64, vw1 = t.vw + 8 * t.ldw;
  const u32 vc = (lane >> 2) * 3584 + ((lane & 3) << 4);
  const u32 sx = (t.rt * 128 + m * 32) * 1024, sc = m * 16 * 3584 + t.cc;
  const u32 d4 = m * 4096, d2 = m * 2048, d1 = m * 1024;
  auto ringf = [&](auto FF) {
    constexpr int G = decltype(FF)::v / ND, S = G % 3, I = decltype(FF)::v % ND;
    const u32 sg = (m * 16 + G * 64) * t.ldw;
    if constexpr (I == 4) {
      if (t.ffn) dma<lwb(S) + 8192, G * 64 * 3584>(t.srd_w, vc, sc, d1);
    }
    if constexpr (I < 4) dma<lx(S) + I * 1024, I * 8192 + G * 128>(t.srd_x, (I & 1) ? vx1 : t.vx0, sx, d4);
    else if constexpr (I < 6) dma<lwa(S) + (I & 1) * 1024, 0>(t.srd_w, (I & 1) ? vw1 : t.vw, sg + t.c0, d2);
    else if constexpr (I < 8) dma<lwa(S) + 8192 + (I & 1) * 1024, 0>(t.srd_w, (I & 1) ? vw1 : t.vw, sg + t.c1, d2);
    else dma<lwb(S) + (I & 1) * 1024, 0>(t.srd_w, (I & 1) ? vw1 : t.vw, sg + t.c2, d2);
  };
  constexpr auto rend = [](int G) { return G < 0 ? ND + 3 : G == 0 ? 2 * ND + 5 : G == 1 ? 3 * ND + 8 : G < 6 ? (G + 3) * ND : 8 * ND; };
  sfor<ND, rend(-1)>(ringf);
  load_tag(c, lane < 16 ? blk_of(t.rt, lane) : lane < 32 ? blk_of(lane & 15, t.ct) : lane < 40 ? m * 64 + (lane & 7) * 8 + ga : b,
           lane < 16 ? 64 + t.ct : lane < 32 ? 80 + t.rt : 32 + hd);
  BSrc b2, bo;
  b2.init(c, w2, 1792, 448);
  bo.init(c, wo, 512, 128);
  const u32 hh = lane >> 5, blk = (lane >> 4) & 1, q = (lane & 15) >> 2, p = lane & 3, r = lane & 31;
  init_ab(c);
  u32x4 bf[8][2];
  auto ldb = [&](auto NN) {
    constexpr int N = decltype(NN)::v;
    bf[N - 28][0] = rdb<N>(c, 0);
    bf[N - 28][1] = rdb<N>(c, 1);
  };
  u64 ps = 0, grs = 0;
  f32x4 gc[4];
  __builtin_amdgcn_s_setprio(1);
  STAMP(c, 1);
  WAITV(4);
  BAR();
  STAMP(c, 2);
  constexpr auto wo_in = [](int G) { return G == 3 ? 28 : G == 4 ? 29 : G == 6 ? 30 : G == 7 ? 31 : -1; };
  float sv[4], scs;
  sfor<0, 8>([&](auto GG) {
    constexpr int G = decltype(GG)::v;
    constexpr int NRB = G == 6 ? 10 : G == 7 ? 2 * NB7 : 0;
    if constexpr (G == 5 || G == 6) ldb(IC<23 + G>{});
    if constexpr (G == 1) {
      const u32 cv = *(LAS u32*)(L_ST + (m * 64 + lane) * 4), rv = *(LAS u32*)(L_ST + 1024 + (lane & 15) * 4);
      __builtin_amdgcn_raw_buffer_store_b32(cv, c.ws, lane * 4, CS_OFF + (t.ct * 16 + t.rt) * 1024 + m * 256, AUX);
      asm volatile("s_mov_b64 exec, %3\n\tglobal_store_dword %0, %1, %2 sc1\n\ts_mov_b64 exec, -1" ::"v"(RS_OFF + (t.rt * 128 + t.ct * 8) * 8 + (lane & 15) * 4),
                   "v"(rv), "s"(c.wsp), "s"(m == 0 ? 0xffffull : 0ull)
                   : "memory");
      set_tag(c);
    }
    if constexpr (G == 2) publish(c, 0xffull << (8 * m));
    if constexpr (G == 4) ps = peek(c, MB_OFF + b * 1024 + 512, 32);
    if constexpr (G == 5) {
      await(c, MB_OFF + b * 1024 + 512, 32, ps);
      gld64<0>(grs, RS_OFF + (t.rt * 128 + m * 32 + r) * 8, c.wsp);
      const u32 bc = CS_OFF + t.ct * 16384 + (lane >> 4) * 4096 + m * 256 + (lane & 15) * 16;
      gld128<0>(gc[0], bc, c.wsp); gld128<1024>(gc[1], bc, c.wsp); gld128<2048>(gc[2], bc, c.wsp); gld128<3072>(gc[3], bc, c.wsp);
    }
    sfor<rend(G - 1), rend(G)>(ringf);
    if constexpr (G >= 6)
      sfor<(G - 6) * 5, (G - 6) * 5 + NRB / 2>([&](auto NN) {
        constexpr int N = decltype(NN)::v;
        b2.issue<N>(c);
        SB();
        if constexpr (N == 0) {
#pragma unroll
          for (int k = 0; k < 4; k++) OPAQUE(gc[k]);
#pragma unroll
          for (int k = 0; k < 4; k++) {
            sv[k] = gc[0][k];
#pragma unroll
            for (int u = 1; u < 4; u++) {
              sv[k] += gc[u][k];
              OPAQUE(sv[k]);
            }
          }
        } else if constexpr (N == 1) {
          swap32(sv[0], sv[2]);
          swap32(sv[1], sv[3]);
          sv[0] += sv[2];
          OPAQUE(sv[0]);
          sv[1] += sv[3];
          auto sw16 = __builtin_amdgcn_permlane16_swap(__float_as_uint(sv[0]), __float_as_uint(sv[1]), false, false);
          scs = __uint_as_float(sw16[0]) + __uint_as_float(sw16[1]);
          OPAQUE(scs);
        } else if constexpr (N == 2) {
          const u32 h2 = pk(scs, scs);
          const float lo = scs - __uint_as_float(h2 & 0xffff0000u);
          *(LAS u32x4*)(L_ST + (m * 64 + (lane & 15) * 4 + (lane >> 4)) * 16) = u32x4{h2, pk(lo, lo), 0u, 0u};
        } else if constexpr (N == 3) {
          OPAQUE(grs);
          *(LAS u32x4*)(L_ST + 4096 + (m * 32 + r) * 16) = u32x4{(u32)grs, (u32)grs, 0u, 0u};
          *(LAS u32*)(L_RS + m * 128 + r * 4) = (u32)(grs >> 32);
        }
      });
    WAITL0();
    if constexpr (wo_in(G) > 0) bo.issue<wo_in(G)>(c);
    STAMP(c, 3 + 2 * G);
    if constexpr (G < 7) WAITV(G < 3 ? rend(G) - (G + 2) * ND : rend(G) - rend(G - 1) + NRB + (wo_in(G) > 0 ? 2 : 0));
    if (G == 7 && !t.ffn) {
      const u32 awp = L_WPHI + 1024 + (4 * hh + q) * 32 + p * 8, sg = blk ? 0x80008000u : 0u;
      const u32x4 w0 = wphi_frag(awp, sg), w1 = wphi_frag(awp + 512, sg);
      const float sl = __uint_as_float((u32)(grs >> 32)) * LOG2E;
      sem_wait(SEM_K + m, 1);
      const u32 ak = ((m >> 1) ? L_SP2 : L_SP1) + (m & 1) * 2048 + lane * 16;
      const f32x16 P0 = mfma(w0, lds128(ak), f32x16{}), P1 = mfma(w1, lds128(ak + 1024), f32x16{});
      BAR();
      const u32 apw = lwa(1) + m * 2048 + r * 64 + ((hh ^ ((r >> 1) & 7)) << 3);
      phi_store(apw, P0, sl);
      phi_store(apw + 8192, P1, sl);
      WAITL0();
    }
    BAR();
    STAMP(c, 4 + 2 * G);
  });
  __builtin_amdgcn_s_setprio(2);
  const u32 mb = MB_OFF + b * 1024;
  const u32 fk = mb + 128, fo = mb + 256 + 64 * m, fq = mb + 768 + 16 * m;
  u64 pq = peek(c, fq, 2);
  WAITV(1);
  ldb(IC<30>{});
  ldb(IC<31>{});
  sem_post(SEM_B + m);
  WAITL0();
  bo.issue<32>(c);
  bo.issue<33>(c);
  WAITV(4);
  STAMP(c, 20);
  await(c, fq, 2, pq);
  STAMP(c, 24);
  u32x4 p00, p01, p10, p11;
  u64 pkv;
  float rs0, rs1;
  {
    u32x4 qf[2];
#pragma unroll
    for (int u = 0; u < 2; u++) qf[u] = ld128(c.ws, lane * 16, QF_OFF + ((8 * ga + 2 * m + u) * 32 + hd) * 1024);
    rs0 = __uint_as_float(__builtin_amdgcn_raw_buffer_load_b32(c.ws, lane * 8 + 4, RS_OFF + (8 * ga + 2 * m) * 256, AUX));
    OPAQUE(rs0);
    SB();
    ldb(IC<32>{});
    ldb(IC<33>{});
    WAITL0();
    bo.issue<34>(c);
    bo.issue<35>(c);
    rs1 = rs0;
    swap32(rs0, rs1);
    const u32x4 wp = wphi_frag(L_WPA + (hd & 1) * 512 + (4 * hh + q) * 32 + p * 8, blk ? 0x80008000u : 0u);
    const f32x16 Q0 = mfma(wp, qf[0], f32x16{}), Q1 = mfma(wp, qf[1], f32x16{});
    dual_softmax(Q0, rs0 * LOG2E, p00, p01);
    OPAQUE(p00); OPAQUE(p01);
    SB();
    pkv = peek(c, fk, 16);
    dual_softmax(Q1, rs1 * LOG2E, p10, p11);
    OPAQUE(p10); OPAQUE(p11);
  }
  STAMP(c, 21);
  WAITV(0);
  ldb(IC<34>{});
  ldb(IC<35>{});
  sem_post(SEM_B + m);
  await(c, fk, 16, pkv);
  STAMP(c, 25);
  {
    f32x4 part[8];
#pragma unroll
    for (int s = 0; s < 8; s++) part[s] = __builtin_bit_cast(f32x4, ld128(c.ws, hh * 2048 + r * 16, KV_OFF + (hd * 16 + 2 * s) * 2048 + m * 512));
    f32x4 z = __builtin_bit_cast(f32x4, ld128(c.ws, (lane & 15) * 128 + blk * 16, KZ_OFF + hd * 2048 + m * 32));
    SB();
    float v[4] = {part[0][0], part[0][1], part[0][2], part[0][3]};
#pragma unroll
    for (int s = 1; s < 8; s++)
#pragma unroll
      for (int k = 0; k < 4; k++) {
        v[k] += part[s][k];
        OPAQUE(v[k]);
      }
    swap32(v[0], v[2]);
    swap32(v[1], v[3]);
    v[0] += v[2];
    OPAQUE(v[0]);
    *(LAS u32*)(L_WPHI + r * 32 + m * 8 + hh * 4) = pk(v[0], v[1] + v[3]);
    float za = hh ? z[2] : z[0], zb = hh ? z[3] : z[1];
    sfor<0, 4>([&](auto KK) {
      za += __int_as_float(__builtin_amdgcn_update_dpp(0, __float_as_int(za), 0x120 + (8 >> decltype(KK)::v), 0xf, 0xf, false));
      zb += __int_as_float(__builtin_amdgcn_update_dpp(0, __float_as_int(zb), 0x120 + (8 >> decltype(KK)::v), 0xf, 0xf, false));
    });
    *(LAS u32*)(L_WPHI + (32 + blk) * 32 + m * 8 + hh * 4) = pk(za, zb);
    sem_post(SEM_A);
    sem_wait(SEM_A, 4);
    const u32 e = blk == 0 ? hh * 16 + (lane & 15) : 32 + hh;
    const u32x4 k0 = lds128(L_WPHI + e * 32), k1 = lds128(L_WPHI + e * 32 + 16);
    f32x16 Z0 = mfma(k0, p00, f32x16{}), Z1 = mfma(k0, p10, f32x16{});
    Z0 = mfma(k1, p01, Z0);
    Z1 = mfma(k1, p11, Z1);
    constexpr int NO = 0;
    const float r0 = __builtin_amdgcn_rcpf(mul1(Z0[8 - NO] + 1e-6f, rs0)), r1 = __builtin_amdgcn_rcpf(mul1(Z1[8 - NO] + 1e-6f, rs1));
    u32x4 o0, o1;
#pragma unroll
    for (int d = 0; d < 4; d++) {
      o0[d] = pk(mul1(Z0[NO + 2 * d], r0), mul1(Z0[NO + 2 * d + 1], r0));
      o1[d] = pk(mul1(Z1[NO + 2 * d], r1), mul1(Z1[NO + 2 * d + 1], r1));
    }
    st128(o0, c.ws, lane * 16, OF_OFF + ((8 * ga + 2 * m) * 32 + hd) * 1024);
    st128(o1, c.ws, lane * 16, OF_OFF + ((8 * ga + 2 * m + 1) * 32 + hd) * 1024);
  }
  STAMP(c, 26);
  WAITV(0);
  publish(c, 0xff00000000ull);
  STAMP(c, 27);
  const u32 orow = 64 * i + 32 * (m >> 1) + r, oidx = orow * 512 + 64 * ((b >> 3) & 7) + 32 * (m & 1) + 8 * hh;
  u32x4 xr[2] = {*(const GAS u32x4*)(x + oidx), *(const GAS u32x4*)(x + oidx + 16)};
  float frs = __uint_as_float(__builtin_amdgcn_raw_buffer_load_b32(c.ws, orow * 8 + 4, RS_OFF, AUX));
  u64 po = peek(c, fo, 8);
  WAITV(0);
  OPAQUE(xr[0]); OPAQUE(xr[1]); OPAQUE(frs);
  await(c, fo, 8, po);
  STAMP(c, 30);
  u32x4 af[8][2];
#pragma unroll
  for (int n = 0; n < 8; n++)
#pragma unroll
    for (int u = 0; u < 2; u++) af[n][u] = ld128(c.ws, lane * 16, OF_OFF + ((2 * i + ((m >> 1) ^ u)) * 32 + 8 * m + n) * 1024);
  SB();
  f32x16 acc[4];
  constexpr int NSM = 4;
  sfor<0, NSM>([&](auto NN) {
    constexpr int N = decltype(NN)::v;
    if constexpr (N == 0) {
      mma0(acc[0], bf[N][0], af[N][0]); mma0(acc[1], bf[N][1], af[N][0]); mma0(acc[2], bf[N][0], af[N][1]); mma0(acc[3], bf[N][1], af[N][1]);
    } else {
      mma(acc[0], bf[N][0], af[N][0]); mma(acc[1], bf[N][1], af[N][0]); mma(acc[2], bf[N][0], af[N][1]); mma(acc[3], bf[N][1], af[N][1]);
    }
  });
  sem_wait(SEM_B + m, 3);
  SB();
  sfor<0, 4>([&](auto KK) {
    constexpr int k = 3 - decltype(KK)::v;
    sfor<NSM, 8>([&](auto NN) {
      constexpr int N = decltype(NN)::v;
      mma(acc[k], bf[N][k & 1], af[N][k >> 1]);
      if constexpr (N == NSM + 1 && k < 3) {
        SB();
        put_block<10 + 2 * k>(c, acc[k + 1]);
        if constexpr (k == 0) sem_post(SEM_H);
        SB();
      }
    });
  });
  STAMP(c, 31);
  sem_wait(SEM_H, 4);
  STAMP(c, 32);
  asm volatile("s_nop 7" : "+v"(acc[0]));
  f32x4 v[4] = {q4<0>(acc[0]), q4<4>(acc[0]), q4<8>(acc[0]), q4<12>(acc[0])};
  add_block<10>(c, m ^ 1, v);
  add_block<12>(c, m ^ 2, v);
  add_block<14>(c, m ^ 3, v);
  sem_wait(SEM_C, 4);
  STAMP(c, 33);
  add_block<0>(c, m, v);
  add_block<2>(c, m ^ 1, v);
  add_block<4>(c, m ^ 2, v);
  add_block<6>(c, m ^ 3, v);
  if (threadIdx.x == 319) __hip_atomic_store((GAS u64*)(c.wsp + EP_OFF + b * 8), c.tag, __ATOMIC_RELAXED, __HIP_MEMORY_SCOPE_AGENT);
#pragma unroll
  for (int h = 0; h < 2; h++) {
    float e[8] = {v[2 * h][0], v[2 * h][1], v[2 * h][2], v[2 * h][3], v[2 * h + 1][0], v[2 * h + 1][1], v[2 * h + 1][2], v[2 * h + 1][3]};
#pragma unroll
    for (int k = 0; k < 4; k++) swap32(e[k], e[4 + k]);
    u32x4 o;
#pragma unroll
    for (int d = 0; d < 4; d++)
      o[d] = pk(__builtin_fmaf(e[2 * d], frs, __uint_as_float(xr[h][d] << 16)), __builtin_fmaf(e[2 * d + 1], frs, __uint_as_float(xr[h][d] & 0xffff0000u)));
    *(GAS u32x4*)(out + oidx + 16 * h) = o;
  }
}

template <bool FFN> struct Slots {
  static constexpr int NBK = FFN ? 7 : 6, N7 = 28 * NBK, NS = N7 + 5 * NBK;
  static constexpr int blk(int n) {
    if (n < N7) return n % NBK;
    const int k = (n - N7) / 5;
    return (!FFN && k < 2) ? 1 - k : k;
  }
  static constexpr int step(int n) { return n < N7 ? n / NBK : ((n - N7) % 5 == 0 ? -1 : 27 + (n - N7) % 5); }
  static constexpr int bar(int G) { return (G < 7 ? (4 * G + 3) * NBK : NS - NBK) + 1; }
};
template <bool FFN>
DI void compute1(Ctx& c, const Tile& t, u32x4 xa, u32x4 xb) {
  using SL = Slots<FFN>;
  constexpr int NBK = SL::NBK;
  const u32 lane = c.lane, m = c.m;
  const int rt = t.rt, j = t.j;
  const u32 hh = lane >> 5, blk = (lane >> 4) & 1, q = (lane & 15) >> 2, p = lane & 3, r = lane & 31;
  u32 ax[4], aw[2][2], ac;
#pragma unroll
  for (int s = 0; s < 4; s++) ax[s] = m * 4096 + r * 128 + (((2 * s + hh) ^ ((r >> 1) & 7)) << 4);
  const u32 at = (((2 * blk + (p >> 1)) ^ ((q >> 1) << 2)) << 4) + 8 * (p & 1);
  {
    const u32 a = (8 * hh + q) * 128 + at;
    aw[0][0] = a + LWA; aw[0][1] = (a ^ 64) + LWA; aw[1][0] = a + LWB; aw[1][1] = (a ^ 64) + LWB;
    ac = (8 * hh + q) * 64 + 32 * blk + 8 * p + LWB + 8192;
    OPAQUE(aw[0][0]); OPAQUE(aw[0][1]); OPAQUE(aw[1][0]); OPAQUE(aw[1][1]); OPAQUE(ac);
  }
  u32 acs = L_ST + r * 16;
  OPAQUE(acs);
  auto rdw = [&](auto NN) -> u32x4 {
    constexpr int T = SL::step(decltype(NN)::v), M = SL::blk(decltype(NN)::v);
    if constexpr (T < 0) return lds128(acs + M * 512);
    else {
      constexpr int S = (T >> 2) % 3, s = T & 3;
      if constexpr (M < 6) {
        const u32 a = (M < 4 ? aw[0][M & 1] + S * 16384 + (M >> 1) * 8192 : aw[1][M & 1] + S * 12288) + s * 2048;
        return trd2(a, a + 512);
      } else {
        const u32 a = ac + S * 12288 + s * 1024;
        return trd2(a, a + 256);
      }
    }
  };
  u32x4 xf[4], fm, wf[2 * NBK];
  f32x16 acc[NBK], P0, P1;
  float rstd;
  u32x2 sv[8];
  u32x4 eo;
  float sa;
  const u32 tb = rt * 4 + m;
  const u32 obase = FFN ? HF_OFF + tb * 112 * 1024 : QF_OFF + (tb * 32 + 4 * j) * 1024;
  WAITL0();
  STAMP(c, 1);
  WAITV(NCP);
  BAR();
  STAMP(c, 2);
  xf[0] = lds128(ax[0]);
  sfor<0, NBK>([&](auto NN) { wf[decltype(NN)::v] = rdw(NN); });
  SB();
  auto filler = [&](auto NN) {
    constexpr int n = decltype(NN)::v;
    if constexpr (n == SL::bar(0) - 5) {
      WAITV(0);
      OPAQUE(xa);
      OPAQUE(xb);
      const u32 a = LBS + m * 4096 + (4 * hh + q) * 128 + at;
#pragma unroll
      for (int k = 0; k < 8; k++) sv[k] = trd((a ^ ((k >> 2) * 64)) + (k & 3) * 1024);
      set_tag(c);
    }
    if constexpr (n == SL::bar(0) - 2) {
      u32 ones = ONES2;
      asm volatile("" : "+s"(ones));
      float st[4] = {0.f, 0.f, 0.f, 0.f}, cs[2] = {0.f, 0.f};
#pragma unroll
      for (int d = 0; d < 4; d++) {
        st[0] = dot2(ones, xa[d], st[0]);
        st[1] = dot2(xa[d], xa[d], st[1]);
        st[2] = dot2(ones, xb[d], st[2]);
        st[3] = dot2(xb[d], xb[d], st[3]);
      }
#pragma unroll
      for (int d = 0; d < 8; d++) {
        cs[0] = dot2(ones, sv[d >> 1][d & 1], cs[0]);
        cs[1] = dot2(ones, sv[4 + (d >> 1)][d & 1], cs[1]);
      }
      swap32(st[0], st[1]);
      swap32(st[2], st[3]);
      swap32(cs[0], cs[1]);
      *(LAS float*)(L_ST + (m * 64 + lane) * 4) = cs[0] + cs[1];
      st[0] += st[1];
      OPAQUE(st[0]);
      st[2] += st[3];
      auto sw16 = __builtin_amdgcn_permlane16_swap(__float_as_uint(st[0]), __float_as_uint(st[2]), false, false);
      sa = __uint_as_float(sw16[0]) + __uint_as_float(sw16[1]);
      OPAQUE(sa);
    }
    if constexpr (n == SL::bar(0) - 1) {
      sfor<0, 4>([&](auto KK) { sa += __int_as_float(__builtin_amdgcn_update_dpp(0, __float_as_int(sa), 0x120 + (8 >> decltype(KK)::v), 0xf, 0xf, false)); });
      float mean = sa * (1.f / 512.f), ms = mean;
      swap32(mean, ms);
      ms = __builtin_amdgcn_rsqf(mx(__builtin_fmaf(-mean, mean, ms), 0.f) + 1e-5f);
      const u32 mh = pk(-mean, -mean);
      const float ml = -mean - __uint_as_float(mh & 0xffff0000u);
      *(LAS u32x2*)(L_ST + ((lane & 47) == 0 ? 1024 + m * 16 + blk * 8 : 2048 + lane * 8)) = u32x2{(mh & 0xffffu) | (pk(ml, ml) & 0xffff0000u), __float_as_uint(ms)};
    }
    if constexpr (n >= SL::N7 + 5) {
      constexpr int g = (n - SL::N7) % 5, M = SL::blk(n - 5);
      if constexpr (g >= 1) {
        constexpr int s = (g - 1) >> 1, h = (g - 1) & 1;
        if constexpr (FFN || M < 4) {
          eo[2 * h] = pk2(pr<8 * s + 4 * h>(acc[M]));
          eo[2 * h + 1] = pk2(pr<8 * s + 4 * h + 2>(acc[M]));
          if constexpr (FFN) {
            eo[2 * h] = relu2(eo[2 * h]);
            eo[2 * h + 1] = relu2(eo[2 * h + 1]);
          }
          if constexpr (h == 0) OPAQUE(eo);
        }
        if constexpr (h == 1) {
          if constexpr (FFN) st128(eo, c.ws, lane * 16, obase + ((t.c0 >> 5) + 2 * M + s) * 1024);
          else if constexpr (M == 1) {
            *(LAS u32x4*)(((m >> 1) ? L_SP2 : L_SP1) + (m & 1) * 2048 + s * 1024 + lane * 16) = eo;
            if constexpr (s == 1) sem_post(SEM_K + m);
          } else if constexpr (M == 0) {
            const u32x4 wp = wphi_frag(L_WPHI + s * 512 + (4 * hh + q) * 32 + p * 8, blk ? 0x80008000u : 0u);
            asm volatile("s_nop 1\n\tv_mfma_f32_32x32x16_bf16 %0, %1, %2, 0" : "=&v"(s ? P1 : P0) : "v"(wp), "v"(eo));
          } else if constexpr (M < 4) st128(eo, c.ws, lane * 16, obase + (2 * (M - 2) + s) * 1024);
        }
      }
    }
  };
  sfor<0, SL::NS>([&](auto NN) {
    constexpr int n = decltype(NN)::v, T = SL::step(n), M = SL::blk(n);
    constexpr bool TR = !FFN && M >= 4;
    const u32x4 xo = T < 0 ? fm : xf[T & 3], wo_ = wf[n % (2 * NBK)];
    if constexpr (T == 0) mma0(acc[M], TR ? xo : wo_, TR ? wo_ : xo);
    else mma(acc[M], TR ? xo : wo_, TR ? wo_ : xo);
    constexpr int G = n < SL::N7 ? n / (4 * NBK) : 7;
    if constexpr (n == SL::bar(G)) {
      WAITL0();
      STAMP(c, 3 + 2 * G);
      BAR();
      STAMP(c, 4 + 2 * G);
    }
    if constexpr (n + NBK < SL::NS && n != SL::bar(G) - 1) {
      if constexpr (n < SL::N7 && (n == SL::bar(G) || (M == 0 && (T & 3) != 3))) {
        if constexpr (T == 27) {
          fm = lds128(hh ? L_ZERO : L_ST + 4096 + (m * 32 + r) * 16);
          xf[0] = lds128(ax[0] + 16384); xf[1] = lds128(ax[1] + 16384); xf[2] = lds128(ax[2] + 16384);
        } else xf[(T + 1) & 3] = lds128(ax[(T + 1) & 3] + (((T + 1) >> 2) % 3) * 16384);
      }
      if constexpr (n == SL::bar(G)) wf[(n - 1 + NBK) % (2 * NBK)] = rdw(IC<n - 1 + NBK>{});
      wf[(n + NBK) % (2 * NBK)] = rdw(IC<n + NBK>{});
      if constexpr (n == SL::N7) {
        xf[3] = lds128(ax[3] + 16384);
        if constexpr (!FFN) rstd = *(LAS float*)(L_RS + m * 128 + r * 4);
      }
    }
    filler(NN);
    SB();
  });
  __builtin_amdgcn_s_setprio(3);
  if constexpr (FFN) {
    asm volatile("s_nop 15" : "+v"(acc[6]));
    sfor<0, 2>([&](auto SS) {
      u32x4 o = pack8<decltype(SS)::v>(acc[6]);
#pragma unroll
      for (int d = 0; d < 4; d++) o[d] = relu2(o[d]);
      st128(o, c.ws, lane * 16, obase + ((t.cc >> 5) + decltype(SS)::v) * 1024);
    });
  } else {
    const u32 apw = lx(1) + m * 2048 + r * 64 + ((hh ^ ((r >> 1) & 7)) << 3);
    phi_store(apw, P0, rstd * LOG2E);
    SB();
    WAITV(0);
    publish(c, 0xf00ull);
    const u32 av0 = L_ST + m * 2048 + lane * 16, av1 = lwb(1) + m * 2048 + lane * 16;
    sfor<0, 2>([&](auto TT) {
      constexpr int tt = decltype(TT)::v;
      const f32x4 ra = ldsf4(L_RS + m * 128 + (16 * tt + 4 * hh) * 4), rb = ldsf4(L_RS + m * 128 + (16 * tt + 8 + 4 * hh) * 4);
      const f32x4 a0 = q4<8 * tt>(acc[4]) * ra, b0 = q4<8 * tt + 4>(acc[4]) * rb, a1 = q4<8 * tt>(acc[5]) * ra, b1 = q4<8 * tt + 4>(acc[5]) * rb;
      *(LAS u32x4*)(av0 + tt * 1024) = u32x4{pk(a0[0], a0[1]), pk(a0[2], a0[3]), pk(b0[0], b0[1]), pk(b0[2], b0[3])};
      *(LAS u32x4*)(av1 + tt * 1024) = u32x4{pk(a1[0], a1[1]), pk(a1[2], a1[3]), pk(b1[0], b1[1]), pk(b1[2], b1[3])};
    });
    phi_store(apw + 8192, P1, rstd * LOG2E);
    WAITL0();
    STAMP(c, 19);
    BAR();
    const u32 t0 = 4 * hh + q;
    const u32 apr = ((m >> 1) ? lwa(1) : lx(1)) + (m & 1) * 8192 + t0 * 64 + (((4 * blk + p) ^ (t0 >> 1)) << 3);
    const u32 avr = ((m >> 1) ? lwb(1) : L_ST) + lane * 16;
    const bool isv = blk == (m & 1);
    u32x4 pa[8], vb[8];
#pragma unroll
    for (int k = 0; k < 8; k++) {
      pa[k] = trd2(apr + k * 1024, (apr ^ 32) + k * 1024 + 512);
      vb[k] = lds128(avr + k * 1024);
    }
    sem_post(SEM_KV);
    f32x16 X = f32x16{};
#pragma unroll
    for (int k = 0; k < 8; k++) {
#pragma unroll
      for (int d = 0; d < 4; d++) vb[k][d] = isv ? vb[k][d] : ONES2;
      X = mfma(pa[k], vb[k], X);
    }
    const u32 pi = (4 * j + m) * 16 + rt;
    const u32 ve = isv ? KV_OFF + pi * 2048 + (hh * 16 + (lane & 15)) * 16 : KZ_OFF + pi * 128 + hh * 16, vs = isv ? 512 : 32;
    st128(__builtin_bit_cast(u32x4, q4<0>(X)), c.ws, ve, 0);
    st128(__builtin_bit_cast(u32x4, q4<4>(X)), c.ws, ve + vs, 0);
    st128(__builtin_bit_cast(u32x4, q4<8>(X)), c.ws, ve + 2 * vs, 0);
    st128(__builtin_bit_cast(u32x4, q4<12>(X)), c.ws, ve + 3 * vs, 0);
  }
  STAMP(c, 20);
  __builtin_amdgcn_s_setprio(0);
  if constexpr (!FFN) {
    WAITV(0);
    publish(c, 0xffull);
    sem_wait(SEM_KV, 4);
  }
}
DI void compute2(Ctx& c, bool ffn, const u16* w2) {
  const u32 lane = c.lane, m = c.m;
  const int b = blockIdx.x, i = (b & 7) * 4 + (b >> 6);
  init_ab(c);
  BSrc b2;
  b2.init(c, w2, 1792, 448);
  auto issue = [&](auto NN) { b2.issue<decltype(NN)::v>(c); };
  u32x4 af[28][2], bf[28][2];
  auto ldb = [&](auto NN) {
    constexpr int N = decltype(NN)::v;
    bf[N][0] = rdb<N>(c, 0);
    bf[N][1] = rdb<N>(c, 1);
  };
  sfor<5 + NB7, 10>(issue);
  sfor<10, 10 + NB7 + 2>(issue);
  WAITV(14);
  publish(c, ffn ? 0xffull : 0ull);
  STAMP(c, 23);
  sem_wait(SEM_B + m, 1);
  sfor<0, 5>(ldb);
  sfor<10 + NB7 + 2, 16>(issue);
  WAITL0();
  sfor<16, 21>(issue);
  WAITV(22);
  sfor<5, 10>(ldb);
  const u32 fh = MB_OFF + b * 1024 + 32 * m;
  u64 ph = peek(c, fh, 4);
  WAITL0();
  sfor<21, 26>(issue);
  STAMP(c, 24);
  WAITV(10);
  await(c, fh, 4, ph);
  asm volatile("" : "+s"(c.rot));
  STAMP(c, 25);
  const u32 ah = HF_OFF + ((2 * i) * 112 + 28 * m) * 1024;
  const u32 va[2] = {lane * 16 + (m >> 1) * 114688, lane * 16 + ((m >> 1) ^ 1) * 114688};
  auto lda = [&](auto NN, int u) {
    constexpr int N = decltype(NN)::v;
    if constexpr (N < 28) af[N][u] = ld128(c.ws, va[u], ah + hstep<N>(c) * 1024);
  };
  sfor<0, CW>([&](auto NN) { lda(NN, 0); lda(NN, 1); });
  SB();
  STAMP(c, 27);
  f32x16 ac2[4];
  sfor<0, 24>([&](auto NN) {
    constexpr int n = decltype(NN)::v;
    sfor<0, 4>([&](auto KK) {
      constexpr int k = decltype(KK)::v;
      if constexpr (n == 0) mma0(ac2[k], bf[n][k & 1], af[n][k >> 1]);
      else mma(ac2[k], bf[n][k & 1], af[n][k >> 1]);
      SB();
      if constexpr ((k & 1) == 1 && n + CW < 28) lda(IC<n + CW>{}, k >> 1);
      if constexpr ((k & 1) == 0 && n >= 6) bf[n + 4][k >> 1] = rdb<n + 4>(c, k >> 1);
      if constexpr (k == 3 && n == 8) {
        sem_wait(SEM_B + m, 2);
        sfor<26, 28>(issue);
      }
      if constexpr (k == 3 && n == 11) sem_post(SEM_B + m);
      SB();
    });
  });
  STAMP(c, 31);
  sfor<0, 4>([&](auto KK) {
    constexpr int k = decltype(KK)::v;
    sfor<24, 28>([&](auto NN) {
      constexpr int n = decltype(NN)::v;
      mma(ac2[k], bf[n][k & 1], af[n][k >> 1]);
      if constexpr (n == 25 && k > 0) {
        SB();
        put_block<2 * k - 2>(c, ac2[k - 1]);
        SB();
      }
    });
  });
  asm volatile("s_nop 15" : "+v"(ac2[3]));
  put_block<6>(c, ac2[3]);
  sem_post(SEM_C);
  STAMP(c, 32);
}

#define ROTB 1
__global__ __launch_bounds__(512, 2) void fused_k(const u16* x, const u16* wqkv, const u16* wphi, const u16* wo, const u16* w1, const u16* w2, u16* out,
                                                  char* ws) {
  __shared__ __attribute__((aligned(16))) char smem[163840];
  asm volatile("" ::"v"((LAS char*)smem));
  const int b = blockIdx.x, xcd = b & 7, r = b >> 3;
  u64 pc0;
  asm volatile("s_getpc_b64 %0" : "=s"(pc0));
  Ctx c;
  c.dbg = 0;
  STAMP(c, 0);
  c.lane = threadIdx.x & 63;
  const u32 w = __builtin_amdgcn_readfirstlane(threadIdx.x >> 6);
  c.m = w & 3;
  c.ws = __builtin_amdgcn_make_buffer_rsrc(ws, 0, WS_BYTES, 0x00020000);
  c.wsp = ws;
  c.rot = ROTB == 0 ? 0 : ROTB == 1 ? (b >> 6) * 7 : ((b >> 6) * 7 + (((b >> 3) & 7) * 7 >> 1)) % 28;
  const int rt = (xcd >> 2) * 8 + (r >> 2), ct = (xcd & 3) * 4 + (r & 3), j = ct >> 1;
  Tile t;
  t.rt = rt;
  t.j = j;
  t.ct = ct;
  t.ffn = ct & 1;
  t.ldw = t.ffn ? 3584 : 3072;
  const u32 lane = c.lane, m = c.m;
  t.srd_x = make_srd(x, 2048 * 512 * 2);
  t.srd_w = make_srd(t.ffn ? w1 : wqkv, 512 * t.ldw);
  t.vx0 = (lane >> 3) * 1024 + (((lane & 7) ^ (lane >> 4)) << 4);
  {
    const u32 sx = (rt * 128 + m * 32 + (w >> 2) * 16) * 1024, dx = m * 4096 + (w >> 2) * 2048;
    dma<LX, 0>(t.srd_x, t.vx0, sx, dx);
    dma<LX + 1024, 8192>(t.srd_x, t.vx0 ^ 64, sx, dx);
    t.vw = (lane >> 3) * t.ldw + (((lane & 7) ^ (((lane >> 4) & 1) << 2)) << 4);
    t.c0 = t.ffn ? 448 * j + (j & 1) * 64 : 1024 + 128 * j;
    t.c1 = t.ffn ? t.c0 + 128 : 128 * j;
    t.c2 = t.ffn ? t.c0 + 256 : 2048 + 128 * j;
    t.cc = t.ffn ? 448 * j + ((j & 1) ? 0 : 384) : 0;
    dma<LWA, 0>(t.srd_w, t.vw, w * 8 * t.ldw + t.c0, w * 1024);
    dma<LWA + 8192, 0>(t.srd_w, t.vw, w * 8 * t.ldw + t.c1, w * 1024);
    dma<LWB, 0>(t.srd_w, t.vw, w * 8 * t.ldw + t.c2, w * 1024);
    if (t.ffn && w >= 4) dma<LWB + 8192, 0>(t.srd_w, (lane >> 2) * 3584 + ((lane & 3) << 4), m * 16 * 3584 + t.cc, m * 1024);
    BAR();
    SB();
  }
  if (__builtin_expect(w >= 4, 1)) {
    helper(c, t, x, wo, w2, out);
  } else {
    u32x4 xa, xb;
    {
      u32 off = ((r * 4 + m) * 3 + (lane & 3)) * 128;
      off = off < CODE_BYTES ? off : 0;
      asm volatile("s_mov_b32 m0, %2\n\ts_mov_b64 exec, 7\n\tglobal_load_lds_dword %0, %1\n\ts_mov_b64 exec, -1" ::"v"(off), "s"(pc0), "i"(L_ST + 4096) : "memory");
      const u32 vs = m < 3 ? t.vw : (lane >> 3) * t.ldw + ((lane & 3) << 4);
      const u32 so = rt * 32 * t.ldw + (m == 0 ? t.c0 : m == 1 ? t.c1 : m == 2 ? t.c2 : t.cc);
      dma<LBS, 0>(t.srd_w, vs, so, m * 4096);
      dma<LBS + 1024, 0>(t.srd_w, vs, so + 8 * t.ldw, m * 4096);
      dma<LBS + 2048, 0>(t.srd_w, vs, so + 16 * t.ldw, m * 4096);
      dma<LBS + 3072, 0>(t.srd_w, vs, so + 24 * t.ldw, m * 4096);
      const u32 xo = (rt * 128 + ct * 8 + m * 2) * 1024 + lane * 16;
      asm volatile("global_load_dwordx4 %0, %1, %2" : "=v"(xa) : "v"(xo), "s"(x) : "memory");
      asm volatile("global_load_dwordx4 %0, %1, %2 offset:1024" : "=v"(xb) : "v"(xo), "s"(x) : "memory");
      dma<L_WPHI, 0>(make_srd(wphi, 32 * 512), lane * 16, m < 2 ? (u32)(j * 2048 + m * 1024) : (u32)(((b & 7) * 2 + ((b >> 4) & 1)) * 1024), (m < 2 ? m : 2) * 1024);
      const u32 ii = 2 * rt + (m >> 1), l7 = lane & 7;
      const u32 t1 = t.ffn ? (ii & 3) * 64 + (ii >> 2) + l7 * 8 : l7 * 32 + m * 8 + j, f1 = t.ffn ? j * 2 + (m & 1) : 16 + rt;
      load_tag(c, lane < 8 ? t1 : lane < 12 ? (rt >> 1) * 32 + (lane & 3) * 8 + j : b, lane < 8 ? f1 : 96 + (rt & 1) * 4 + m);
      *(LAS u32*)(L_SEM + (lane & 31) * 4) = 0;
    }
    if (t.ffn) compute1<true>(c, t, xa, xb);
    else compute1<false>(c, t, xa, xb);
    compute2(c, t.ffn, w2);
  }
  STAMP(c, 34);
  if (b == 0) while (__builtin_amdgcn_s_memrealtime() < c.tp + 4000) __builtin_amdgcn_s_sleep(2);
}

extern "C" void kernel_launch(void* const* d_in, const int* in_sizes, int n_in, void* d_out, int out_size, void* d_ws, size_t ws_size,
                              hipStream_t stream) {
  const u16 *x = (const u16*)d_in[0], *wqkv = (const u16*)d_in[1], *wphi = (const u16*)d_in[2], *wo = (const u16*)d_in[3];
  const u16 *w1 = (const u16*)d_in[5], *w2 = (const u16*)d_in[6];
  fused_k<<<256, 512, 0, stream>>>(x, wqkv, wphi, wo, w1, w2, (u16*)d_out, (char*)d_ws);
}
```

```cpp
#include <hip/hip_runtime.h>
#pragma clang diagnostic ignored "-Wint-to-pointer-cast"
typedef unsigned short u16;
typedef unsigned u32;
typedef unsigned long long u64;
typedef u32 u32x2 __attribute__((ext_vector_type(2)));
typedef u32 u32x4 __attribute__((ext_vector_type(4)));
typedef short s16x2 __attribute__((ext_vector_type(2)));
typedef short s16x4 __attribute__((ext_vector_type(4)));
typedef float f32x2 __attribute__((ext_vector_type(2)));
typedef float f32x4 __attribute__((ext_vector_type(4)));
typedef float f32x16 __attribute__((ext_vector_type(16)));
typedef __bf16 b16x2 __attribute__((ext_vector_type(2)));
typedef __bf16 b16x8 __attribute__((ext_vector_type(8)));
#define LAS __attribute__((address_space(3)))
#define GAS __attribute__((address_space(1)))
#define DI __device__ __forceinline__
#define SB() __builtin_amdgcn_sched_barrier(0)
#define BAR() __builtin_amdgcn_s_barrier()
#define WAITV(n) do { asm volatile("s_waitcnt vmcnt(%0)" ::"i"(n) : "memory"); SB(); } while (0)
#define WAITL0() asm volatile("s_waitcnt lgkmcnt(0)" ::: "memory")
#define OPAQUE(x) asm volatile("" : "+v"(x))

constexpr u32 HF_OFF = 0;
constexpr u32 QF_OFF = 64u * 112u * 1024u;
constexpr u32 OF_OFF = QF_OFF + 64u * 32u * 1024u;
constexpr u32 KV_OFF = OF_OFF + 64u * 32u * 1024u;
constexpr u32 KZ_OFF = KV_OFF + 32u * 16u * 2048u;
constexpr u32 CS_OFF = KZ_OFF + 32u * 16u * 128u;
constexpr u32 RS_OFF = CS_OFF + 256u * 1024u;
constexpr u32 MB_OFF = RS_OFF + 2048u * 8u;
constexpr u32 EP_OFF = MB_OFF + 256u * 1024u;
constexpr u32 WS_BYTES = EP_OFF + 2048u;
constexpr u32 LX = 0, LWA = 49152, LWB = 98304, LBS = 135168, LSCR = 159744;
constexpr u32 lx(int s) { return LX + s * 16384; }
constexpr u32 lwa(int s) { return LWA + s * 16384; }
constexpr u32 lwb(int s) { return LWB + s * 12288; }
constexpr u32 L_WPHI = LSCR, L_WPA = LSCR + 2048, L_RS = LSCR + 3072, L_SEM = LSCR + 3584, L_ZERO = LSCR + 3648;
constexpr u32 L_SP1 = lwb(1) + 8192, L_SP2 = lwb(2) + 8192;
constexpr u32 L_ST = LBS + 16384;
constexpr int AUX = 16;

DI f32x16 mfma(u32x4 a, u32x4 b, f32x16 c) {
  return __builtin_amdgcn_mfma_f32_32x32x16_bf16(__builtin_bit_cast(b16x8, a), __builtin_bit_cast(b16x8, b), c, 0, 0, 0);
}
DI void mma(f32x16& d, u32x4 a, u32x4 b) { asm volatile("v_mfma_f32_32x32x16_bf16 %0, %1, %2, %0" : "+v"(d) : "v"(a), "v"(b)); }
DI void mma0(f32x16& d, u32x4 a, u32x4 b) { asm volatile("v_mfma_f32_32x32x16_bf16 %0, %1, %2, 0" : "=&v"(d) : "v"(a), "v"(b)); }
DI void mma8(f32x16& d, u32x2 a, u32x2 b) { asm volatile("v_mfma_f32_32x32x8_bf16 %0, %1, %2, %0" : "+v"(d) : "v"(a), "v"(b)); }
DI u32x2 trd(u32 addr) { return __builtin_bit_cast(u32x2, __builtin_amdgcn_ds_read_tr16_b64_v4i16((LAS s16x4*)addr)); }
DI u32x4 trd2(u32 a0, u32 a1) {
  u32x2 lo = trd(a0), hi = trd(a1);
  return __builtin_shufflevector(lo, hi, 0, 1, 2, 3);
}
DI u32x4 lds128(u32 addr) { return *(LAS u32x4*)addr; }
DI f32x4 ldsf4(u32 addr) { return *(LAS f32x4*)addr; }
DI u32 pk(float a, float b) {
  f32x2 v = {a, b};
  return __builtin_bit_cast(u32, __builtin_convertvector(v, b16x2));
}
DI u32 pk2(f32x2 v) { return __builtin_bit_cast(u32, __builtin_convertvector(v, b16x2)); }
DI u32 relu2(u32 v) { return __builtin_bit_cast(u32, __builtin_elementwise_max(__builtin_bit_cast(s16x2, v), s16x2{0, 0})); }
extern "C" __device__ float fmul_legacy_(float, float) __asm("llvm.amdgcn.fmul.legacy");
DI float mul1(float a, float b) { return fmul_legacy_(a, b); }
DI float mx(float a, float b) { return __builtin_elementwise_maximum(a, b); }
DI float dot2(u32 a, u32 b, float c) {
  return __builtin_amdgcn_fdot2_f32_bf16(__builtin_bit_cast(b16x2, a), __builtin_bit_cast(b16x2, b), c, false);
}
DI void swap32(float& a, float& b) {
  auto r = __builtin_amdgcn_permlane32_swap(__float_as_uint(a), __float_as_uint(b), false, false);
  a = __uint_as_float(r[0]);
  b = __uint_as_float(r[1]);
}
template <int I> DI f32x2 pr(const f32x16& a) { return __builtin_shufflevector(a, a, I, I + 1); }
template <int I> DI f32x4 q4(const f32x16& a) { return __builtin_shufflevector(a, a, I, I + 1, I + 2, I + 3); }
DI u32x4 make_srd(const void* p, u32 bytes) {
  u64 a = (u64)p;
  u32x4 r = {(u32)a, (u32)(a >> 32) & 0xffffu, bytes, 0x00020000u};
  return r;
}
template <u32 LI, u32 SI>
DI void dma(u32x4 srd, u32 voff, u32 sbase, u32 lbase) {
  u32 t;
  asm volatile("s_add_u32 m0, %4, %6\n\ts_add_u32 %0, %3, %5\n\tbuffer_load_dwordx4 %1, %2, %0 offen lds"
               : "=&s"(t)
               : "v"(voff), "s"(srd), "s"(sbase), "s"(lbase), "i"(SI), "i"(LI)
               : "memory", "scc");
}
DI void st128(u32x4 v, __amdgpu_buffer_rsrc_t rs, u32 voff, u32 soff) {
  __builtin_amdgcn_raw_buffer_store_b128(v, rs, voff, soff, AUX);
  asm volatile("s_nop 1" ::"v"(v) : "memory");
}
#define LDAUX AUX
DI u32x4 ld128(__amdgpu_buffer_rsrc_t rs, u32 voff, u32 soff) { return __builtin_amdgcn_raw_buffer_load_b128(rs, voff, soff, LDAUX); }
template <int OFF> DI void gld64(u64& d, u32 voff, const char* base) {
  asm volatile("global_load_dwordx2 %0, %1, %2 offset:%3 sc1" : "=v"(d) : "v"(voff), "s"(base), "i"(OFF) : "memory");
}
template <int OFF> DI void gld128(f32x4& d, u32 voff, const char* base) {
  asm volatile("global_load_dwordx4 %0, %1, %2 offset:%3 sc1" : "=v"(d) : "v"(voff), "s"(base), "i"(OFF) : "memory");
}
template <int OFF> DI void warm1(u32& d, const void* base) { asm volatile("s_load_dword %0, %1, %2" : "+s"(d) : "s"(base), "i"(OFF)); }
template <int N> struct IC { static constexpr int v = N; };
template <int B, int E, class F> DI void sfor(F&& f) {
  if constexpr (B < E) {
    f(IC<B>{});
    sfor<B + 1, E>(f);
  }
}
constexpr u32 ONES2 = 0x3f803f80u;
constexpr float LOG2E = 1.4426950408889634f;

DI void dual_softmax(const f32x16& P, float sl, u32x4& o0, u32x4& o1) {
  float m0 = mx(mx(mx(mx(mx(mx(mx(P[0], P[1]), P[2]), P[3]), P[4]), P[5]), P[6]), P[7]);
  float m1 = mx(mx(mx(mx(mx(mx(mx(P[8], P[9]), P[10]), P[11]), P[12]), P[13]), P[14]), P[15]);
  swap32(m0, m1);
  m0 = mx(m0, m1) * -sl;
  m1 = m0;
  swap32(m0, m1);
  f32x2 e[8];
  sfor<0, 8>([&](auto II) {
    constexpr int I = decltype(II)::v;
    const float m = I < 4 ? m0 : m1;
    e[I] = __builtin_elementwise_fma(pr<2 * I>(P), f32x2{sl, sl}, f32x2{m, m});
  });
#pragma unroll
  for (int i = 0; i < 8; i++) e[i] = f32x2{__builtin_amdgcn_exp2f(e[i][0]), __builtin_amdgcn_exp2f(e[i][1])};
  const f32x2 s0 = (e[0] + e[1]) + (e[2] + e[3]), s1 = (e[4] + e[5]) + (e[6] + e[7]);
  m0 = s0[0] + s0[1];
  OPAQUE(m0);
  m1 = s1[0] + s1[1];
  swap32(m0, m1);
  m0 = __builtin_amdgcn_rcpf(m0 + m1);
  m1 = m0;
  swap32(m0, m1);
#pragma unroll
  for (int i = 0; i < 4; i++) {
    o0[i] = pk2(e[i] * m0);
    o1[i] = pk2(e[4 + i] * m1);
  }
}
DI u32x4 wphi_frag(u32 a, u32 sg) {
  u32x4 f = trd2(a, a + 256);
  f[0] ^= sg; f[1] ^= sg; f[2] ^= sg; f[3] ^= sg;
  return f;
}
DI void phi_store(u32 apw, const f32x16& P, float sl) {
  u32x4 o0, o1;
  dual_softmax(P, sl, o0, o1);
  *(LAS u32x2*)(apw) = u32x2{o0[0], o0[1]};
  *(LAS u32x2*)(apw ^ 16) = u32x2{o0[2], o0[3]};
  *(LAS u32x2*)(apw ^ 32) = u32x2{o1[0], o1[1]};
  *(LAS u32x2*)(apw ^ 48) = u32x2{o1[2], o1[3]};
}
template <int S> DI u32x4 pack8(const f32x16& a) { return u32x4{pk2(pr<8 * S>(a)), pk2(pr<8 * S + 2>(a)), pk2(pr<8 * S + 4>(a)), pk2(pr<8 * S + 6>(a))}; }

constexpr u32 slot_loc(int s, int k) { return k < 2 ? lx(s) + k * 8192 : k < 4 ? lwa(s) + (k - 2) * 8192 : lwb(s); }
constexpr u32 bloc(int n) {
  return n < 5 ? slot_loc(2, n) : n < 10 ? slot_loc(0, n - 5) : n < 15 ? slot_loc(1, n - 10) : n == 15 ? LBS + 16384
       : n < 26 ? bloc(n - 16) : LBS + (n & 1) * 8192;
}

#define STAMP(c, i) do {} while (0)
#define HSTAMP(c, i) do {} while (0)
struct Ctx {
  mutable u32 dbg;
  __amdgpu_buffer_rsrc_t ws;
  char* wsp;
  u32 m, lane;
  u32 ab[3][2];
  u32 rot;
  u64 tag;
  u64 own;
  u32 fl;
};
template <int N> DI u32x4 rdb(const Ctx& c, int cb) {
  constexpr u32 W = bloc(N) / 57344;
  const u32 a = c.ab[W][cb] + (bloc(N) - W * 57344);
  return trd2(a, a + 1024);
}
DI void init_ab(Ctx& c) {
  const u32 lane = c.lane, m = c.m, hh = lane >> 5, blk = (lane >> 4) & 1, q = (lane & 15) >> 2, p = lane & 3;
#pragma unroll
  for (int cb = 0; cb < 2; cb++) {
    const u32 t = m * 2048 + (4 * hh + q) * 128 + (((4 * (cb ^ (m & 1)) + 2 * blk + (p >> 1)) ^ ((q >> 1) << 2)) << 4) + 8 * (p & 1);
#pragma unroll
    for (int k = 0; k < 3; k++) {
      c.ab[k][cb] = t + k * 57344;
      OPAQUE(c.ab[k][cb]);
    }
  }
}
DI void load_tag(Ctx& c, u32 tgt, u32 slot) {
  c.fl = MB_OFF + tgt * 1024 + slot * 8;
  asm volatile("global_load_dwordx2 %0, %1, %2 sc1" : "=v"(c.tag) : "v"(EP_OFF + tgt * 8), "s"(c.wsp) : "memory");
}
DI void set_tag(Ctx& c) {
  OPAQUE(c.tag);
  c.tag += 1;
  c.own = ((u64)(u32)__builtin_amdgcn_readlane((u32)(c.tag >> 32), 63) << 32) | (u32)__builtin_amdgcn_readlane((u32)c.tag, 63);
}
DI void publish(const Ctx& c, u64 mask) {
  asm volatile("s_mov_b64 exec, %3\n\tglobal_atomic_swap_x2 %0, %1, %2 sc1\n\ts_mov_b64 exec, -1" ::"v"(c.fl), "v"(c.tag), "s"(c.wsp), "s"(mask) : "memory");
}
DI u64 peek(const Ctx& c, u32 off, int n) {
  u64 v = c.own;
  const u64 z = 0;
  asm volatile("s_mov_b64 exec, %4\n\tglobal_atomic_or_x2 %0, %1, %2, %3 sc0 sc1\n\ts_mov_b64 exec, -1"
               : "+v"(v)
               : "v"(off + c.lane * 8), "v"(z), "s"(c.wsp), "s"((1ull << n) - 1)
               : "memory");
  return v;
}
DI void await(const Ctx& c, u32 off, int n, u64 v) {
  OPAQUE(v);
#pragma unroll 1
  for (int it = 0; it < (1 << 16) && !__all(v == c.own); it++) {
    __builtin_amdgcn_s_sleep(2);
    v = peek(c, off, n);
    asm volatile("s_waitcnt vmcnt(0)" : "+v"(v)::"memory");
  }
}
DI void sem_post(u32 idx) {
  asm volatile("s_mov_b64 exec, 1\n\tds_add_u32 %0, %1\n\ts_mov_b64 exec, -1" ::"v"(L_SEM + idx * 4), "v"(1u) : "memory");
}
DI void sem_wait(u32 idx, u32 n) {
#pragma unroll 1
  for (int it = 0; it < (1 << 20); it++) {
    if (__builtin_amdgcn_readfirstlane(*(volatile LAS u32*)(L_SEM + idx * 4)) >= n) break;
    __builtin_amdgcn_s_sleep(1);
  }
  asm volatile("" ::: "memory");
}
constexpr u32 SEM_B = 0, SEM_K = 8, SEM_A = 12, SEM_KV = 13, SEM_C = 14, SEM_H = 15;
template <int N> DI u32 hstep(const Ctx& c) {
  const u32 h = c.rot + N;
  return h >= 28 ? h - 28 : h;
}
struct BSrc {
  u32x4 srd;
  u32 vb, sb, d2;
  DI void init(const Ctx& c, const u16* w, u32 rows, u32 rows_per_q) {
    srd = make_srd(w, rows * 1024);
    vb = (c.lane >> 3) * 1024 + (((c.lane & 7) ^ (((c.lane >> 4) & 1) << 2)) << 4);
    sb = c.m * rows_per_q * 1024 + ((blockIdx.x >> 3) & 7) * 128;
    d2 = c.m * 2048;
  }
  template <int N> DI void issue(const Ctx& c) const {
    const u32 so = sb + (N < 28 ? hstep<N>(c) : N - 28) * 16384;
    dma<bloc(N), 0>(srd, vb, so, d2);
    dma<bloc(N) + 1024, 8192>(srd, vb, so, d2);
  }
};

struct Tile {
  int rt, j, ct;
  bool ffn;
  u32 ldw;
  u32 c0, c1, c2, cc;
  u32x4 srd_x, srd_w;
  u32 vx0, vw;
};
DI u32 blk_of(u32 rt, u32 ct) { return (((rt & 7) * 4 + (ct & 3)) << 3) + (rt >> 3) * 4 + (ct >> 2); }

template <int N> DI void put_block(const Ctx& c, const f32x16& a) {
  const u32 p = c.m * 2048 + c.lane * 16 + bloc(N);
  *(LAS u32x4*)p = pack8<0>(a);
  *(LAS u32x4*)(p + 1024) = pack8<1>(a);
}
template <int N> DI void get_block(const Ctx& c, u32 src, u32x4 (&f)[2]) {
  const u32 p = src * 2048 + c.lane * 16 + bloc(N);
  f[0] = lds128(p);
  f[1] = lds128(p + 1024);
}

#define PFCODE 34816
#define TOUCH 0
#define WARM 1
#define WN 1
#define WD 1
#define TLBW 0
#define SG 0
#define WOFF 14
#define CW 8
#define NB7 4
constexpr int NCP = 8;
constexpr u32 CODE_BYTES = PFCODE;
DI void helper(Ctx& c, const Tile& t, const u16* x, const u16* wo, const u16* w2, u16* out) {
  constexpr int ND = 10;
  const u32 lane = c.lane, m = c.m;
  const int b = blockIdx.x, i = (b & 7) * 4 + (b >> 6);
  const int hd = (b & 7) * 4 + ((b >> 3) & 3), ga = b >> 5;
  const u32 vx1 = t.vx0 ^ 64, vw1 = t.vw + 8 * t.ldw;
  const u32 vc = (lane >> 2) * 3584 + ((lane & 3) << 4);
  const u32 sx = (t.rt * 128 + m * 32) * 1024, sc = m * 16 * 3584 + t.cc;
  const u32 d4 = m * 4096, d2 = m * 2048, d1 = m * 1024;
  u32 wd = 0;
  const char *pb = (const char*)w2 + (b * 4 + m) * 1792, *pc = (const char*)wo + (b * 4 + m) * 512;
  const char* pa = c.wsp + HF_OFF + ((t.rt * 4 + m) * 112 + t.j * 14 + (t.ffn ? 7 : 0)) * 1024;
  const char* pq_ = c.wsp + QF_OFF + ((t.rt * 4 + m) * 32 + 4 * t.j + (t.ffn ? 2 : 0)) * 1024;
  const char* pd = c.wsp + OF_OFF + ((8 * ga + 2 * m) * 32 + hd) * 1024;
  const char* pf = c.wsp + KV_OFF + ((4 * t.j + m) * 16 + t.rt) * 2048 + (t.ffn ? 1024 : 0);
  auto warm = [&](auto KK) {
    constexpr int k = decltype(KK)::v - WOFF;
    if constexpr (!WARM || k < 0) {
    } else if constexpr (k < 28) warm1<k * 64>(wd, pb);
    else if constexpr (k < 36) warm1<(k - 28) * 64>(wd, pc);
    else if constexpr (WARM >= 2 && k < 52) warm1<(k - 36) * 128>(wd, pq_);
    else if constexpr (WARM >= 2 && k < 60) warm1<(k - 52) * 128>(wd, pf);
    else if constexpr (WARM >= 2 && k < 76) warm1<((k - 60) >> 3) * 32768 + ((k - 60) & 7) * 128>(wd, pd);
    else if constexpr (WARM >= 3 && k < 132) warm1<(k - 76) * 128>(wd, pa);
  };
  auto ringf = [&](auto FF) {
    constexpr int G = decltype(FF)::v / ND, S = G % 3, I = decltype(FF)::v % ND;
    const u32 sg = (m * 16 + G * 64) * t.ldw;
    if constexpr (I == 4) {
      if (t.ffn) dma<lwb(S) + 8192, G * 64 * 3584>(t.srd_w, vc, sc, d1);
    }
    if constexpr (I < 4) dma<lx(S) + I * 1024, I * 8192 + G * 128>(t.srd_x, (I & 1) ? vx1 : t.vx0, sx, d4);
    else if constexpr (I < 6) dma<lwa(S) + (I & 1) * 1024, 0>(t.srd_w, (I & 1) ? vw1 : t.vw, sg + t.c0, d2);
    else if constexpr (I < 8) dma<lwa(S) + 8192 + (I & 1) * 1024, 0>(t.srd_w, (I & 1) ? vw1 : t.vw, sg + t.c1, d2);
    else dma<lwb(S) + (I & 1) * 1024, 0>(t.srd_w, (I & 1) ? vw1 : t.vw, sg + t.c2, d2);
  };
  constexpr auto rend = [](int G) { return G < 0 ? ND + 3 : G == 0 ? 2 * ND + 5 : G == 1 ? 3 * ND + 8 : G < 6 ? (G + 3) * ND : 8 * ND; };
  sfor<ND, rend(-1)>(ringf);
  load_tag(c, lane < 16 ? blk_of(t.rt, lane) : lane < 32 ? blk_of(lane & 15, t.ct) : lane < 40 ? m * 64 + (lane & 7) * 8 + ga : b,
           lane < 16 ? 64 + t.ct : lane < 32 ? 80 + t.rt : 32 + hd);
  BSrc b2, bo;
  b2.init(c, w2, 1792, 448);
  bo.init(c, wo, 512, 128);
  const u32 hh = lane >> 5, blk = (lane >> 4) & 1, q = (lane & 15) >> 2, p = lane & 3, r = lane & 31;
  init_ab(c);
  u32x4 bf[8][2];
  auto ldb = [&](auto NN) {
    constexpr int N = decltype(NN)::v;
    bf[N - 28][0] = rdb<N>(c, 0);
    bf[N - 28][1] = rdb<N>(c, 1);
  };
  u64 ps = 0, grs = 0;
  f32x4 gc[4];
  __builtin_amdgcn_s_setprio(1);
  HSTAMP(c, 1);
  WAITV(4);
  BAR();
  HSTAMP(c, 2);
  constexpr auto wo_in = [](int G) { return G == 3 ? 28 : G == 4 ? 29 : G == 6 ? 30 : G == 7 ? 31 : -1; };
  float sv[4], scs;
  u32 a_cf = L_ST + (m * 64 + (lane & 15) * 4 + (lane >> 4)) * 8, a_fm = L_ST + 4096 + (m * 32 + r) * 8 + hh * 4, a_lr = L_RS + m * 128 + r * 4;
  u32 a_rs = RS_OFF + (t.rt * 128 + m * 32 + r) * 8, a_cs = CS_OFF + t.ct * 16384 + (lane >> 4) * 4096 + m * 256 + (lane & 15) * 16;
  OPAQUE(a_cf); OPAQUE(a_fm); OPAQUE(a_lr); OPAQUE(a_rs); OPAQUE(a_cs);
  sfor<0, 8>([&](auto GG) {
    constexpr int G = decltype(GG)::v;
    constexpr int NRB = G == 6 ? 10 : G == 7 ? 2 * NB7 : 0;
    if constexpr (G == 5 || G == 6) ldb(IC<23 + G>{});
    if constexpr (G == SG + 1) {
      const u32 cv = *(LAS u32*)(L_ST + (m * 64 + lane) * 4), rv = *(LAS u32*)(L_ST + 1024 + (lane & 15) * 4);
      __builtin_amdgcn_raw_buffer_store_b32(cv, c.ws, lane * 4, CS_OFF + (t.ct * 16 + t.rt) * 1024 + m * 256, AUX);
      asm volatile("s_mov_b64 exec, %3\n\tglobal_store_dword %0, %1, %2 sc1\n\ts_mov_b64 exec, -1" ::"v"(RS_OFF + (t.rt * 128 + t.ct * 8) * 8 + (lane & 15) * 4),
                   "v"(rv), "s"(c.wsp), "s"(m == 0 ? 0xffffull : 0ull)
                   : "memory");
      set_tag(c);
    }
    if constexpr (G == SG + 2) publish(c, 0xffull << (8 * m));
    auto gather = [&]() {
      await(c, MB_OFF + b * 1024 + 512, 32, ps);
      gld64<0>(grs, a_rs, c.wsp);
      gld128<0>(gc[0], a_cs, c.wsp); gld128<1024>(gc[1], a_cs, c.wsp); gld128<2048>(gc[2], a_cs, c.wsp); gld128<3072>(gc[3], a_cs, c.wsp);
    };
    if constexpr (SG == 0 && G == 4) ps = peek(c, MB_OFF + b * 1024 + 512, 32);
    if constexpr (SG == 0 && G == 5) gather();
    sfor<rend(G - 1), rend(G)>([&](auto FF) {
      ringf(FF);
      constexpr int u = decltype(FF)::v - rend(-1);
      sfor<u * WN / WD, (u + 1) * WN / WD>(warm);
    });
    if constexpr (SG == 1 && G == 5) {
      WAITV(ND);
      gather();
    }
    if constexpr (G >= 6)
      sfor<(G - 6) * 5, (G - 6) * 5 + NRB / 2>([&](auto NN) {
        constexpr int N = decltype(NN)::v - SG;
        b2.issue<N + SG>(c);
        SB();
        if constexpr (N == 0) {
          if constexpr (SG == 1) WAITV(4);
#pragma unroll
          for (int k = 0; k < 4; k++) OPAQUE(gc[k]);
#pragma unroll
          for (int k = 0; k < 4; k++) {
            sv[k] = gc[0][k];
#pragma unroll
            for (int u = 1; u < 4; u++) {
              sv[k] += gc[u][k];
              OPAQUE(sv[k]);
            }
          }
        } else if constexpr (N == 1) {
          swap32(sv[0], sv[2]);
          swap32(sv[1], sv[3]);
          sv[0] += sv[2];
          OPAQUE(sv[0]);
          sv[1] += sv[3];
          auto sw16 = __builtin_amdgcn_permlane16_swap(__float_as_uint(sv[0]), __float_as_uint(sv[1]), false, false);
          scs = __uint_as_float(sw16[0]) + __uint_as_float(sw16[1]);
          OPAQUE(scs);
        } else if constexpr (N == 2) {
          const u32 h2 = pk(scs, scs);
          const float lo = scs - __uint_as_float(h2 & 0xffff0000u);
          *(LAS u32x2*)a_cf = u32x2{h2, pk(lo, lo)};
        } else if constexpr (N == 3) {
          OPAQUE(grs);
          *(LAS u32*)a_fm = (u32)grs;
          *(LAS u32*)a_lr = (u32)(grs >> 32);
        }
      });
    if constexpr (G >= 6) WAITL0();
    if constexpr (wo_in(G) > 0) bo.issue<wo_in(G)>(c);
    if constexpr (SG == 1 && G == 4) ps = peek(c, MB_OFF + b * 1024 + 512, 32);
    HSTAMP(c, 3 + 2 * G);
    if constexpr (G < 7) WAITV(G < 3 ? rend(G) - (G + 2) * ND : rend(G) - rend(G - 1) + NRB + (wo_in(G) > 0 ? 2 : 0) + (SG == 1 && G == 4 ? 1 : SG == 1 && G == 5 ? 5 : 0));
    if (G == 7 && !t.ffn) {
      const u32 awp = L_WPHI + 1024 + (4 * hh + q) * 32 + p * 8, sg = blk ? 0x80008000u : 0u;
      const u32x4 w0 = wphi_frag(awp, sg), w1 = wphi_frag(awp + 512, sg);
      const float sl = __uint_as_float((u32)(grs >> 32)) * LOG2E;
      sem_wait(SEM_K + m, 1);
      const u32 ak = ((m >> 1) ? L_SP2 : L_SP1) + (m & 1) * 2048 + lane * 16;
      const f32x16 P0 = mfma(w0, lds128(ak), f32x16{}), P1 = mfma(w1, lds128(ak + 1024), f32x16{});
      BAR();
      const u32 apw = lwa(1) + m * 2048 + r * 64 + ((hh ^ ((r >> 1) & 7)) << 3);
      phi_store(apw, P0, sl);
      phi_store(apw + 8192, P1, sl);
      WAITL0();
    }
    BAR();
    HSTAMP(c, 4 + 2 * G);
  });
  __builtin_amdgcn_s_setprio(2);
  const u32 mb = MB_OFF + b * 1024;
  const u32 fk = mb + 128, fo = mb + 256 + 64 * m, fq = mb + 768 + 16 * m;
  u64 pq = peek(c, fq, 2);
  WAITV(1);
  ldb(IC<30>{});
  ldb(IC<31>{});
  sem_post(SEM_B + m);
  WAITL0();
  asm volatile("" : "+s"(wd));
  bo.issue<32>(c);
  bo.issue<33>(c);
  WAITV(4);
  STAMP(c, 20);
  await(c, fq, 2, pq);
  STAMP(c, 24);
  u32x4 p00, p01, p10, p11;
  u64 pkv;
  float rs0, rs1;
  {
    u32x4 qf[2];
#pragma unroll
    for (int u = 0; u < 2; u++) qf[u] = ld128(c.ws, lane * 16, QF_OFF + ((8 * ga + 2 * m + u) * 32 + hd) * 1024);
    rs0 = __uint_as_float(__builtin_amdgcn_raw_buffer_load_b32(c.ws, lane * 8 + 4, RS_OFF + (8 * ga + 2 * m) * 256, AUX));
    OPAQUE(rs0);
    SB();
    ldb(IC<32>{});
    ldb(IC<33>{});
    WAITL0();
    bo.issue<34>(c);
    bo.issue<35>(c);
    rs1 = rs0;
    swap32(rs0, rs1);
    const u32x4 wp = wphi_frag(L_WPA + (hd & 1) * 512 + (4 * hh + q) * 32 + p * 8, blk ? 0x80008000u : 0u);
    const f32x16 Q0 = mfma(wp, qf[0], f32x16{}), Q1 = mfma(wp, qf[1], f32x16{});
    dual_softmax(Q0, rs0 * LOG2E, p00, p01);
    OPAQUE(p00); OPAQUE(p01);
    SB();
    pkv = peek(c, fk, 16);
    dual_softmax(Q1, rs1 * LOG2E, p10, p11);
    OPAQUE(p10); OPAQUE(p11);
  }
  STAMP(c, 21);
  WAITV(0);
  ldb(IC<34>{});
  ldb(IC<35>{});
  sem_post(SEM_B + m);
  await(c, fk, 16, pkv);
  STAMP(c, 25);
  {
    f32x4 part[8];
#pragma unroll
    for (int s = 0; s < 8; s++) part[s] = __builtin_bit_cast(f32x4, ld128(c.ws, hh * 2048 + r * 16, KV_OFF + (hd * 16 + 2 * s) * 2048 + m * 512));
    f32x4 z = __builtin_bit_cast(f32x4, ld128(c.ws, (lane & 15) * 128 + blk * 16, KZ_OFF + hd * 2048 + m * 32));
    SB();
    float v[4] = {part[0][0], part[0][1], part[0][2], part[0][3]};
#pragma unroll
    for (int s = 1; s < 8; s++)
#pragma unroll
      for (int k = 0; k < 4; k++) {
        v[k] += part[s][k];
        OPAQUE(v[k]);
      }
    swap32(v[0], v[2]);
    swap32(v[1], v[3]);
    v[0] += v[2];
    OPAQUE(v[0]);
    *(LAS u32*)(L_WPHI + r * 32 + m * 8 + hh * 4) = pk(v[0], v[1] + v[3]);
    float za = hh ? z[2] : z[0], zb = hh ? z[3] : z[1];
    sfor<0, 4>([&](auto KK) {
      za += __int_as_float(__builtin_amdgcn_update_dpp(0, __float_as_int(za), 0x120 + (8 >> decltype(KK)::v), 0xf, 0xf, false));
      zb += __int_as_float(__builtin_amdgcn_update_dpp(0, __float_as_int(zb), 0x120 + (8 >> decltype(KK)::v), 0xf, 0xf, false));
    });
    *(LAS u32*)(L_WPHI + (32 + blk) * 32 + m * 8 + hh * 4) = pk(za, zb);
    sem_post(SEM_A);
    sem_wait(SEM_A, 4);
    const u32 e = blk == 0 ? hh * 16 + (lane & 15) : 32 + hh;
    const u32x4 k0 = lds128(L_WPHI + e * 32), k1 = lds128(L_WPHI + e * 32 + 16);
    f32x16 Z0 = mfma(k0, p00, f32x16{}), Z1 = mfma(k0, p10, f32x16{});
    Z0 = mfma(k1, p01, Z0);
    Z1 = mfma(k1, p11, Z1);
    constexpr int NO = 0;
    const float r0 = __builtin_amdgcn_rcpf(mul1(Z0[8 - NO] + 1e-6f, rs0)), r1 = __builtin_amdgcn_rcpf(mul1(Z1[8 - NO] + 1e-6f, rs1));
    u32x4 o0, o1;
#pragma unroll
    for (int d = 0; d < 4; d++) {
      o0[d] = pk(mul1(Z0[NO + 2 * d], r0), mul1(Z0[NO + 2 * d + 1], r0));
      o1[d] = pk(mul1(Z1[NO + 2 * d], r1), mul1(Z1[NO + 2 * d + 1], r1));
    }
    st128(o0, c.ws, lane * 16, OF_OFF + ((8 * ga + 2 * m) * 32 + hd) * 1024);
    st128(o1, c.ws, lane * 16, OF_OFF + ((8 * ga + 2 * m + 1) * 32 + hd) * 1024);
  }
  STAMP(c, 26);
  WAITV(0);
  publish(c, 0xff00000000ull);
  STAMP(c, 27);
  const u32 orow = 64 * i + 32 * (m >> 1) + r, oidx = orow * 512 + 64 * ((b >> 3) & 7) + 32 * (m & 1) + 8 * hh;
  u32x4 xr[2] = {*(const GAS u32x4*)(x + oidx), *(const GAS u32x4*)(x + oidx + 16)};
  float frs = __uint_as_float(__builtin_amdgcn_raw_buffer_load_b32(c.ws, orow * 8 + 4, RS_OFF, AUX));
  u64 po = peek(c, fo, 8);
  u32x4 sel[2];
  {
    const u32 one = (((r >> 2) & 1) == hh) ? ((r & 1) ? 0x3f800000u : 0x3f80u) : 0u, dw = ((r >> 3) & 1) * 2 + ((r >> 1) & 1);
#pragma unroll
    for (int d = 0; d < 4; d++) {
      sel[0][d] = (dw == (u32)d && blk == 0) ? one : 0u;
      sel[1][d] = (dw == (u32)d && blk == 1) ? one : 0u;
    }
    OPAQUE(sel[0]); OPAQUE(sel[1]);
  }
  WAITV(0);
  OPAQUE(xr[0]); OPAQUE(xr[1]); OPAQUE(frs);
  await(c, fo, 8, po);
  STAMP(c, 30);
  u32x4 af[8][2];
#pragma unroll
  for (int n = 0; n < 8; n++)
#pragma unroll
    for (int u = 0; u < 2; u++) af[n][u] = ld128(c.ws, lane * 16, OF_OFF + ((2 * i + ((m >> 1) ^ u)) * 32 + 8 * m + n) * 1024);
  SB();
  f32x16 acc[4];
  constexpr int NSM = 4;
  sfor<0, NSM>([&](auto NN) {
    constexpr int N = decltype(NN)::v;
    if constexpr (N == 0) {
      mma0(acc[0], bf[N][0], af[N][0]); mma0(acc[1], bf[N][1], af[N][0]); mma0(acc[2], bf[N][0], af[N][1]); mma0(acc[3], bf[N][1], af[N][1]);
    } else {
      mma(acc[0], bf[N][0], af[N][0]); mma(acc[1], bf[N][1], af[N][0]); mma(acc[2], bf[N][0], af[N][1]); mma(acc[3], bf[N][1], af[N][1]);
    }
  });
  sem_wait(SEM_B + m, 3);
  SB();
  sfor<0, 4>([&](auto KK) {
    constexpr int k = 3 - decltype(KK)::v;
    sfor<NSM, 8>([&](auto NN) {
      constexpr int N = decltype(NN)::v;
      mma(acc[k], bf[N][k & 1], af[N][k >> 1]);
      if constexpr (N == NSM + 1 && k < 3) {
        SB();
        put_block<10 + k>(c, acc[k + 1]);
        if constexpr (k == 0) sem_post(SEM_H);
        SB();
      }
    });
  });
  STAMP(c, 31);
  sem_wait(SEM_H, 4);
  STAMP(c, 32);
  {
    u32x4 f[7][2];
    get_block<10>(c, m ^ 1, f[0]);
    get_block<11>(c, m ^ 2, f[1]);
    get_block<12>(c, m ^ 3, f[2]);
#pragma unroll
    for (int k = 0; k < 3; k++) { mma(acc[0], sel[0], f[k][0]); mma(acc[0], sel[1], f[k][1]); }
    sem_wait(SEM_C, 4);
    STAMP(c, 33);
    get_block<0>(c, m, f[3]);
    get_block<1>(c, m ^ 1, f[4]);
    get_block<2>(c, m ^ 2, f[5]);
    get_block<3>(c, m ^ 3, f[6]);
#pragma unroll
    for (int k = 3; k < 7; k++) { mma(acc[0], sel[0], f[k][0]); mma(acc[0], sel[1], f[k][1]); }
  }
  if (threadIdx.x == 319) __hip_atomic_store((GAS u64*)(c.wsp + EP_OFF + b * 8), c.tag, __ATOMIC_RELAXED, __HIP_MEMORY_SCOPE_AGENT);
  asm volatile("s_nop 15" : "+v"(acc[0]));
#pragma unroll
  for (int h = 0; h < 2; h++) {
    float e[8];
#pragma unroll
    for (int k = 0; k < 8; k++) e[k] = acc[0][8 * h + k];
#pragma unroll
    for (int k = 0; k < 4; k++) swap32(e[k], e[4 + k]);
    u32x4 o;
#pragma unroll
    for (int d = 0; d < 4; d++)
      o[d] = pk(__builtin_fmaf(e[2 * d], frs, __uint_as_float(xr[h][d] << 16)), __builtin_fmaf(e[2 * d + 1], frs, __uint_as_float(xr[h][d] & 0xffff0000u)));
    *(GAS u32x4*)(out + oidx + 16 * h) = o;
  }
}

template <bool FFN> struct Slots {
  static constexpr int NBK = FFN ? 7 : 6, N7 = 28 * NBK, NS = N7 + 5 * NBK;
  static constexpr int blk(int n) {
    if (n < N7) return n % NBK;
    const int k = (n - N7) / 5;
    return (!FFN && k < 2) ? 1 - k : k;
  }
  static constexpr int step(int n) { return n < N7 ? n / NBK : ((n - N7) % 5 == 0 ? -1 : 27 + (n - N7) % 5); }
  static constexpr int bar(int G) { return (G < 7 ? (4 * G + 3) * NBK : NS - NBK) + 1; }
};
template <bool FFN>
DI void compute1(Ctx& c, const Tile& t, u32x4 xa, u32x4 xb) {
  using SL = Slots<FFN>;
  constexpr int NBK = SL::NBK;
  const u32 lane = c.lane, m = c.m;
  const int rt = t.rt, j = t.j;
  const u32 hh = lane >> 5, blk = (lane >> 4) & 1, q = (lane & 15) >> 2, p = lane & 3, r = lane & 31;
  u32 ax[4], aw[2][2], ac;
#pragma unroll
  for (int s = 0; s < 4; s++) ax[s] = m * 4096 + r * 128 + (((2 * s + hh) ^ ((r >> 1) & 7)) << 4);
  const u32 at = (((2 * blk + (p >> 1)) ^ ((q >> 1) << 2)) << 4) + 8 * (p & 1);
  {
    const u32 a = (8 * hh + q) * 128 + at;
    aw[0][0] = a + LWA; aw[0][1] = (a ^ 64) + LWA; aw[1][0] = a + LWB; aw[1][1] = (a ^ 64) + LWB;
    ac = (8 * hh + q) * 64 + 32 * blk + 8 * p + LWB + 8192;
    OPAQUE(aw[0][0]); OPAQUE(aw[0][1]); OPAQUE(aw[1][0]); OPAQUE(aw[1][1]); OPAQUE(ac);
  }
  u32 acs = L_ST + r * 8;
  OPAQUE(acs);
  auto rdw = [&](auto NN) -> u32x4 {
    constexpr int T = SL::step(decltype(NN)::v), M = SL::blk(decltype(NN)::v);
    if constexpr (T < 0) {
      const u32x2 v = *(LAS u32x2*)(acs + M * 256);
      return __builtin_shufflevector(v, v, 0, 1, -1, -1);
    }
    else {
      constexpr int S = (T >> 2) % 3, s = T & 3;
      if constexpr (M < 6) {
        const u32 a = (M < 4 ? aw[0][M & 1] + S * 16384 + (M >> 1) * 8192 : aw[1][M & 1] + S * 12288) + s * 2048;
        return trd2(a, a + 512);
      } else {
        const u32 a = ac + S * 12288 + s * 1024;
        return trd2(a, a + 256);
      }
    }
  };
  u32x4 xf[4], wf[2 * NBK];
  u32x2 fm;
  f32x16 acc[NBK], P0, P1;
  float rstd;
  u32x2 sv[8];
  u32x4 eo;
  float sa;
  const u32 tb = rt * 4 + m;
  const u32 obase = FFN ? HF_OFF + tb * 112 * 1024 : QF_OFF + (tb * 32 + 4 * j) * 1024;
  WAITL0();
  STAMP(c, 1);
  WAITV(NCP);
  BAR();
  STAMP(c, 2);
  xf[0] = lds128(ax[0]);
  sfor<0, NBK>([&](auto NN) { wf[decltype(NN)::v] = rdw(NN); });
  SB();
  auto filler = [&](auto NN) {
    constexpr int n = decltype(NN)::v;
    if constexpr (n == SL::bar(SG) - 5) {
      WAITV(0);
      OPAQUE(xa);
      OPAQUE(xb);
      const u32 a = LBS + m * 4096 + (4 * hh + q) * 128 + at;
#pragma unroll
      for (int k = 0; k < 8; k++) sv[k] = trd((a ^ ((k >> 2) * 64)) + (k & 3) * 1024);
      set_tag(c);
    }
    if constexpr (n == SL::bar(SG) - 2) {
      u32 ones = ONES2;
      asm volatile("" : "+s"(ones));
      float st[4] = {0.f, 0.f, 0.f, 0.f}, cs[2] = {0.f, 0.f};
#pragma unroll
      for (int d = 0; d < 4; d++) {
        st[0] = dot2(ones, xa[d], st[0]);
        st[1] = dot2(xa[d], xa[d], st[1]);
        st[2] = dot2(ones, xb[d], st[2]);
        st[3] = dot2(xb[d], xb[d], st[3]);
      }
#pragma unroll
      for (int d = 0; d < 8; d++) {
        cs[0] = dot2(ones, sv[d >> 1][d & 1], cs[0]);
        cs[1] = dot2(ones, sv[4 + (d >> 1)][d & 1], cs[1]);
      }
      swap32(st[0], st[1]);
      swap32(st[2], st[3]);
      swap32(cs[0], cs[1]);
      *(LAS float*)(L_ST + (m * 64 + lane) * 4) = cs[0] + cs[1];
      st[0] += st[1];
      OPAQUE(st[0]);
      st[2] += st[3];
      auto sw16 = __builtin_amdgcn_permlane16_swap(__float_as_uint(st[0]), __float_as_uint(st[2]), false, false);
      sa = __uint_as_float(sw16[0]) + __uint_as_float(sw16[1]);
      OPAQUE(sa);
    }
    if constexpr (n == SL::bar(SG) - 1) {
      sfor<0, 4>([&](auto KK) { sa += __int_as_float(__builtin_amdgcn_update_dpp(0, __float_as_int(sa), 0x120 + (8 >> decltype(KK)::v), 0xf, 0xf, false)); });
      float mean = sa * (1.f / 512.f), ms = mean;
      swap32(mean, ms);
      ms = __builtin_amdgcn_rsqf(mx(__builtin_fmaf(-mean, mean, ms), 0.f) + 1e-5f);
      const u32 mh = pk(-mean, -mean);
      const float ml = -mean - __uint_as_float(mh & 0xffff0000u);
      *(LAS u32x2*)(L_ST + ((lane & 47) == 0 ? 1024 + m * 16 + blk * 8 : 2048 + lane * 8)) = u32x2{(mh & 0xffffu) | (pk(ml, ml) & 0xffff0000u), __float_as_uint(ms)};
    }
    if constexpr (n >= SL::N7 + 5) {
      constexpr int g = (n - SL::N7) % 5, M = SL::blk(n - 5);
      if constexpr (g >= 1) {
        constexpr int s = (g - 1) >> 1, h = (g - 1) & 1;
        if constexpr (FFN || M < 4) {
          eo[2 * h] = pk2(pr<8 * s + 4 * h>(acc[M]));
          eo[2 * h + 1] = pk2(pr<8 * s + 4 * h + 2>(acc[M]));
          if constexpr (FFN) {
            eo[2 * h] = relu2(eo[2 * h]);
            eo[2 * h + 1] = relu2(eo[2 * h + 1]);
          }
          if constexpr (h == 0) OPAQUE(eo);
        }
        if constexpr (h == 1) {
          if constexpr (FFN) st128(eo, c.ws, lane * 16, obase + ((t.c0 >> 5) + 2 * M + s) * 1024);
          else if constexpr (M == 1) {
            *(LAS u32x4*)(((m >> 1) ? L_SP2 : L_SP1) + (m & 1) * 2048 + s * 1024 + lane * 16) = eo;
            if constexpr (s == 1) sem_post(SEM_K + m);
          } else if constexpr (M == 0) {
            const u32x4 wp = wphi_frag(L_WPHI + s * 512 + (4 * hh + q) * 32 + p * 8, blk ? 0x80008000u : 0u);
            asm volatile("s_nop 1\n\tv_mfma_f32_32x32x16_bf16 %0, %1, %2, 0" : "=&v"(s ? P1 : P0) : "v"(wp), "v"(eo));
          } else if constexpr (M < 4) st128(eo, c.ws, lane * 16, obase + (2 * (M - 2) + s) * 1024);
        }
      }
    }
  };
  sfor<0, SL::NS>([&](auto NN) {
    constexpr int n = decltype(NN)::v, T = SL::step(n), M = SL::blk(n);
    constexpr bool TR = !FFN && M >= 4;
    const u32x4 xo = xf[T & 3], wo_ = wf[n % (2 * NBK)];
    if constexpr (T < 0) {
      const u32x2 cf = __builtin_shufflevector(wo_, wo_, 0, 1);
      mma8(acc[M], TR ? fm : cf, TR ? cf : fm);
    } else if constexpr (T == 0) mma0(acc[M], TR ? xo : wo_, TR ? wo_ : xo);
    else mma(acc[M], TR ? xo : wo_, TR ? wo_ : xo);
    constexpr int G = n < SL::N7 ? n / (4 * NBK) : 7;
    if constexpr (n == SL::bar(G)) {
      WAITL0();
      STAMP(c, 3 + 2 * G);
      BAR();
      STAMP(c, 4 + 2 * G);
    }
    if constexpr (n + NBK < SL::NS && n != SL::bar(G) - 1) {
      if constexpr (n < SL::N7 && (n == SL::bar(G) || (M == 0 && (T & 3) != 3))) {
        if constexpr (T == 27) {
          fm = *(LAS u32x2*)(hh ? L_ZERO : L_ST + 4096 + (m * 32 + r) * 8);
          xf[0] = lds128(ax[0] + 16384); xf[1] = lds128(ax[1] + 16384); xf[2] = lds128(ax[2] + 16384);
        } else xf[(T + 1) & 3] = lds128(ax[(T + 1) & 3] + (((T + 1) >> 2) % 3) * 16384);
      }
      if constexpr (n == SL::bar(G)) wf[(n - 1 + NBK) % (2 * NBK)] = rdw(IC<n - 1 + NBK>{});
      wf[(n + NBK) % (2 * NBK)] = rdw(IC<n + NBK>{});
      if constexpr (n == SL::N7) {
        xf[3] = lds128(ax[3] + 16384);
        if constexpr (!FFN) rstd = *(LAS float*)(L_RS + m * 128 + r * 4);
      }
    }
    filler(NN);
    SB();
  });
  __builtin_amdgcn_s_setprio(3);
  if constexpr (FFN) {
    asm volatile("s_nop 15" : "+v"(acc[6]));
    sfor<0, 2>([&](auto SS) {
      u32x4 o = pack8<decltype(SS)::v>(acc[6]);
#pragma unroll
      for (int d = 0; d < 4; d++) o[d] = relu2(o[d]);
      st128(o, c.ws, lane * 16, obase + ((t.cc >> 5) + decltype(SS)::v) * 1024);
    });
  } else {
    const u32 apw = lx(1) + m * 2048 + r * 64 + ((hh ^ ((r >> 1) & 7)) << 3);
    phi_store(apw, P0, rstd * LOG2E);
    SB();
    WAITV(0);
    publish(c, 0xf00ull);
    const u32 av0 = L_ST + m * 2048 + lane * 16, av1 = lwb(1) + m * 2048 + lane * 16;
    sfor<0, 2>([&](auto TT) {
      constexpr int tt = decltype(TT)::v;
      const f32x4 ra = ldsf4(L_RS + m * 128 + (16 * tt + 4 * hh) * 4), rb = ldsf4(L_RS + m * 128 + (16 * tt + 8 + 4 * hh) * 4);
      const f32x4 a0 = q4<8 * tt>(acc[4]) * ra, b0 = q4<8 * tt + 4>(acc[4]) * rb, a1 = q4<8 * tt>(acc[5]) * ra, b1 = q4<8 * tt + 4>(acc[5]) * rb;
      *(LAS u32x4*)(av0 + tt * 1024) = u32x4{pk(a0[0], a0[1]), pk(a0[2], a0[3]), pk(b0[0], b0[1]), pk(b0[2], b0[3])};
      *(LAS u32x4*)(av1 + tt * 1024) = u32x4{pk(a1[0], a1[1]), pk(a1[2], a1[3]), pk(b1[0], b1[1]), pk(b1[2], b1[3])};
    });
    phi_store(apw + 8192, P1, rstd * LOG2E);
    WAITL0();
    STAMP(c, 19);
    BAR();
    const u32 t0 = 4 * hh + q;
    const u32 apr = ((m >> 1) ? lwa(1) : lx(1)) + (m & 1) * 8192 + t0 * 64 + (((4 * blk + p) ^ (t0 >> 1)) << 3);
    const u32 avr = ((m >> 1) ? lwb(1) : L_ST) + lane * 16;
    const bool isv = blk == (m & 1);
    u32x4 pa[8], vb[8];
#pragma unroll
    for (int k = 0; k < 8; k++) {
      pa[k] = trd2(apr + k * 1024, (apr ^ 32) + k * 1024 + 512);
      vb[k] = lds128(avr + k * 1024);
    }
    sem_post(SEM_KV);
    f32x16 X = f32x16{};
#pragma unroll
    for (int k = 0; k < 8; k++) {
#pragma unroll
      for (int d = 0; d < 4; d++) vb[k][d] = isv ? vb[k][d] : ONES2;
      X = mfma(pa[k], vb[k], X);
    }
    const u32 pi = (4 * j + m) * 16 + rt;
    const u32 ve = isv ? KV_OFF + pi * 2048 + (hh * 16 + (lane & 15)) * 16 : KZ_OFF + pi * 128 + hh * 16, vs = isv ? 512 : 32;
    st128(__builtin_bit_cast(u32x4, q4<0>(X)), c.ws, ve, 0);
    st128(__builtin_bit_cast(u32x4, q4<4>(X)), c.ws, ve + vs, 0);
    st128(__builtin_bit_cast(u32x4, q4<8>(X)), c.ws, ve + 2 * vs, 0);
    st128(__builtin_bit_cast(u32x4, q4<12>(X)), c.ws, ve + 3 * vs, 0);
  }
  STAMP(c, 20);
  __builtin_amdgcn_s_setprio(0);
  if constexpr (!FFN) {
    WAITV(0);
    publish(c, 0xffull);
    sem_wait(SEM_KV, 4);
  }
}
DI void compute2(Ctx& c, bool ffn, const u16* w2) {
  const u32 lane = c.lane, m = c.m;
  const int b = blockIdx.x, i = (b & 7) * 4 + (b >> 6);
  init_ab(c);
  BSrc b2;
  b2.init(c, w2, 1792, 448);
  auto issue = [&](auto NN) { b2.issue<decltype(NN)::v>(c); };
  u32x4 af[28][2], bf[28][2];
  auto ldb = [&](auto NN) {
    constexpr int N = decltype(NN)::v;
    bf[N][0] = rdb<N>(c, 0);
    bf[N][1] = rdb<N>(c, 1);
  };
  sfor<5 + NB7, 10>(issue);
  sfor<10, 10 + NB7 + 2>(issue);
  WAITV(14);
  publish(c, ffn ? 0xffull : 0ull);
  STAMP(c, 23);
  sem_wait(SEM_B + m, 1);
  sfor<0, 5>(ldb);
  sfor<10 + NB7 + 2, 16>(issue);
  WAITL0();
  sfor<16, 21>(issue);
  WAITV(22);
  sfor<5, 10>(ldb);
  const u32 fh = MB_OFF + b * 1024 + 32 * m;
  u64 ph = peek(c, fh, 4);
  WAITL0();
  sfor<21, 26>(issue);
  STAMP(c, 24);
  WAITV(10);
  await(c, fh, 4, ph);
  asm volatile("" : "+s"(c.rot));
  STAMP(c, 25);
  const u32 ah = HF_OFF + ((2 * i) * 112 + 28 * m) * 1024;
  const u32 va[2] = {lane * 16 + (m >> 1) * 114688, lane * 16 + ((m >> 1) ^ 1) * 114688};
  auto lda = [&](auto NN, int u) {
    constexpr int N = decltype(NN)::v;
    if constexpr (N < 28) af[N][u] = ld128(c.ws, va[u], ah + hstep<N>(c) * 1024);
  };
  sfor<0, CW>([&](auto NN) { lda(NN, 0); lda(NN, 1); });
  SB();
  STAMP(c, 27);
  f32x16 ac2[4];
  sfor<0, 24>([&](auto NN) {
    constexpr int n = decltype(NN)::v;
    sfor<0, 4>([&](auto KK) {
      constexpr int k = decltype(KK)::v;
      if constexpr (n == 0) mma0(ac2[k], bf[n][k & 1], af[n][k >> 1]);
      else mma(ac2[k], bf[n][k & 1], af[n][k >> 1]);
      SB();
      if constexpr ((k & 1) == 1 && n + CW < 28) lda(IC<n + CW>{}, k >> 1);
      if constexpr ((k & 1) == 0 && n >= 6) bf[n + 4][k >> 1] = rdb<n + 4>(c, k >> 1);
      if constexpr (k == 3 && n == 8) {
        sem_wait(SEM_B + m, 2);
        sfor<26, 28>(issue);
      }
      if constexpr (k == 3 && n == 11) sem_post(SEM_B + m);
      SB();
    });
  });
  STAMP(c, 31);
  sfor<0, 4>([&](auto KK) {
    constexpr int k = decltype(KK)::v;
    sfor<24, 28>([&](auto NN) {
      constexpr int n = decltype(NN)::v;
      mma(ac2[k], bf[n][k & 1], af[n][k >> 1]);
      if constexpr (n == 25 && k > 0) {
        SB();
        put_block<k - 1>(c, ac2[k - 1]);
        SB();
      }
    });
  });
  asm volatile("s_nop 15" : "+v"(ac2[3]));
  put_block<3>(c, ac2[3]);
  sem_post(SEM_C);
  STAMP(c, 32);
}

#define ROTB 1
__global__ __launch_bounds__(512, 2) void fused_k(const u16* x, const u16* wqkv, const u16* wphi, const u16* wo, const u16* w1, const u16* w2, u16* out,
                                                  char* ws) {
  __shared__ __attribute__((aligned(16))) char smem[163840];
  asm volatile("" ::"v"((LAS char*)smem));
  const int b = blockIdx.x, xcd = b & 7, r = b >> 3;
  u64 pc0;
  asm volatile("s_getpc_b64 %0" : "=s"(pc0));
  {
    u32 off = ((r * 8 + (threadIdx.x >> 6)) * 3 + (threadIdx.x & 3)) * 64;
    off = off < CODE_BYTES ? off : 0;
    asm volatile("s_mov_b32 m0, %2\n\ts_mov_b64 exec, 7\n\tglobal_load_lds_dword %0, %1\n\ts_mov_b64 exec, -1" ::"v"(off), "s"(pc0), "i"(L_ST + 7168) : "memory");
  }
  Ctx c;
  c.dbg = 0;
  STAMP(c, 0);
  c.lane = threadIdx.x & 63;
  const u32 w = __builtin_amdgcn_readfirstlane(threadIdx.x >> 6);
  c.m = w & 3;
  c.ws = __builtin_amdgcn_make_buffer_rsrc(ws, 0, WS_BYTES, 0x00020000);
  c.wsp = ws;
  c.rot = ROTB == 0 ? 0 : ROTB == 1 ? (b >> 6) * 7 : ((b >> 6) * 7 + (((b >> 3) & 7) * 7 >> 1)) % 28;
  const int rt = (xcd >> 2) * 8 + (r >> 2), ct = (xcd & 3) * 4 + (r & 3), j = ct >> 1;
  Tile t;
  t.rt = rt;
  t.j = j;
  t.ct = ct;
  t.ffn = ct & 1;
  t.ldw = t.ffn ? 3584 : 3072;
  const u32 lane = c.lane, m = c.m;
  t.srd_x = make_srd(x, 2048 * 512 * 2);
  t.srd_w = make_srd(t.ffn ? w1 : wqkv, 512 * t.ldw);
  t.vx0 = (lane >> 3) * 1024 + (((lane & 7) ^ (lane >> 4)) << 4);
  {
    const u32 sx = (rt * 128 + m * 32 + (w >> 2) * 16) * 1024, dx = m * 4096 + (w >> 2) * 2048;
    dma<LX, 0>(t.srd_x, t.vx0, sx, dx);
    dma<LX + 1024, 8192>(t.srd_x, t.vx0 ^ 64, sx, dx);
    t.vw = (lane >> 3) * t.ldw + (((lane & 7) ^ (((lane >> 4) & 1) << 2)) << 4);
    t.c0 = t.ffn ? 448 * j + (j & 1) * 64 : 1024 + 128 * j;
    t.c1 = t.ffn ? t.c0 + 128 : 128 * j;
    t.c2 = t.ffn ? t.c0 + 256 : 2048 + 128 * j;
    t.cc = t.ffn ? 448 * j + ((j & 1) ? 0 : 384) : 0;
    dma<LWA, 0>(t.srd_w, t.vw, w * 8 * t.ldw + t.c0, w * 1024);
    dma<LWA + 8192, 0>(t.srd_w, t.vw, w * 8 * t.ldw + t.c1, w * 1024);
    dma<LWB, 0>(t.srd_w, t.vw, w * 8 * t.ldw + t.c2, w * 1024);
    if (t.ffn && w >= 4) dma<LWB + 8192, 0>(t.srd_w, (lane >> 2) * 3584 + ((lane & 3) << 4), m * 16 * 3584 + t.cc, m * 1024);
    BAR();
    SB();
  }
  if (__builtin_expect(w >= 4, 1)) {
    helper(c, t, x, wo, w2, out);
  } else {
    u32x4 xa, xb;
    {
      const u32 vs = m < 3 ? t.vw : (lane >> 3) * t.ldw + ((lane & 3) << 4);
      const u32 so = rt * 32 * t.ldw + (m == 0 ? t.c0 : m == 1 ? t.c1 : m == 2 ? t.c2 : t.cc);
      dma<LBS, 0>(t.srd_w, vs, so, m * 4096);
      dma<LBS + 1024, 0>(t.srd_w, vs, so + 8 * t.ldw, m * 4096);
      dma<LBS + 2048, 0>(t.srd_w, vs, so + 16 * t.ldw, m * 4096);
      dma<LBS + 3072, 0>(t.srd_w, vs, so + 24 * t.ldw, m * 4096);
      const u32 xo = (rt * 128 + ct * 8 + m * 2) * 1024 + lane * 16;
      asm volatile("global_load_dwordx4 %0, %1, %2" : "=v"(xa) : "v"(xo), "s"(x) : "memory");
      asm volatile("global_load_dwordx4 %0, %1, %2 offset:1024" : "=v"(xb) : "v"(xo), "s"(x) : "memory");
      dma<L_WPHI, 0>(make_srd(wphi, 32 * 512), lane * 16, m < 2 ? (u32)(j * 2048 + m * 1024) : (u32)(((b & 7) * 2 + ((b >> 4) & 1)) * 1024), (m < 2 ? m : 2) * 1024);
      const u32 ii = 2 * rt + (m >> 1), l7 = lane & 7;
      const u32 t1 = t.ffn ? (ii & 3) * 64 + (ii >> 2) + l7 * 8 : l7 * 32 + m * 8 + j, f1 = t.ffn ? j * 2 + (m & 1) : 16 + rt;
      load_tag(c, lane < 8 ? t1 : lane < 12 ? (rt >> 1) * 32 + (lane & 3) * 8 + j : b, lane < 8 ? f1 : 96 + (rt & 1) * 4 + m);
      *(LAS u32*)(L_SEM + (lane & 31) * 4) = 0;
    }
    if (t.ffn) compute1<true>(c, t, xa, xb);
    else compute1<false>(c, t, xa, xb);
    compute2(c, t.ffn, w2);
  }
}

extern "C" void kernel_launch(void* const* d_in, const int* in_sizes, int n_in, void* d_out, int out_size, void* d_ws, size_t ws_size,
                              hipStream_t stream) {
  const u16 *x = (const u16*)d_in[0], *wqkv = (const u16*)d_in[1], *wphi = (const u16*)d_in[2], *wo = (const u16*)d_in[3];
  const u16 *w1 = (const u16*)d_in[5], *w2 = (const u16*)d_in[6];
  fused_k<<<256, 512, 0, stream>>>(x, wqkv, wphi, wo, w1, w2, (u16*)d_out, (char*)d_ws + ((ws_size - WS_BYTES) & ~(size_t)0xfffff));
}
```

```cpp
#include <hip/hip_runtime.h>
#pragma clang diagnostic ignored "-Wint-to-pointer-cast"
typedef unsigned short u16;
typedef unsigned u32;
typedef unsigned long long u64;
typedef u32 u32x2 __attribute__((ext_vector_type(2)));
typedef u32 u32x4 __attribute__((ext_vector_type(4)));
typedef short s16x2 __attribute__((ext_vector_type(2)));
typedef short s16x4 __attribute__((ext_vector_type(4)));
typedef float f32x2 __attribute__((ext_vector_type(2)));
typedef float f32x4 __attribute__((ext_vector_type(4)));
typedef float f32x16 __attribute__((ext_vector_type(16)));
typedef __bf16 b16x2 __attribute__((ext_vector_type(2)));
typedef __bf16 b16x8 __attribute__((ext_vector_type(8)));
#define LAS __attribute__((address_space(3)))
#define GAS __attribute__((address_space(1)))
#define DI __device__ __forceinline__
#define SB() __builtin_amdgcn_sched_barrier(0)
#define BAR() __builtin_amdgcn_s_barrier()
#define WAITV(n) do { asm volatile("s_waitcnt vmcnt(%0)" ::"i"(n) : "memory"); SB(); } while (0)
#define WAITL0() asm volatile("s_waitcnt lgkmcnt(0)" ::: "memory")
#define OPAQUE(x) asm volatile("" : "+v"(x))

#define NTX 0
#define NTW 0
#define NTS 0
#define NTSX ""
constexpr u32 HF_OFF = 0;
constexpr u32 QF_OFF = 64u * 112u * 1024u;
constexpr u32 OF_OFF = QF_OFF + 64u * 32u * 1024u;
constexpr u32 KV_OFF = OF_OFF + 64u * 32u * 1024u;
constexpr u32 KZ_OFF = KV_OFF + 32u * 16u * 2048u;
constexpr u32 CS_OFF = KZ_OFF + 32u * 16u * 128u;
constexpr u32 RS_OFF = CS_OFF + 256u * 1024u;
constexpr u32 MB_OFF = RS_OFF + 2048u * 8u;
constexpr u32 EP_OFF = MB_OFF + 256u * 1024u;
constexpr u32 WS_BYTES = EP_OFF + 2048u;
constexpr u32 LX = 0, LWA = 49152, LWB = 98304, LBS = 135168, LSCR = 159744;
constexpr u32 lx(int s) { return LX + s * 16384; }
constexpr u32 lwa(int s) { return LWA + s * 16384; }
constexpr u32 lwb(int s) { return LWB + s * 12288; }
constexpr u32 L_WPHI = LSCR, L_WPA = LSCR + 2048, L_RS = LSCR + 3072, L_SEM = LSCR + 3584, L_ZERO = LSCR + 3648;
constexpr u32 L_SP1 = lwb(1) + 8192, L_SP2 = lwb(2) + 8192;
constexpr u32 L_ST = LBS + 16384;
constexpr int AUX = 16;

DI f32x16 mfma(u32x4 a, u32x4 b, f32x16 c) {
  return __builtin_amdgcn_mfma_f32_32x32x16_bf16(__builtin_bit_cast(b16x8, a), __builtin_bit_cast(b16x8, b), c, 0, 0, 0);
}
DI void mma(f32x16& d, u32x4 a, u32x4 b) { asm volatile("v_mfma_f32_32x32x16_bf16 %0, %1, %2, %0" : "+v"(d) : "v"(a), "v"(b)); }
DI void mma0(f32x16& d, u32x4 a, u32x4 b) { asm volatile("v_mfma_f32_32x32x16_bf16 %0, %1, %2, 0" : "=&v"(d) : "v"(a), "v"(b)); }
DI void mma8(f32x16& d, u32x2 a, u32x2 b) { asm volatile("v_mfma_f32_32x32x8_bf16 %0, %1, %2, %0" : "+v"(d) : "v"(a), "v"(b)); }
DI u32x2 trd(u32 addr) { return __builtin_bit_cast(u32x2, __builtin_amdgcn_ds_read_tr16_b64_v4i16((LAS s16x4*)addr)); }
DI u32x4 trd2(u32 a0, u32 a1) {
  u32x2 lo = trd(a0), hi = trd(a1);
  return __builtin_shufflevector(lo, hi, 0, 1, 2, 3);
}
DI u32x4 lds128(u32 addr) { return *(LAS u32x4*)addr; }
DI f32x4 ldsf4(u32 addr) { return *(LAS f32x4*)addr; }
DI u32 pk(float a, float b) {
  f32x2 v = {a, b};
  return __builtin_bit_cast(u32, __builtin_convertvector(v, b16x2));
}
DI u32 pk2(f32x2 v) { return __builtin_bit_cast(u32, __builtin_convertvector(v, b16x2)); }
DI u32 relu2(u32 v) { return __builtin_bit_cast(u32, __builtin_elementwise_max(__builtin_bit_cast(s16x2, v), s16x2{0, 0})); }
extern "C" __device__ float fmul_legacy_(float, float) __asm("llvm.amdgcn.fmul.legacy");
DI float mul1(float a, float b) { return fmul_legacy_(a, b); }
DI float mx(float a, float b) { return __builtin_elementwise_maximum(a, b); }
DI float dot2(u32 a, u32 b, float c) {
  return __builtin_amdgcn_fdot2_f32_bf16(__builtin_bit_cast(b16x2, a), __builtin_bit_cast(b16x2, b), c, false);
}
DI void swap32(float& a, float& b) {
  auto r = __builtin_amdgcn_permlane32_swap(__float_as_uint(a), __float_as_uint(b), false, false);
  a = __uint_as_float(r[0]);
  b = __uint_as_float(r[1]);
}
template <int I> DI f32x2 pr(const f32x16& a) { return __builtin_shufflevector(a, a, I, I + 1); }
template <int I> DI f32x4 q4(const f32x16& a) { return __builtin_shufflevector(a, a, I, I + 1, I + 2, I + 3); }
DI u32x4 make_srd(const void* p, u32 bytes) {
  u64 a = (u64)p;
  u32x4 r = {(u32)a, (u32)(a >> 32) & 0xffffu, bytes, 0x00020000u};
  return r;
}
template <u32 LI, u32 SI, bool NT = false>
DI void dma(u32x4 srd, u32 voff, u32 sbase, u32 lbase) {
  u32 t;
  if constexpr (NT) {
    asm volatile("s_add_u32 m0, %4, %6\n\ts_add_u32 %0, %3, %5\n\tbuffer_load_dwordx4 %1, %2, %0 offen nt lds"
                 : "=&s"(t)
                 : "v"(voff), "s"(srd), "s"(sbase), "s"(lbase), "i"(SI), "i"(LI)
                 : "memory", "scc");
  } else
  asm volatile("s_add_u32 m0, %4, %6\n\ts_add_u32 %0, %3, %5\n\tbuffer_load_dwordx4 %1, %2, %0 offen lds"
               : "=&s"(t)
               : "v"(voff), "s"(srd), "s"(sbase), "s"(lbase), "i"(SI), "i"(LI)
               : "memory", "scc");
}
DI void st128(u32x4 v, __amdgpu_buffer_rsrc_t rs, u32 voff, u32 soff) {
  __builtin_amdgcn_raw_buffer_store_b128(v, rs, voff, soff, AUX);
  asm volatile("s_nop 1" ::"v"(v) : "memory");
}
#define LDAUX AUX
DI u32x4 ld128(__amdgpu_buffer_rsrc_t rs, u32 voff, u32 soff) { return __builtin_amdgcn_raw_buffer_load_b128(rs, voff, soff, LDAUX); }
template <int OFF> DI void gld64(u64& d, u32 voff, const char* base) {
  asm volatile("global_load_dwordx2 %0, %1, %2 offset:%3 sc1" : "=v"(d) : "v"(voff), "s"(base), "i"(OFF) : "memory");
}
template <int OFF> DI void gld128(f32x4& d, u32 voff, const char* base) {
  asm volatile("global_load_dwordx4 %0, %1, %2 offset:%3 sc1" : "=v"(d) : "v"(voff), "s"(base), "i"(OFF) : "memory");
}
template <int OFF> DI void warm1(u32& d, const void* base) { asm volatile("s_load_dword %0, %1, %2" : "+s"(d) : "s"(base), "i"(OFF)); }
template <int N> struct IC { static constexpr int v = N; };
template <int B, int E, class F> DI void sfor(F&& f) {
  if constexpr (B < E) {
    f(IC<B>{});
    sfor<B + 1, E>(f);
  }
}
constexpr u32 ONES2 = 0x3f803f80u;
constexpr float LOG2E = 1.4426950408889634f;

DI void dual_softmax(const f32x16& P, float sl, u32x4& o0, u32x4& o1) {
  float m0 = mx(mx(mx(mx(mx(mx(mx(P[0], P[1]), P[2]), P[3]), P[4]), P[5]), P[6]), P[7]);
  float m1 = mx(mx(mx(mx(mx(mx(mx(P[8], P[9]), P[10]), P[11]), P[12]), P[13]), P[14]), P[15]);
  swap32(m0, m1);
  m0 = mx(m0, m1) * -sl;
  m1 = m0;
  swap32(m0, m1);
  f32x2 e[8];
  sfor<0, 8>([&](auto II) {
    constexpr int I = decltype(II)::v;
    const float m = I < 4 ? m0 : m1;
    e[I] = __builtin_elementwise_fma(pr<2 * I>(P), f32x2{sl, sl}, f32x2{m, m});
  });
#pragma unroll
  for (int i = 0; i < 8; i++) e[i] = f32x2{__builtin_amdgcn_exp2f(e[i][0]), __builtin_amdgcn_exp2f(e[i][1])};
  const f32x2 s0 = (e[0] + e[1]) + (e[2] + e[3]), s1 = (e[4] + e[5]) + (e[6] + e[7]);
  m0 = s0[0] + s0[1];
  OPAQUE(m0);
  m1 = s1[0] + s1[1];
  swap32(m0, m1);
  m0 = __builtin_amdgcn_rcpf(m0 + m1);
  m1 = m0;
  swap32(m0, m1);
#pragma unroll
  for (int i = 0; i < 4; i++) {
    o0[i] = pk2(e[i] * m0);
    o1[i] = pk2(e[4 + i] * m1);
  }
}
DI u32x4 wphi_frag(u32 a, u32 sg) {
  u32x4 f = trd2(a, a + 256);
  f[0] ^= sg; f[1] ^= sg; f[2] ^= sg; f[3] ^= sg;
  return f;
}
DI void phi_store(u32 apw, const f32x16& P, float sl) {
  u32x4 o0, o1;
  dual_softmax(P, sl, o0, o1);
  *(LAS u32x2*)(apw) = u32x2{o0[0], o0[1]};
  *(LAS u32x2*)(apw ^ 16) = u32x2{o0[2], o0[3]};
  *(LAS u32x2*)(apw ^ 32) = u32x2{o1[0], o1[1]};
  *(LAS u32x2*)(apw ^ 48) = u32x2{o1[2], o1[3]};
}
template <int S> DI u32x4 pack8(const f32x16& a) { return u32x4{pk2(pr<8 * S>(a)), pk2(pr<8 * S + 2>(a)), pk2(pr<8 * S + 4>(a)), pk2(pr<8 * S + 6>(a))}; }

constexpr u32 slot_loc(int s, int k) { return k < 2 ? lx(s) + k * 8192 : k < 4 ? lwa(s) + (k - 2) * 8192 : lwb(s); }
constexpr u32 bloc(int n) {
  return n < 5 ? slot_loc(2, n) : n < 10 ? slot_loc(0, n - 5) : n < 15 ? slot_loc(1, n - 10) : n == 15 ? LBS + 16384
       : n < 26 ? bloc(n - 16) : LBS + (n & 1) * 8192;
}

#define STAMP(c, i) do {} while (0)
#define HSTAMP(c, i) do {} while (0)
struct Ctx {
  mutable u32 dbg;
  __amdgpu_buffer_rsrc_t ws;
  char* wsp;
  u32 m, lane;
  u32 ab[3][2];
  u32 rot;
  u64 tag;
  u64 own;
  u32 fl;
};
template <int N> DI u32x4 rdb(const Ctx& c, int cb) {
  constexpr u32 W = bloc(N) / 57344;
  const u32 a = c.ab[W][cb] + (bloc(N) - W * 57344);
  return trd2(a, a + 1024);
}
DI void init_ab(Ctx& c) {
  const u32 lane = c.lane, m = c.m, hh = lane >> 5, blk = (lane >> 4) & 1, q = (lane & 15) >> 2, p = lane & 3;
#pragma unroll
  for (int cb = 0; cb < 2; cb++) {
    const u32 t = m * 2048 + (4 * hh + q) * 128 + (((4 * (cb ^ (m & 1)) + 2 * blk + (p >> 1)) ^ ((q >> 1) << 2)) << 4) + 8 * (p & 1);
#pragma unroll
    for (int k = 0; k < 3; k++) {
      c.ab[k][cb] = t + k * 57344;
      OPAQUE(c.ab[k][cb]);
    }
  }
}
DI void load_tag(Ctx& c, u32 tgt, u32 slot) {
  c.fl = MB_OFF + tgt * 1024 + slot * 8;
  asm volatile("global_load_dwordx2 %0, %1, %2 sc1" : "=v"(c.tag) : "v"(EP_OFF + tgt * 8), "s"(c.wsp) : "memory");
}
DI void set_tag(Ctx& c) {
  OPAQUE(c.tag);
  c.tag += 1;
  c.own = ((u64)(u32)__builtin_amdgcn_readlane((u32)(c.tag >> 32), 63) << 32) | (u32)__builtin_amdgcn_readlane((u32)c.tag, 63);
}
DI void publish(const Ctx& c, u64 mask) {
  asm volatile("s_mov_b64 exec, %3\n\tglobal_atomic_swap_x2 %0, %1, %2 sc1\n\ts_mov_b64 exec, -1" ::"v"(c.fl), "v"(c.tag), "s"(c.wsp), "s"(mask) : "memory");
}
DI u64 peek(const Ctx& c, u32 off, int n) {
  u64 v = c.own;
  const u64 z = 0;
  asm volatile("s_mov_b64 exec, %4\n\tglobal_atomic_or_x2 %0, %1, %2, %3 sc0 sc1\n\ts_mov_b64 exec, -1"
               : "+v"(v)
               : "v"(off + c.lane * 8), "v"(z), "s"(c.wsp), "s"((1ull << n) - 1)
               : "memory");
  return v;
}
DI void await(const Ctx& c, u32 off, int n, u64 v) {
  OPAQUE(v);
#pragma unroll 1
  for (int it = 0; it < (1 << 16) && !__all(v == c.own); it++) {
    __builtin_amdgcn_s_sleep(2);
    v = peek(c, off, n);
    asm volatile("s_waitcnt vmcnt(0)" : "+v"(v)::"memory");
  }
}
DI void sem_post(u32 idx) {
  asm volatile("s_mov_b64 exec, 1\n\tds_add_u32 %0, %1\n\ts_mov_b64 exec, -1" ::"v"(L_SEM + idx * 4), "v"(1u) : "memory");
}
DI void sem_wait(u32 idx, u32 n) {
  u32 v, sv, left = 1u << 20;
  asm volatile(
      "1:\n\tds_read_b32 %0, %3\n\ts_sub_u32 %2, %2, 1\n\ts_waitcnt lgkmcnt(0)\n\ts_cbranch_scc1 2f\n\tv_readfirstlane_b32 %1, %0\n\ts_nop 0\n\t"
      "s_cmp_lt_u32 %1, %4\n\ts_cbranch_scc1 1b\n2:"
      : "=&v"(v), "=&s"(sv), "+s"(left)
      : "v"(L_SEM + idx * 4), "s"(n)
      : "memory", "scc");
}
constexpr u32 SEM_B = 0, SEM_K = 8, SEM_A = 12, SEM_KV = 13, SEM_C = 14, SEM_H = 15;
template <int N> DI u32 hstep(const Ctx& c) {
  const u32 h = c.rot + N;
  return h >= 28 ? h - 28 : h;
}
struct BSrc {
  u32x4 srd;
  u32 vb, sb, d2;
  DI void init(const Ctx& c, const u16* w, u32 rows, u32 rows_per_q) {
    srd = make_srd(w, rows * 1024);
    vb = (c.lane >> 3) * 1024 + (((c.lane & 7) ^ (((c.lane >> 4) & 1) << 2)) << 4);
    sb = c.m * rows_per_q * 1024 + ((blockIdx.x >> 3) & 7) * 128;
    d2 = __builtin_amdgcn_readfirstlane((threadIdx.x & 192) * 32);
  }
  template <int N> DI void issue(const Ctx& c) const {
    const u32 so = sb + (N < 28 ? hstep<N>(c) : N - 28) * 16384;
    dma<bloc(N), 0>(srd, vb, so, d2);
    dma<bloc(N) + 1024, 8192>(srd, vb, so, d2);
  }
};

struct Tile {
  int rt, j, ct;
  bool ffn;
  u32 ldw;
  u32 c0, c1, c2, cc;
  u32x4 srd_x, srd_w;
  u32 vx0, vw;
};
DI u32 blk_of(u32 rt, u32 ct) { return (((rt & 7) * 4 + (ct & 3)) << 3) + (rt >> 3) * 4 + (ct >> 2); }

template <int N> DI void put_block(const Ctx& c, const f32x16& a) {
  const u32 p = c.m * 2048 + c.lane * 16 + bloc(N);
  *(LAS u32x4*)p = pack8<0>(a);
  *(LAS u32x4*)(p + 1024) = pack8<1>(a);
}
template <int N> DI void get_block(const Ctx& c, u32 src, u32x4 (&f)[2]) {
  const u32 p = src * 2048 + c.lane * 16 + bloc(N);
  f[0] = lds128(p);
  f[1] = lds128(p + 1024);
}

#define PFCODE 34816
#define TOUCH 0
#define WARM 1
#define WN 1
#define WD 1
#define TLBW 0
#define OAUX 0
#define WARMO 0
#define SG 0
#define WOFF 14
#define CW 8
#define NB7 4
constexpr int NCP = 8;
constexpr u32 CODE_BYTES = PFCODE;
DI void helper(Ctx& c, const Tile& t, const u16* x, const u16* wo, const u16* w2, u16* out) {
  constexpr int ND = 10;
  const u32 lane = c.lane, m = c.m;
  const int b = blockIdx.x, i = (b & 7) * 4 + (b >> 6);
  const int hd = (b & 7) * 4 + ((b >> 3) & 3), ga = b >> 5;
  const u32 vx1 = t.vx0 ^ 64, vw1 = t.vw + 8 * t.ldw;
  const u32 vc = (lane >> 2) * 3584 + ((lane & 3) << 4);
  const u32 sx = (t.rt * 128 + m * 32) * 1024, sc = m * 16 * 3584 + t.cc;
  const u32 d4 = m * 4096, d2 = m * 2048, d1 = m * 1024;
  u32 wd = 0;
  const char *pb = (const char*)w2 + (b * 4 + m) * 1792, *pc = (const char*)wo + (b * 4 + m) * 512;
  const char* pa = c.wsp + HF_OFF + ((t.rt * 4 + m) * 112 + t.j * 14 + (t.ffn ? 7 : 0)) * 1024;
  const char* pq_ = c.wsp + QF_OFF + ((t.rt * 4 + m) * 32 + 4 * t.j + (t.ffn ? 2 : 0)) * 1024;
  const char* pd = c.wsp + OF_OFF + ((8 * ga + 2 * m) * 32 + hd) * 1024;
  const char* pf = c.wsp + KV_OFF + ((4 * t.j + m) * 16 + t.rt) * 2048 + (t.ffn ? 1024 : 0);
  const char* pe = (const char*)out + (64 * i + 16 * m) * 1024 + ((b >> 3) & 7) * 128;
  auto warm = [&](auto KK) {
    constexpr int k = decltype(KK)::v - WOFF;
    if constexpr (!WARM || k < 0) {
    } else if constexpr (k < 28) warm1<k * 64>(wd, pb);
    else if constexpr (k < 36) warm1<(k - 28) * 64>(wd, pc);
    else if constexpr (WARMO && k < 52) warm1<(k - 36) * 1024>(wd, pe);
    else if constexpr (WARM >= 2 && k < 52) warm1<(k - 36) * 128>(wd, pq_);
    else if constexpr (WARM >= 2 && k < 60) warm1<(k - 52) * 128>(wd, pf);
    else if constexpr (WARM >= 2 && k < 76) warm1<((k - 60) >> 3) * 32768 + ((k - 60) & 7) * 128>(wd, pd);
    else if constexpr (WARM >= 3 && k < 132) warm1<(k - 76) * 128>(wd, pa);
  };
  auto ringf = [&](auto FF) {
    constexpr int G = decltype(FF)::v / ND, S = G % 3, I = decltype(FF)::v % ND;
    const u32 sg = (m * 16 + G * 64) * t.ldw;
    if constexpr (I == 4) {
      if (t.ffn) dma<lwb(S) + 8192, G * 64 * 3584, NTW>(t.srd_w, vc, sc, d1);
    }
    if constexpr (I < 4) dma<lx(S) + I * 1024, I * 8192 + G * 128, NTX>(t.srd_x, (I & 1) ? vx1 : t.vx0, sx, d4);
    else if constexpr (I < 6) dma<lwa(S) + (I & 1) * 1024, 0, NTW>(t.srd_w, (I & 1) ? vw1 : t.vw, sg + t.c0, d2);
    else if constexpr (I < 8) dma<lwa(S) + 8192 + (I & 1) * 1024, 0, NTW>(t.srd_w, (I & 1) ? vw1 : t.vw, sg + t.c1, d2);
    else dma<lwb(S) + (I & 1) * 1024, 0, NTW>(t.srd_w, (I & 1) ? vw1 : t.vw, sg + t.c2, d2);
  };
  constexpr auto rend = [](int G) { return G < 0 ? ND + 3 : G == 0 ? 2 * ND + 5 : G == 1 ? 3 * ND + 8 : G < 6 ? (G + 3) * ND : 8 * ND; };
  sfor<ND, rend(-1)>(ringf);
  load_tag(c, lane < 16 ? blk_of(t.rt, lane) : lane < 32 ? blk_of(lane & 15, t.ct) : lane < 40 ? m * 64 + (lane & 7) * 8 + ga : b,
           lane < 16 ? 64 + t.ct : lane < 32 ? 80 + t.rt : 32 + hd);
  BSrc b2, bo;
  b2.init(c, w2, 1792, 448);
  bo.init(c, wo, 512, 128);
  const u32 hh = lane >> 5, blk = (lane >> 4) & 1, q = (lane & 15) >> 2, p = lane & 3, r = lane & 31;
  init_ab(c);
  u32x4 bf[8][2];
  auto ldb = [&](auto NN) {
    constexpr int N = decltype(NN)::v;
    bf[N - 28][0] = rdb<N>(c, 0);
    bf[N - 28][1] = rdb<N>(c, 1);
  };
  u64 ps = 0, grs = 0;
  f32x4 gc[4];
  __builtin_amdgcn_s_setprio(1);
  HSTAMP(c, 1);
  WAITV(4);
  BAR();
  HSTAMP(c, 2);
  constexpr auto wo_in = [](int G) { return G == 3 ? 28 : G == 4 ? 29 : G == 6 ? 30 : G == 7 ? 31 : -1; };
  float sv[4], scs;
  u32 a_cf = L_ST + (m * 64 + (lane & 15) * 4 + (lane >> 4)) * 8, a_fm = L_ST + 4096 + (m * 32 + r) * 8 + hh * 4, a_lr = L_RS + m * 128 + r * 4;
  u32 a_rs = RS_OFF + (t.rt * 128 + m * 32 + r) * 8, a_cs = CS_OFF + t.ct * 16384 + (lane >> 4) * 4096 + m * 256 + (lane & 15) * 16;
  OPAQUE(a_cf); OPAQUE(a_fm); OPAQUE(a_lr); OPAQUE(a_rs); OPAQUE(a_cs);
  sfor<0, 8>([&](auto GG) {
    constexpr int G = decltype(GG)::v;
    constexpr int NRB = G == 6 ? 10 : G == 7 ? 2 * NB7 : 0;
    if constexpr (G == 5 || G == 6) ldb(IC<23 + G>{});
    if constexpr (G == SG + 1) {
      const u32 cv = *(LAS u32*)(L_ST + m * 2048 + lane * 4), rv = *(LAS u32*)(L_ST + ((lane >> 2) & 3) * 2048 + 1024 + (lane & 3) * 4);
      __builtin_amdgcn_raw_buffer_store_b32(cv, c.ws, lane * 4, CS_OFF + (t.ct * 16 + t.rt) * 1024 + m * 256, AUX);
      asm volatile("s_mov_b64 exec, %3\n\tglobal_store_dword %0, %1, %2 sc1\n\ts_mov_b64 exec, -1" ::"v"(RS_OFF + (t.rt * 128 + t.ct * 8) * 8 + (lane & 15) * 4),
                   "v"(rv), "s"(c.wsp), "s"(m == 0 ? 0xffffull : 0ull)
                   : "memory");
      set_tag(c);
    }
    if constexpr (G == SG + 2) publish(c, 0xffull << (8 * m));
    auto gather = [&]() {
      await(c, MB_OFF + b * 1024 + 512, 32, ps);
      gld64<0>(grs, a_rs, c.wsp);
      gld128<0>(gc[0], a_cs, c.wsp); gld128<1024>(gc[1], a_cs, c.wsp); gld128<2048>(gc[2], a_cs, c.wsp); gld128<3072>(gc[3], a_cs, c.wsp);
    };
    if constexpr (SG == 0 && G == 4) ps = peek(c, MB_OFF + b * 1024 + 512, 32);
    if constexpr (SG == 0 && G == 5) gather();
    sfor<rend(G - 1), rend(G)>([&](auto FF) {
      ringf(FF);
      constexpr int u = decltype(FF)::v - rend(-1);
      sfor<u * WN / WD, (u + 1) * WN / WD>(warm);
    });
    if constexpr (SG == 1 && G == 5) {
      WAITV(ND);
      gather();
    }
    if constexpr (G >= 6)
      sfor<(G - 6) * 5, (G - 6) * 5 + NRB / 2>([&](auto NN) {
        constexpr int N = decltype(NN)::v - SG;
        b2.issue<N + SG>(c);
        SB();
        if constexpr (N == 0) {
          if constexpr (SG == 1) WAITV(4);
#pragma unroll
          for (int k = 0; k < 4; k++) OPAQUE(gc[k]);
#pragma unroll
          for (int k = 0; k < 4; k++) {
            sv[k] = gc[0][k];
#pragma unroll
            for (int u = 1; u < 4; u++) {
              sv[k] += gc[u][k];
              OPAQUE(sv[k]);
            }
          }
        } else if constexpr (N == 1) {
          swap32(sv[0], sv[2]);
          swap32(sv[1], sv[3]);
          sv[0] += sv[2];
          OPAQUE(sv[0]);
          sv[1] += sv[3];
          auto sw16 = __builtin_amdgcn_permlane16_swap(__float_as_uint(sv[0]), __float_as_uint(sv[1]), false, false);
          scs = __uint_as_float(sw16[0]) + __uint_as_float(sw16[1]);
          OPAQUE(scs);
        } else if constexpr (N == 2) {
          const u32 h2 = pk(scs, scs);
          const float lo = scs - __uint_as_float(h2 & 0xffff0000u);
          *(LAS u32x2*)a_cf = u32x2{h2, pk(lo, lo)};
        } else if constexpr (N == 3) {
          OPAQUE(grs);
          *(LAS u32*)a_fm = (u32)grs;
          *(LAS u32*)a_lr = (u32)(grs >> 32);
        }
      });
    if constexpr (G >= 6) WAITL0();
    if constexpr (wo_in(G) > 0) bo.issue<wo_in(G)>(c);
    if constexpr (SG == 1 && G == 4) ps = peek(c, MB_OFF + b * 1024 + 512, 32);
    HSTAMP(c, 3 + 2 * G);
    if constexpr (G < 7) WAITV(G < 3 ? rend(G) - (G + 2) * ND : rend(G) - rend(G - 1) + NRB + (wo_in(G) > 0 ? 2 : 0) + (SG == 1 && G == 4 ? 1 : SG == 1 && G == 5 ? 5 : 0));
    if (G == 7 && !t.ffn) {
      const u32 awp = L_WPHI + 1024 + (4 * hh + q) * 32 + p * 8, sg = blk ? 0x80008000u : 0u;
      const u32x4 w0 = wphi_frag(awp, sg), w1 = wphi_frag(awp + 512, sg);
      const float sl = __uint_as_float((u32)(grs >> 32)) * LOG2E;
      sem_wait(SEM_K + m, 1);
      const u32 ak = ((m >> 1) ? L_SP2 : L_SP1) + (m & 1) * 2048 + lane * 16;
      const f32x16 P0 = mfma(w0, lds128(ak), f32x16{}), P1 = mfma(w1, lds128(ak + 1024), f32x16{});
      BAR();
      const u32 apw = lwa(1) + m * 2048 + r * 64 + ((hh ^ ((r >> 1) & 7)) << 3);
      phi_store(apw, P0, sl);
      phi_store(apw + 8192, P1, sl);
      WAITL0();
    }
    BAR();
    HSTAMP(c, 4 + 2 * G);
  });
  __builtin_amdgcn_s_setprio(2);
  const u32 mb = MB_OFF + b * 1024;
  const u32 fk = mb + 128, fo = mb + 256 + 64 * m, fq = mb + 768 + 16 * m;
  u64 pq = peek(c, fq, 2);
  WAITV(1);
  ldb(IC<30>{});
  ldb(IC<31>{});
  sem_post(SEM_B + m);
  WAITL0();
  asm volatile("" : "+s"(wd));
  bo.issue<32>(c);
  bo.issue<33>(c);
  WAITV(4);
  STAMP(c, 20);
  await(c, fq, 2, pq);
  STAMP(c, 24);
  u32x4 p00, p01, p10, p11;
  u64 pkv;
  float rs0, rs1;
  {
    u32x4 qf[2];
#pragma unroll
    for (int u = 0; u < 2; u++) qf[u] = ld128(c.ws, lane * 16, QF_OFF + ((8 * ga + 2 * m + u) * 32 + hd) * 1024);
    rs0 = __uint_as_float(__builtin_amdgcn_raw_buffer_load_b32(c.ws, lane * 8 + 4, RS_OFF + (8 * ga + 2 * m) * 256, AUX));
    OPAQUE(rs0);
    SB();
    ldb(IC<32>{});
    ldb(IC<33>{});
    WAITL0();
    bo.issue<34>(c);
    bo.issue<35>(c);
    rs1 = rs0;
    swap32(rs0, rs1);
    const u32x4 wp = wphi_frag(L_WPA + (hd & 1) * 512 + (4 * hh + q) * 32 + p * 8, blk ? 0x80008000u : 0u);
    const f32x16 Q0 = mfma(wp, qf[0], f32x16{}), Q1 = mfma(wp, qf[1], f32x16{});
    dual_softmax(Q0, rs0 * LOG2E, p00, p01);
    OPAQUE(p00); OPAQUE(p01);
    SB();
    pkv = peek(c, fk, 16);
    dual_softmax(Q1, rs1 * LOG2E, p10, p11);
    OPAQUE(p10); OPAQUE(p11);
  }
  STAMP(c, 21);
  WAITV(0);
  ldb(IC<34>{});
  ldb(IC<35>{});
  sem_post(SEM_B + m);
  await(c, fk, 16, pkv);
  STAMP(c, 25);
  {
    f32x4 part[8];
#pragma unroll
    for (int s = 0; s < 8; s++) part[s] = __builtin_bit_cast(f32x4, ld128(c.ws, hh * 2048 + r * 16, KV_OFF + (hd * 16 + 2 * s) * 2048 + m * 512));
    f32x4 z = __builtin_bit_cast(f32x4, ld128(c.ws, (lane & 15) * 128 + blk * 16, KZ_OFF + hd * 2048 + m * 32));
    SB();
    float v[4] = {part[0][0], part[0][1], part[0][2], part[0][3]};
#pragma unroll
    for (int s = 1; s < 8; s++)
#pragma unroll
      for (int k = 0; k < 4; k++) {
        v[k] += part[s][k];
        OPAQUE(v[k]);
      }
    swap32(v[0], v[2]);
    swap32(v[1], v[3]);
    v[0] += v[2];
    OPAQUE(v[0]);
    *(LAS u32*)(L_WPHI + r * 32 + m * 8 + hh * 4) = pk(v[0], v[1] + v[3]);
    float za = hh ? z[2] : z[0], zb = hh ? z[3] : z[1];
    sfor<0, 4>([&](auto KK) {
      za += __int_as_float(__builtin_amdgcn_update_dpp(0, __float_as_int(za), 0x120 + (8 >> decltype(KK)::v), 0xf, 0xf, false));
      zb += __int_as_float(__builtin_amdgcn_update_dpp(0, __float_as_int(zb), 0x120 + (8 >> decltype(KK)::v), 0xf, 0xf, false));
    });
    *(LAS u32*)(L_WPHI + (32 + blk) * 32 + m * 8 + hh * 4) = pk(za, zb);
    sem_post(SEM_A);
    sem_wait(SEM_A, 4);
    const u32 e = blk == 0 ? hh * 16 + (lane & 15) : 32 + hh;
    const u32x4 k0 = lds128(L_WPHI + e * 32), k1 = lds128(L_WPHI + e * 32 + 16);
    f32x16 Z0 = mfma(k0, p00, f32x16{}), Z1 = mfma(k0, p10, f32x16{});
    Z0 = mfma(k1, p01, Z0);
    Z1 = mfma(k1, p11, Z1);
    constexpr int NO = 0;
    const float r0 = __builtin_amdgcn_rcpf(mul1(Z0[8 - NO] + 1e-6f, rs0)), r1 = __builtin_amdgcn_rcpf(mul1(Z1[8 - NO] + 1e-6f, rs1));
    u32x4 o0, o1;
#pragma unroll
    for (int d = 0; d < 4; d++) {
      o0[d] = pk(mul1(Z0[NO + 2 * d], r0), mul1(Z0[NO + 2 * d + 1], r0));
      o1[d] = pk(mul1(Z1[NO + 2 * d], r1), mul1(Z1[NO + 2 * d + 1], r1));
    }
    st128(o0, c.ws, lane * 16, OF_OFF + ((8 * ga + 2 * m) * 32 + hd) * 1024);
    st128(o1, c.ws, lane * 16, OF_OFF + ((8 * ga + 2 * m + 1) * 32 + hd) * 1024);
  }
  STAMP(c, 26);
  WAITV(0);
  publish(c, 0xff00000000ull);
  STAMP(c, 27);
  const u32 orow = 64 * i + 32 * (m >> 1) + r, oidx = orow * 512 + 64 * ((b >> 3) & 7) + 32 * (m & 1) + 8 * hh;
  u32x4 xr[2] = {*(const GAS u32x4*)(x + oidx), *(const GAS u32x4*)(x + oidx + 16)};
  float frs = __uint_as_float(__builtin_amdgcn_raw_buffer_load_b32(c.ws, orow * 8 + 4, RS_OFF, AUX));
  u64 po = peek(c, fo, 8);
  u32x4 sel[2];
  {
    const u32 one = (((r >> 2) & 1) == hh) ? ((r & 1) ? 0x3f800000u : 0x3f80u) : 0u, dw = ((r >> 3) & 1) * 2 + ((r >> 1) & 1);
#pragma unroll
    for (int d = 0; d < 4; d++) {
      sel[0][d] = (dw == (u32)d && blk == 0) ? one : 0u;
      sel[1][d] = (dw == (u32)d && blk == 1) ? one : 0u;
    }
    OPAQUE(sel[0]); OPAQUE(sel[1]);
  }
  WAITV(0);
  OPAQUE(xr[0]); OPAQUE(xr[1]); OPAQUE(frs);
  await(c, fo, 8, po);
  STAMP(c, 30);
  u32x4 af[8][2];
#pragma unroll
  for (int n = 0; n < 8; n++)
#pragma unroll
    for (int u = 0; u < 2; u++) af[n][u] = ld128(c.ws, lane * 16, OF_OFF + ((2 * i + ((m >> 1) ^ u)) * 32 + 8 * m + n) * 1024);
  SB();
  f32x16 acc[4];
  constexpr int NSM = 4;
  sfor<0, NSM>([&](auto NN) {
    constexpr int N = decltype(NN)::v;
    if constexpr (N == 0) {
      mma0(acc[0], bf[N][0], af[N][0]); mma0(acc[1], bf[N][1], af[N][0]); mma0(acc[2], bf[N][0], af[N][1]); mma0(acc[3], bf[N][1], af[N][1]);
    } else {
      mma(acc[0], bf[N][0], af[N][0]); mma(acc[1], bf[N][1], af[N][0]); mma(acc[2], bf[N][0], af[N][1]); mma(acc[3], bf[N][1], af[N][1]);
    }
  });
  sem_wait(SEM_B + m, 3);
  SB();
  sfor<0, 4>([&](auto KK) {
    constexpr int k = 3 - decltype(KK)::v;
    sfor<NSM, 8>([&](auto NN) {
      constexpr int N = decltype(NN)::v;
      mma(acc[k], bf[N][k & 1], af[N][k >> 1]);
      if constexpr (N == NSM + 1 && k < 3) {
        SB();
        put_block<10 + k>(c, acc[k + 1]);
        if constexpr (k == 0) sem_post(SEM_H);
        SB();
      }
    });
  });
  STAMP(c, 31);
  sem_wait(SEM_H, 4);
  STAMP(c, 32);
  {
    u32x4 f[7][2];
    get_block<10>(c, m ^ 1, f[0]);
    get_block<11>(c, m ^ 2, f[1]);
    get_block<12>(c, m ^ 3, f[2]);
#pragma unroll
    for (int k = 0; k < 3; k++) { mma(acc[0], sel[0], f[k][0]); mma(acc[0], sel[1], f[k][1]); }
    sem_wait(SEM_C, 4);
    STAMP(c, 33);
    get_block<0>(c, m, f[3]);
    get_block<1>(c, m ^ 1, f[4]);
    get_block<2>(c, m ^ 2, f[5]);
    get_block<3>(c, m ^ 3, f[6]);
#pragma unroll
    for (int k = 3; k < 7; k++) { mma(acc[0], sel[0], f[k][0]); mma(acc[0], sel[1], f[k][1]); }
  }
  if (threadIdx.x == 319) __hip_atomic_store((GAS u64*)(c.wsp + EP_OFF + b * 8), c.tag, __ATOMIC_RELAXED, __HIP_MEMORY_SCOPE_AGENT);
  asm volatile("s_nop 15" : "+v"(acc[0]));
#pragma unroll
  for (int h = 0; h < 2; h++) {
    float e[8];
#pragma unroll
    for (int k = 0; k < 8; k++) e[k] = acc[0][8 * h + k];
#pragma unroll
    for (int k = 0; k < 4; k++) swap32(e[k], e[4 + k]);
    u32x4 o;
#pragma unroll
    for (int d = 0; d < 4; d++)
      o[d] = pk(__builtin_fmaf(e[2 * d], frs, __uint_as_float(xr[h][d] << 16)), __builtin_fmaf(e[2 * d + 1], frs, __uint_as_float(xr[h][d] & 0xffff0000u)));
    __builtin_amdgcn_raw_buffer_store_b128(o, __builtin_amdgcn_make_buffer_rsrc(out, 0, 2048 * 1024, 0x00020000), oidx * 2 + 32 * h, 0, OAUX);
  }
}

template <bool FFN> struct Slots {
  static constexpr int NBK = FFN ? 7 : 6, N7 = 28 * NBK, NS = N7 + 5 * NBK;
  static constexpr int blk(int n) {
    if (n < N7) return n % NBK;
    const int k = (n - N7) / 5;
    return (!FFN && k < 2) ? 1 - k : k;
  }
  static constexpr int step(int n) { return n < N7 ? n / NBK : ((n - N7) % 5 == 0 ? -1 : 27 + (n - N7) % 5); }
  static constexpr int bar(int G) { return (G < 7 ? (4 * G + 3) * NBK : NS - NBK) + 1; }
};
template <bool FFN>
DI void compute1(Ctx& c, const Tile& t) {
  u32x4 xa, xb;
  using SL = Slots<FFN>;
  constexpr int NBK = SL::NBK;
  const u32 lane = c.lane, m = c.m;
  const int rt = t.rt, j = t.j;
  const u32 hh = lane >> 5, blk = (lane >> 4) & 1, q = (lane & 15) >> 2, p = lane & 3, r = lane & 31;
  u32 ax[4], aw[2][2], ac;
#pragma unroll
  for (int s = 0; s < 4; s++) ax[s] = m * 4096 + r * 128 + (((2 * s + hh) ^ ((r >> 1) & 7)) << 4);
  const u32 at = (((2 * blk + (p >> 1)) ^ ((q >> 1) << 2)) << 4) + 8 * (p & 1);
  {
    const u32 a = (8 * hh + q) * 128 + at;
    aw[0][0] = a + LWA; aw[0][1] = (a ^ 64) + LWA; aw[1][0] = a + LWB; aw[1][1] = (a ^ 64) + LWB;
    ac = (8 * hh + q) * 64 + 32 * blk + 8 * p + LWB + 8192;
    OPAQUE(aw[0][0]); OPAQUE(aw[0][1]); OPAQUE(aw[1][0]); OPAQUE(aw[1][1]); OPAQUE(ac);
  }
  u32 acs = L_ST + r * 8;
  OPAQUE(acs);
  auto rdw = [&](auto NN) -> u32x4 {
    constexpr int T = SL::step(decltype(NN)::v), M = SL::blk(decltype(NN)::v);
    if constexpr (T < 0) {
      const u32x2 v = *(LAS u32x2*)(acs + M * 256);
      return __builtin_shufflevector(v, v, 0, 1, -1, -1);
    }
    else {
      constexpr int S = (T >> 2) % 3, s = T & 3;
      if constexpr (M < 6) {
        const u32 a = (M < 4 ? aw[0][M & 1] + S * 16384 + (M >> 1) * 8192 : aw[1][M & 1] + S * 12288) + s * 2048;
        return trd2(a, a + 512);
      } else {
        const u32 a = ac + S * 12288 + s * 1024;
        return trd2(a, a + 256);
      }
    }
  };
  u32x4 xf[4], wf[2 * NBK];
  u32x2 fm;
  f32x16 acc[NBK], P0, P1;
  float rstd;
  u32x2 sv[8];
  u32x4 eo;
  float sa;
  const u32 tb = rt * 4 + m;
  const u32 obase = FFN ? HF_OFF + tb * 112 * 1024 : QF_OFF + (tb * 32 + 4 * j) * 1024;
  WAITL0();
  STAMP(c, 1);
  WAITV(NCP);
  BAR();
  STAMP(c, 2);
  xf[0] = lds128(ax[0]);
  sfor<0, NBK>([&](auto NN) { wf[decltype(NN)::v] = rdw(NN); });
  SB();
  auto filler = [&](auto NN) {
    constexpr int n = decltype(NN)::v;
    if constexpr (n == SL::bar(SG) - 5) {
      WAITV(0);
      xa = lds128(L_ST + m * 2048 + lane * 16);
      xb = lds128(L_ST + m * 2048 + 1024 + lane * 16);
      const u32 a = LBS + m * 4096 + (4 * hh + q) * 128 + at;
#pragma unroll
      for (int k = 0; k < 8; k++) sv[k] = trd((a ^ ((k >> 2) * 64)) + (k & 3) * 1024);
      set_tag(c);
    }
    if constexpr (n == SL::bar(SG) - 2) {
      u32 ones = ONES2;
      asm volatile("" : "+s"(ones));
      float st[4] = {0.f, 0.f, 0.f, 0.f}, cs[2] = {0.f, 0.f};
#pragma unroll
      for (int d = 0; d < 4; d++) {
        st[0] = dot2(ones, xa[d], st[0]);
        st[1] = dot2(xa[d], xa[d], st[1]);
        st[2] = dot2(ones, xb[d], st[2]);
        st[3] = dot2(xb[d], xb[d], st[3]);
      }
#pragma unroll
      for (int d = 0; d < 8; d++) {
        cs[0] = dot2(ones, sv[d >> 1][d & 1], cs[0]);
        cs[1] = dot2(ones, sv[4 + (d >> 1)][d & 1], cs[1]);
      }
      swap32(st[0], st[1]);
      swap32(st[2], st[3]);
      swap32(cs[0], cs[1]);
      *(LAS float*)(L_ST + m * 2048 + lane * 4) = cs[0] + cs[1];
      st[0] += st[1];
      OPAQUE(st[0]);
      st[2] += st[3];
      auto sw16 = __builtin_amdgcn_permlane16_swap(__float_as_uint(st[0]), __float_as_uint(st[2]), false, false);
      sa = __uint_as_float(sw16[0]) + __uint_as_float(sw16[1]);
      OPAQUE(sa);
    }
    if constexpr (n == SL::bar(SG) - 1) {
      sfor<0, 4>([&](auto KK) { sa += __int_as_float(__builtin_amdgcn_update_dpp(0, __float_as_int(sa), 0x120 + (8 >> decltype(KK)::v), 0xf, 0xf, false)); });
      float mean = sa * (1.f / 512.f), ms = mean;
      swap32(mean, ms);
      ms = __builtin_amdgcn_rsqf(mx(__builtin_fmaf(-mean, mean, ms), 0.f) + 1e-5f);
      const u32 mh = pk(-mean, -mean);
      const float ml = -mean - __uint_as_float(mh & 0xffff0000u);
      *(LAS u32x2*)(L_ST + m * 2048 + 1024 + (((lane >> 4) & 1) | ((lane & 15) << 1) | (lane & 32)) * 8) = u32x2{(mh & 0xffffu) | (pk(ml, ml) & 0xffff0000u), __float_as_uint(ms)};
    }
    if constexpr (n >= SL::N7 + 5) {
      constexpr int g = (n - SL::N7) % 5, M = SL::blk(n - 5);
      if constexpr (g >= 1) {
        constexpr int s = (g - 1) >> 1, h = (g - 1) & 1;
        if constexpr (FFN || M < 4) {
          eo[2 * h] = pk2(pr<8 * s + 4 * h>(acc[M]));
          eo[2 * h + 1] = pk2(pr<8 * s + 4 * h + 2>(acc[M]));
          if constexpr (FFN) {
            eo[2 * h] = relu2(eo[2 * h]);
            eo[2 * h + 1] = relu2(eo[2 * h + 1]);
          }
          if constexpr (h == 0) OPAQUE(eo);
        }
        if constexpr (h == 1) {
          if constexpr (FFN) st128(eo, c.ws, lane * 16, obase + ((t.c0 >> 5) + 2 * M + s) * 1024);
          else if constexpr (M == 1) {
            *(LAS u32x4*)(((m >> 1) ? L_SP2 : L_SP1) + (m & 1) * 2048 + s * 1024 + lane * 16) = eo;
            if constexpr (s == 1) sem_post(SEM_K + m);
          } else if constexpr (M == 0) {
            const u32x4 wp = wphi_frag(L_WPHI + s * 512 + (4 * hh + q) * 32 + p * 8, blk ? 0x80008000u : 0u);
            asm volatile("s_nop 1\n\tv_mfma_f32_32x32x16_bf16 %0, %1, %2, 0" : "=&v"(s ? P1 : P0) : "v"(wp), "v"(eo));
          } else if constexpr (M < 4) st128(eo, c.ws, lane * 16, obase + (2 * (M - 2) + s) * 1024);
        }
      }
    }
  };
  sfor<0, SL::NS>([&](auto NN) {
    constexpr int n = decltype(NN)::v, T = SL::step(n), M = SL::blk(n);
    constexpr bool TR = !FFN && M >= 4;
    const u32x4 xo = xf[T & 3], wo_ = wf[n % (2 * NBK)];
    if constexpr (T < 0) {
      const u32x2 cf = __builtin_shufflevector(wo_, wo_, 0, 1);
      mma8(acc[M], TR ? fm : cf, TR ? cf : fm);
    } else if constexpr (T == 0) mma0(acc[M], TR ? xo : wo_, TR ? wo_ : xo);
    else mma(acc[M], TR ? xo : wo_, TR ? wo_ : xo);
    constexpr int G = n < SL::N7 ? n / (4 * NBK) : 7;
    if constexpr (n == SL::bar(G)) {
      WAITL0();
      STAMP(c, 3 + 2 * G);
      BAR();
      STAMP(c, 4 + 2 * G);
    }
    if constexpr (n + NBK < SL::NS && n != SL::bar(G) - 1) {
      if constexpr (n < SL::N7 && (n == SL::bar(G) || (M == 0 && (T & 3) != 3))) {
        if constexpr (T == 27) {
          fm = *(LAS u32x2*)(hh ? L_ZERO : L_ST + 4096 + (m * 32 + r) * 8);
          xf[0] = lds128(ax[0] + 16384); xf[1] = lds128(ax[1] + 16384); xf[2] = lds128(ax[2] + 16384);
        } else xf[(T + 1) & 3] = lds128(ax[(T + 1) & 3] + (((T + 1) >> 2) % 3) * 16384);
      }
      if constexpr (n == SL::bar(G)) wf[(n - 1 + NBK) % (2 * NBK)] = rdw(IC<n - 1 + NBK>{});
      wf[(n + NBK) % (2 * NBK)] = rdw(IC<n + NBK>{});
      if constexpr (n == SL::N7) {
        xf[3] = lds128(ax[3] + 16384);
        if constexpr (!FFN) rstd = *(LAS float*)(L_RS + m * 128 + r * 4);
      }
    }
    filler(NN);
    SB();
  });
  __builtin_amdgcn_s_setprio(3);
  if constexpr (FFN) {
    asm volatile("s_nop 15" : "+v"(acc[6]));
    sfor<0, 2>([&](auto SS) {
      u32x4 o = pack8<decltype(SS)::v>(acc[6]);
#pragma unroll
      for (int d = 0; d < 4; d++) o[d] = relu2(o[d]);
      st128(o, c.ws, lane * 16, obase + ((t.cc >> 5) + decltype(SS)::v) * 1024);
    });
  } else {
    const u32 apw = lx(1) + m * 2048 + r * 64 + ((hh ^ ((r >> 1) & 7)) << 3);
    phi_store(apw, P0, rstd * LOG2E);
    SB();
    WAITV(0);
    publish(c, 0xf00ull);
    const u32 av0 = L_ST + m * 2048 + lane * 16, av1 = lwb(1) + m * 2048 + lane * 16;
    sfor<0, 2>([&](auto TT) {
      constexpr int tt = decltype(TT)::v;
      const f32x4 ra = ldsf4(L_RS + m * 128 + (16 * tt + 4 * hh) * 4), rb = ldsf4(L_RS + m * 128 + (16 * tt + 8 + 4 * hh) * 4);
      const f32x4 a0 = q4<8 * tt>(acc[4]) * ra, b0 = q4<8 * tt + 4>(acc[4]) * rb, a1 = q4<8 * tt>(acc[5]) * ra, b1 = q4<8 * tt + 4>(acc[5]) * rb;
      *(LAS u32x4*)(av0 + tt * 1024) = u32x4{pk(a0[0], a0[1]), pk(a0[2], a0[3]), pk(b0[0], b0[1]), pk(b0[2], b0[3])};
      *(LAS u32x4*)(av1 + tt * 1024) = u32x4{pk(a1[0], a1[1]), pk(a1[2], a1[3]), pk(b1[0], b1[1]), pk(b1[2], b1[3])};
    });
    phi_store(apw + 8192, P1, rstd * LOG2E);
    WAITL0();
    STAMP(c, 19);
    BAR();
    const u32 t0 = 4 * hh + q;
    const u32 apr = ((m >> 1) ? lwa(1) : lx(1)) + (m & 1) * 8192 + t0 * 64 + (((4 * blk + p) ^ (t0 >> 1)) << 3);
    const u32 avr = ((m >> 1) ? lwb(1) : L_ST) + lane * 16;
    const bool isv = blk == (m & 1);
    u32x4 pa[8], vb[8];
#pragma unroll
    for (int k = 0; k < 8; k++) {
      pa[k] = trd2(apr + k * 1024, (apr ^ 32) + k * 1024 + 512);
      vb[k] = lds128(avr + k * 1024);
    }
    sem_post(SEM_KV);
    f32x16 X = f32x16{};
#pragma unroll
    for (int k = 0; k < 8; k++) {
#pragma unroll
      for (int d = 0; d < 4; d++) vb[k][d] = isv ? vb[k][d] : ONES2;
      X = mfma(pa[k], vb[k], X);
    }
    const u32 pi = (4 * j + m) * 16 + rt;
    const u32 ve = isv ? KV_OFF + pi * 2048 + (hh * 16 + (lane & 15)) * 16 : KZ_OFF + pi * 128 + hh * 16, vs = isv ? 512 : 32;
    st128(__builtin_bit_cast(u32x4, q4<0>(X)), c.ws, ve, 0);
    st128(__builtin_bit_cast(u32x4, q4<4>(X)), c.ws, ve + vs, 0);
    st128(__builtin_bit_cast(u32x4, q4<8>(X)), c.ws, ve + 2 * vs, 0);
    st128(__builtin_bit_cast(u32x4, q4<12>(X)), c.ws, ve + 3 * vs, 0);
  }
  STAMP(c, 20);
  __builtin_amdgcn_s_setprio(0);
  if constexpr (!FFN) {
    WAITV(0);
    publish(c, 0xffull);
    sem_wait(SEM_KV, 4);
  }
}
DI void compute2(Ctx& c, bool ffn, const u16* w2) {
  const u32 lane = c.lane, m = c.m;
  const int b = blockIdx.x, i = (b & 7) * 4 + (b >> 6);
  init_ab(c);
  BSrc b2;
  b2.init(c, w2, 1792, 448);
  auto issue = [&](auto NN) { b2.issue<decltype(NN)::v>(c); };
  u32x4 af[28][2], bf[28][2];
  auto ldb = [&](auto NN) {
    constexpr int N = decltype(NN)::v;
    bf[N][0] = rdb<N>(c, 0);
    bf[N][1] = rdb<N>(c, 1);
  };
  sfor<5 + NB7, 10>(issue);
  sfor<10, 10 + NB7 + 2>(issue);
  WAITV(14);
  publish(c, ffn ? 0xffull : 0ull);
  STAMP(c, 23);
  sem_wait(SEM_B + m, 1);
  sfor<0, 5>(ldb);
  sfor<10 + NB7 + 2, 16>(issue);
  WAITL0();
  sfor<16, 21>(issue);
  WAITV(22);
  sfor<5, 10>(ldb);
  const u32 fh = MB_OFF + b * 1024 + 32 * m;
  u64 ph = peek(c, fh, 4);
  WAITL0();
  sfor<21, 26>(issue);
  STAMP(c, 24);
  WAITV(10);
  await(c, fh, 4, ph);
  asm volatile("" : "+s"(c.rot));
  STAMP(c, 25);
  const u32 ah = HF_OFF + ((2 * i) * 112 + 28 * m) * 1024;
  const u32 va[2] = {lane * 16 + (m >> 1) * 114688, lane * 16 + ((m >> 1) ^ 1) * 114688};
  auto lda = [&](auto NN, int u) {
    constexpr int N = decltype(NN)::v;
    if constexpr (N < 28) af[N][u] = ld128(c.ws, va[u], ah + hstep<N>(c) * 1024);
  };
  sfor<0, CW>([&](auto NN) { lda(NN, 0); lda(NN, 1); });
  SB();
  STAMP(c, 27);
  f32x16 ac2[4];
  sfor<0, 24>([&](auto NN) {
    constexpr int n = decltype(NN)::v;
    sfor<0, 4>([&](auto KK) {
      constexpr int k = decltype(KK)::v;
      if constexpr (n == 0) mma0(ac2[k], bf[n][k & 1], af[n][k >> 1]);
      else mma(ac2[k], bf[n][k & 1], af[n][k >> 1]);
      SB();
      if constexpr ((k & 1) == 1 && n + CW < 28) lda(IC<n + CW>{}, k >> 1);
      if constexpr ((k & 1) == 0 && n >= 6) bf[n + 4][k >> 1] = rdb<n + 4>(c, k >> 1);
      if constexpr (k == 3 && n == 8) {
        sem_wait(SEM_B + m, 2);
        sfor<26, 28>(issue);
      }
      if constexpr (k == 3 && n == 11) sem_post(SEM_B + m);
      SB();
    });
  });
  STAMP(c, 31);
  sfor<0, 4>([&](auto KK) {
    constexpr int k = decltype(KK)::v;
    sfor<24, 28>([&](auto NN) {
      constexpr int n = decltype(NN)::v;
      mma(ac2[k], bf[n][k & 1], af[n][k >> 1]);
      if constexpr (n == 25 && k > 0) {
        SB();
        put_block<k - 1>(c, ac2[k - 1]);
        SB();
      }
    });
  });
  asm volatile("s_nop 15" : "+v"(ac2[3]));
  put_block<3>(c, ac2[3]);
  sem_post(SEM_C);
  STAMP(c, 32);
}

#define ROTB 1
__global__ __launch_bounds__(512, 2) void fused_k(const u16* x, const u16* wqkv, const u16* wphi, const u16* wo, const u16* w1, const u16* w2, u16* out,
                                                  char* ws) {
  __shared__ __attribute__((aligned(16))) char smem[163840];
  asm volatile("" ::"v"((LAS char*)smem));
  const int b = blockIdx.x, xcd = b & 7, r = b >> 3;
  u64 pc0;
  asm volatile("s_getpc_b64 %0" : "=s"(pc0));
  {
    u32 off = ((r * 8 + (threadIdx.x >> 6)) * 3 + (threadIdx.x & 3)) * 64;
    off = off < CODE_BYTES ? off : 0;
    asm volatile("s_mov_b32 m0, %2\n\ts_mov_b64 exec, 7\n\tglobal_load_lds_dword %0, %1\n\ts_mov_b64 exec, -1" ::"v"(off), "s"(pc0), "i"(L_RS) : "memory");
  }
  Ctx c;
  c.dbg = 0;
  STAMP(c, 0);
  c.lane = threadIdx.x & 63;
  const u32 w = __builtin_amdgcn_readfirstlane(threadIdx.x >> 6);
  c.m = w & 3;
  c.ws = __builtin_amdgcn_make_buffer_rsrc(ws, 0, WS_BYTES, 0x00020000);
  c.wsp = ws;
  c.rot = ROTB == 0 ? 0 : ROTB == 1 ? (b >> 6) * 7 : ((b >> 6) * 7 + (((b >> 3) & 7) * 7 >> 1)) % 28;
  const int rt = (xcd >> 2) * 8 + (r >> 2), ct = (xcd & 3) * 4 + (r & 3), j = ct >> 1;
  Tile t;
  t.rt = rt;
  t.j = j;
  t.ct = ct;
  t.ffn = ct & 1;
  t.ldw = t.ffn ? 3584 : 3072;
  const u32 lane = c.lane, m = c.m;
  t.srd_x = make_srd(x, 2048 * 512 * 2);
  t.srd_w = make_srd(t.ffn ? w1 : wqkv, 512 * t.ldw);
  t.vx0 = (lane >> 3) * 1024 + (((lane & 7) ^ (lane >> 4)) << 4);
  {
    const u32 sx = (rt * 128 + m * 32 + (w >> 2) * 16) * 1024, dx = m * 4096 + (w >> 2) * 2048;
    dma<LX, 0, NTX>(t.srd_x, t.vx0, sx, dx);
    dma<LX + 1024, 8192, NTX>(t.srd_x, t.vx0 ^ 64, sx, dx);
    t.vw = (lane >> 3) * t.ldw + (((lane & 7) ^ (((lane >> 4) & 1) << 2)) << 4);
    t.c0 = t.ffn ? 448 * j + (j & 1) * 64 : 1024 + 128 * j;
    t.c1 = t.ffn ? t.c0 + 128 : 128 * j;
    t.c2 = t.ffn ? t.c0 + 256 : 2048 + 128 * j;
    t.cc = t.ffn ? 448 * j + ((j & 1) ? 0 : 384) : 0;
    dma<LWA, 0, NTW>(t.srd_w, t.vw, w * 8 * t.ldw + t.c0, w * 1024);
    dma<LWA + 8192, 0, NTW>(t.srd_w, t.vw, w * 8 * t.ldw + t.c1, w * 1024);
    dma<LWB, 0, NTW>(t.srd_w, t.vw, w * 8 * t.ldw + t.c2, w * 1024);
    if (t.ffn && w >= 4) dma<LWB + 8192, 0, NTW>(t.srd_w, (lane >> 2) * 3584 + ((lane & 3) << 4), m * 16 * 3584 + t.cc, m * 1024);
    BAR();
    SB();
  }
  if (__builtin_expect(w >= 4, 1)) {
    helper(c, t, x, wo, w2, out);
  } else {
    {
      const u32 vs = m < 3 ? t.vw : (lane >> 3) * t.ldw + ((lane & 3) << 4);
      const u32 so = rt * 32 * t.ldw + (m == 0 ? t.c0 : m == 1 ? t.c1 : m == 2 ? t.c2 : t.cc);
      dma<LBS, 0, NTS>(t.srd_w, vs, so, m * 4096);
      dma<LBS + 1024, 0, NTS>(t.srd_w, vs, so + 8 * t.ldw, m * 4096);
      dma<LBS + 2048, 0, NTS>(t.srd_w, vs, so + 16 * t.ldw, m * 4096);
      dma<LBS + 3072, 0, NTS>(t.srd_w, vs, so + 24 * t.ldw, m * 4096);
      dma<L_ST, 0>(t.srd_x, lane * 16, (rt * 128 + ct * 8 + m * 2) * 1024, m * 2048);
      dma<L_ST + 1024, 1024>(t.srd_x, lane * 16, (rt * 128 + ct * 8 + m * 2) * 1024, m * 2048);
      dma<L_WPHI, 0>(make_srd(wphi, 32 * 512), lane * 16, m < 2 ? (u32)(j * 2048 + m * 1024) : (u32)(((b & 7) * 2 + ((b >> 4) & 1)) * 1024), (m < 2 ? m : 2) * 1024);
      const u32 ii = 2 * rt + (m >> 1), l7 = lane & 7;
      const u32 t1 = t.ffn ? (ii & 3) * 64 + (ii >> 2) + l7 * 8 : l7 * 32 + m * 8 + j, f1 = t.ffn ? j * 2 + (m & 1) : 16 + rt;
      load_tag(c, lane < 8 ? t1 : lane < 12 ? (rt >> 1) * 32 + (lane & 3) * 8 + j : b, lane < 8 ? f1 : 96 + (rt & 1) * 4 + m);
      *(LAS u32*)(L_SEM + (lane & 31) * 4) = 0;
    }
    if (t.ffn) compute1<true>(c, t);
    else compute1<false>(c, t);
    compute2(c, t.ffn, w2);
  }
}

extern "C" void kernel_launch(void* const* d_in, const int* in_sizes, int n_in, void* d_out, int out_size, void* d_ws, size_t ws_size,
                              hipStream_t stream) {
  const u16 *x = (const u16*)d_in[0], *wqkv = (const u16*)d_in[1], *wphi = (const u16*)d_in[2], *wo = (const u16*)d_in[3];
  const u16 *w1 = (const u16*)d_in[5], *w2 = (const u16*)d_in[6];
  fused_k<<<256, 512, 0, stream>>>(x, wqkv, wphi, wo, w1, w2, (u16*)d_out, (char*)d_ws);
}
```

```cpp
#include <hip/hip_runtime.h>
#pragma clang diagnostic ignored "-Wint-to-pointer-cast"
typedef unsigned short u16;
typedef unsigned u32;
typedef unsigned long long u64;
typedef u32 u32x2 __attribute__((ext_vector_type(2)));
typedef u32 u32x4 __attribute__((ext_vector_type(4)));
typedef short s16x2 __attribute__((ext_vector_type(2)));
typedef short s16x4 __attribute__((ext_vector_type(4)));
typedef float f32x2 __attribute__((ext_vector_type(2)));
typedef float f32x4 __attribute__((ext_vector_type(4)));
typedef float f32x16 __attribute__((ext_vector_type(16)));
typedef __bf16 b16x2 __attribute__((ext_vector_type(2)));
typedef __bf16 b16x8 __attribute__((ext_vector_type(8)));
#define LAS __attribute__((address_space(3)))
#define GAS __attribute__((address_space(1)))
#define DI __device__ __forceinline__
#define SB() __builtin_amdgcn_sched_barrier(0)
#define BAR() __builtin_amdgcn_s_barrier()
#define WAITV(n) do { asm volatile("s_waitcnt vmcnt(%0)" ::"i"(n) : "memory"); SB(); } while (0)
#define WAITL0() asm volatile("s_waitcnt lgkmcnt(0)" ::: "memory")
#define OPAQUE(x) asm volatile("" : "+v"(x))

constexpr u32 HF_OFF = 0;
constexpr u32 QF_OFF = 64u * 112u * 1024u;
constexpr u32 OF_OFF = QF_OFF + 64u * 32u * 1024u;
constexpr u32 KV_OFF = OF_OFF + 64u * 32u * 1024u;
constexpr u32 KZ_OFF = KV_OFF + 32u * 16u * 2048u;
constexpr u32 CS_OFF = KZ_OFF + 32u * 16u * 128u;
constexpr u32 RS_OFF = CS_OFF + 256u * 1024u;
constexpr u32 MB_OFF = RS_OFF + 2048u * 8u;
constexpr u32 EP_OFF = MB_OFF + 256u * 1024u;
constexpr u32 WS_BYTES = EP_OFF + 2048u;
constexpr u32 LX = 0, LWA = 49152, LWB = 98304, LBS = 135168, LSCR = 159744;
constexpr u32 lx(int s) { return LX + s * 16384; }
constexpr u32 lwa(int s) { return LWA + s * 16384; }
constexpr u32 lwb(int s) { return LWB + s * 12288; }
constexpr u32 L_WPHI = LSCR, L_WPA = LSCR + 2048, L_RS = LSCR + 3072, L_SEM = LSCR + 3584, L_ZERO = LSCR + 3648;
constexpr u32 L_SP1 = lwb(1) + 8192, L_SP2 = lwb(2) + 8192;
constexpr u32 L_ST = LBS + 16384;
constexpr int AUX = 16;

DI f32x16 mfma(u32x4 a, u32x4 b, f32x16 c) {
  return __builtin_amdgcn_mfma_f32_32x32x16_bf16(__builtin_bit_cast(b16x8, a), __builtin_bit_cast(b16x8, b), c, 0, 0, 0);
}
DI void mma(f32x16& d, u32x4 a, u32x4 b) { asm volatile("v_mfma_f32_32x32x16_bf16 %0, %1, %2, %0" : "+v"(d) : "v"(a), "v"(b)); }
DI void mma0(f32x16& d, u32x4 a, u32x4 b) { asm volatile("v_mfma_f32_32x32x16_bf16 %0, %1, %2, 0" : "=&v"(d) : "v"(a), "v"(b)); }
DI void mma8(f32x16& d, u32x2 a, u32x2 b) { asm volatile("v_mfma_f32_32x32x8_bf16 %0, %1, %2, %0" : "+v"(d) : "v"(a), "v"(b)); }
DI u32x2 trd(u32 addr) { return __builtin_bit_cast(u32x2, __builtin_amdgcn_ds_read_tr16_b64_v4i16((LAS s16x4*)addr)); }
DI u32x4 trd2(u32 a0, u32 a1) {
  u32x2 lo = trd(a0), hi = trd(a1);
  return __builtin_shufflevector(lo, hi, 0, 1, 2, 3);
}
DI u32x4 lds128(u32 addr) { return *(LAS u32x4*)addr; }
DI f32x4 ldsf4(u32 addr) { return *(LAS f32x4*)addr; }
DI u32 pk(float a, float b) {
  f32x2 v = {a, b};
  return __builtin_bit_cast(u32, __builtin_convertvector(v, b16x2));
}
DI u32 pk2(f32x2 v) { return __builtin_bit_cast(u32, __builtin_convertvector(v, b16x2)); }
DI u32 relu2(u32 v) { return __builtin_bit_cast(u32, __builtin_elementwise_max(__builtin_bit_cast(s16x2, v), s16x2{0, 0})); }
extern "C" __device__ float fmul_legacy_(float, float) __asm("llvm.amdgcn.fmul.legacy");
DI float mul1(float a, float b) { return fmul_legacy_(a, b); }
DI float mx(float a, float b) { return __builtin_elementwise_maximum(a, b); }
DI float dot2(u32 a, u32 b, float c) {
  return __builtin_amdgcn_fdot2_f32_bf16(__builtin_bit_cast(b16x2, a), __builtin_bit_cast(b16x2, b), c, false);
}
DI void swap32(float& a, float& b) {
  auto r = __builtin_amdgcn_permlane32_swap(__float_as_uint(a), __float_as_uint(b), false, false);
  a = __uint_as_float(r[0]);
  b = __uint_as_float(r[1]);
}
template <int I> DI f32x2 pr(const f32x16& a) { return __builtin_shufflevector(a, a, I, I + 1); }
template <int I> DI f32x4 q4(const f32x16& a) { return __builtin_shufflevector(a, a, I, I + 1, I + 2, I + 3); }
DI u32x4 make_srd(const void* p, u32 bytes) {
  u64 a = (u64)p;
  u32x4 r = {(u32)a, (u32)(a >> 32) & 0xffffu, bytes, 0x00020000u};
  return r;
}
template <u32 LI, u32 SI>
DI void dma(u32x4 srd, u32 voff, u32 sbase, u32 lbase) {
  u32 t;
  asm volatile("s_add_u32 m0, %4, %6\n\ts_add_u32 %0, %3, %5\n\tbuffer_load_dwordx4 %1, %2, %0 offen lds"
               : "=&s"(t)
               : "v"(voff), "s"(srd), "s"(sbase), "s"(lbase), "i"(SI), "i"(LI)
               : "memory", "scc");
}
DI void st128(u32x4 v, __amdgpu_buffer_rsrc_t rs, u32 voff, u32 soff) {
  __builtin_amdgcn_raw_buffer_store_b128(v, rs, voff, soff, AUX);
  asm volatile("s_nop 1" ::"v"(v) : "memory");
}
DI u32x4 ld128(__amdgpu_buffer_rsrc_t rs, u32 voff, u32 soff) { return __builtin_amdgcn_raw_buffer_load_b128(rs, voff, soff, AUX); }
template <int OFF> DI void gld64(u64& d, u32 voff, const char* base) {
  asm volatile("global_load_dwordx2 %0, %1, %2 offset:%3 sc1" : "=v"(d) : "v"(voff), "s"(base), "i"(OFF) : "memory");
}
template <int OFF> DI void gld128(f32x4& d, u32 voff, const char* base) {
  asm volatile("global_load_dwordx4 %0, %1, %2 offset:%3 sc1" : "=v"(d) : "v"(voff), "s"(base), "i"(OFF) : "memory");
}
template <int OFF> DI void warm1(u32& d, const void* base) { asm volatile("s_load_dword %0, %1, %2" : "+s"(d) : "s"(base), "i"(OFF)); }
template <int N> struct IC { static constexpr int v = N; };
template <int B, int E, class F> DI void sfor(F&& f) {
  if constexpr (B < E) {
    f(IC<B>{});
    sfor<B + 1, E>(f);
  }
}
constexpr u32 ONES2 = 0x3f803f80u;
constexpr float LOG2E = 1.4426950408889634f;

DI void dual_softmax(const f32x16& P, float sl, u32x4& o0, u32x4& o1) {
  float m0 = mx(mx(mx(mx(mx(mx(mx(P[0], P[1]), P[2]), P[3]), P[4]), P[5]), P[6]), P[7]);
  float m1 = mx(mx(mx(mx(mx(mx(mx(P[8], P[9]), P[10]), P[11]), P[12]), P[13]), P[14]), P[15]);
  swap32(m0, m1);
  m0 = mx(m0, m1) * -sl;
  m1 = m0;
  swap32(m0, m1);
  f32x2 e[8];
  sfor<0, 8>([&](auto II) {
    constexpr int I = decltype(II)::v;
    const float m = I < 4 ? m0 : m1;
    e[I] = __builtin_elementwise_fma(pr<2 * I>(P), f32x2{sl, sl}, f32x2{m, m});
  });
#pragma unroll
  for (int i = 0; i < 8; i++) e[i] = f32x2{__builtin_amdgcn_exp2f(e[i][0]), __builtin_amdgcn_exp2f(e[i][1])};
  const f32x2 s0 = (e[0] + e[1]) + (e[2] + e[3]), s1 = (e[4] + e[5]) + (e[6] + e[7]);
  m0 = s0[0] + s0[1];
  OPAQUE(m0);
  m1 = s1[0] + s1[1];
  swap32(m0, m1);
  m0 = __builtin_amdgcn_rcpf(m0 + m1);
  m1 = m0;
  swap32(m0, m1);
#pragma unroll
  for (int i = 0; i < 4; i++) {
    o0[i] = pk2(e[i] * m0);
    o1[i] = pk2(e[4 + i] * m1);
  }
}
DI u32x4 wphi_frag(u32 a, u32 sg) {
  u32x4 f = trd2(a, a + 256);
  f[0] ^= sg; f[1] ^= sg; f[2] ^= sg; f[3] ^= sg;
  return f;
}
DI void phi_store(u32 apw, const f32x16& P, float sl) {
  u32x4 o0, o1;
  dual_softmax(P, sl, o0, o1);
  *(LAS u32x2*)(apw) = u32x2{o0[0], o0[1]};
  *(LAS u32x2*)(apw ^ 16) = u32x2{o0[2], o0[3]};
  *(LAS u32x2*)(apw ^ 32) = u32x2{o1[0], o1[1]};
  *(LAS u32x2*)(apw ^ 48) = u32x2{o1[2], o1[3]};
}
template <int S> DI u32x4 pack8(const f32x16& a) { return u32x4{pk2(pr<8 * S>(a)), pk2(pr<8 * S + 2>(a)), pk2(pr<8 * S + 4>(a)), pk2(pr<8 * S + 6>(a))}; }

constexpr u32 slot_loc(int s, int k) { return k < 2 ? lx(s) + k * 8192 : k < 4 ? lwa(s) + (k - 2) * 8192 : lwb(s); }
constexpr u32 bloc(int n) {
  return n < 5 ? slot_loc(2, n) : n < 10 ? slot_loc(0, n - 5) : n < 15 ? slot_loc(1, n - 10) : n == 15 ? LBS + 16384
       : n < 26 ? bloc(n - 16) : LBS + (n & 1) * 8192;
}

struct Ctx {
  __amdgpu_buffer_rsrc_t ws;
  char* wsp;
  u32 m, lane;
  u32 ab[3][2];
  u32 rot;
  u64 tag;
  u64 own;
  u32 fl;
};
template <int N> DI u32x4 rdb(const Ctx& c, int cb) {
  constexpr u32 W = bloc(N) / 57344;
  const u32 a = c.ab[W][cb] + (bloc(N) - W * 57344);
  return trd2(a, a + 1024);
}
DI void init_ab(Ctx& c) {
  const u32 lane = c.lane, m = c.m, hh = lane >> 5, blk = (lane >> 4) & 1, q = (lane & 15) >> 2, p = lane & 3;
#pragma unroll
  for (int cb = 0; cb < 2; cb++) {
    const u32 t = m * 2048 + (4 * hh + q) * 128 + (((4 * (cb ^ (m & 1)) + 2 * blk + (p >> 1)) ^ ((q >> 1) << 2)) << 4) + 8 * (p & 1);
#pragma unroll
    for (int k = 0; k < 3; k++) {
      c.ab[k][cb] = t + k * 57344;
      OPAQUE(c.ab[k][cb]);
    }
  }
}
DI void load_tag(Ctx& c, u32 tgt, u32 slot) {
  c.fl = MB_OFF + tgt * 1024 + slot * 8;
  asm volatile("global_load_dwordx2 %0, %1, %2 sc1" : "=v"(c.tag) : "v"(EP_OFF + tgt * 8), "s"(c.wsp) : "memory");
}
DI void set_tag(Ctx& c) {
  OPAQUE(c.tag);
  c.tag += 1;
  c.own = ((u64)(u32)__builtin_amdgcn_readlane((u32)(c.tag >> 32), 63) << 32) | (u32)__builtin_amdgcn_readlane((u32)c.tag, 63);
}
DI void publish(const Ctx& c, u64 mask) {
  asm volatile("s_mov_b64 exec, %3\n\tglobal_atomic_swap_x2 %0, %1, %2 sc1\n\ts_mov_b64 exec, -1" ::"v"(c.fl), "v"(c.tag), "s"(c.wsp), "s"(mask) : "memory");
}
DI u64 peek(const Ctx& c, u32 off, int n) {
  u64 v = c.own;
  const u64 z = 0;
  asm volatile("s_mov_b64 exec, %4\n\tglobal_atomic_or_x2 %0, %1, %2, %3 sc0 sc1\n\ts_mov_b64 exec, -1"
               : "+v"(v)
               : "v"(off + c.lane * 8), "v"(z), "s"(c.wsp), "s"((1ull << n) - 1)
               : "memory");
  return v;
}
DI void await(const Ctx& c, u32 off, int n, u64 v) {
  OPAQUE(v);
#pragma unroll 1
  for (int it = 0; it < (1 << 16) && !__all(v == c.own); it++) {
    __builtin_amdgcn_s_sleep(2);
    v = peek(c, off, n);
    asm volatile("s_waitcnt vmcnt(0)" : "+v"(v)::"memory");
  }
}
DI void sem_post(u32 idx) {
  asm volatile("s_mov_b64 exec, 1\n\tds_add_u32 %0, %1\n\ts_mov_b64 exec, -1" ::"v"(L_SEM + idx * 4), "v"(1u) : "memory");
}
DI void sem_wait(u32 idx, u32 n) {
  u32 v, sv, left = 1u << 20;
  asm volatile(
      "1:\n\tds_read_b32 %0, %3\n\ts_sub_u32 %2, %2, 1\n\ts_waitcnt lgkmcnt(0)\n\ts_cbranch_scc1 2f\n\tv_readfirstlane_b32 %1, %0\n\ts_nop 0\n\t"
      "s_cmp_lt_u32 %1, %4\n\ts_cbranch_scc1 1b\n2:"
      : "=&v"(v), "=&s"(sv), "+s"(left)
      : "v"(L_SEM + idx * 4), "s"(n)
      : "memory", "scc");
}
constexpr u32 SEM_B = 0, SEM_K = 8, SEM_A = 12, SEM_KV = 13, SEM_C = 14, SEM_H = 15;
template <int N> DI u32 hstep(const Ctx& c) {
  const u32 h = c.rot + N;
  return h >= 28 ? h - 28 : h;
}
struct BSrc {
  u32x4 srd;
  u32 vb, sb, d2;
  DI void init(const Ctx& c, const u16* w, u32 rows, u32 rows_per_q) {
    srd = make_srd(w, rows * 1024);
    vb = (c.lane >> 3) * 1024 + (((c.lane & 7) ^ (((c.lane >> 4) & 1) << 2)) << 4);
    sb = c.m * rows_per_q * 1024 + ((blockIdx.x >> 3) & 7) * 128;
    d2 = __builtin_amdgcn_readfirstlane((threadIdx.x & 192) * 32);
  }
  template <int N> DI void issue(const Ctx& c) const {
    const u32 so = sb + (N < 28 ? hstep<N>(c) : N - 28) * 16384;
    dma<bloc(N), 0>(srd, vb, so, d2);
    dma<bloc(N) + 1024, 8192>(srd, vb, so, d2);
  }
};

struct Tile {
  int rt, j, ct;
  bool ffn;
  u32 ldw;
  u32 c0, c1, c2, cc;
  u32x4 srd_x, srd_w;
  u32 vx0, vw;
};
DI u32 blk_of(u32 rt, u32 ct) { return (((rt & 7) * 4 + (ct & 3)) << 3) + (rt >> 3) * 4 + (ct >> 2); }

template <int N> DI void put_block(const Ctx& c, const f32x16& a) {
  const u32 p = c.m * 2048 + c.lane * 16 + bloc(N);
  *(LAS u32x4*)p = pack8<0>(a);
  *(LAS u32x4*)(p + 1024) = pack8<1>(a);
}
template <int N> DI void get_block(const Ctx& c, u32 src, u32x4 (&f)[2]) {
  const u32 p = src * 2048 + c.lane * 16 + bloc(N);
  f[0] = lds128(p);
  f[1] = lds128(p + 1024);
}

constexpr int CW = 8;
constexpr int NB7 = 4;
constexpr int WOFF = 14;
constexpr int NCP = 8;
constexpr u32 CODE_BYTES = 34816;
DI void helper(Ctx& c, const Tile& t, const u16* x, const u16* wo, const u16* w2, u16* out) {
  constexpr int ND = 10;
  const u32 lane = c.lane, m = c.m;
  const int b = blockIdx.x, i = (b & 7) * 4 + (b >> 6);
  const int hd = (b & 7) * 4 + ((b >> 3) & 3), ga = b >> 5;
  const u32 vx1 = t.vx0 ^ 64, vw1 = t.vw + 8 * t.ldw;
  const u32 vc = (lane >> 2) * 3584 + ((lane & 3) << 4);
  const u32 sx = (t.rt * 128 + m * 32) * 1024, sc = m * 16 * 3584 + t.cc;
  const u32 d4 = m * 4096, d2 = m * 2048, d1 = m * 1024;
  u32 wd = 0;
  const char *pb = (const char*)w2 + (b * 4 + m) * 1792, *pc = (const char*)wo + (b * 4 + m) * 512;
  auto warm = [&](auto KK) {
    constexpr int k = decltype(KK)::v - WOFF;
    if constexpr (k >= 0 && k < 28) warm1<k * 64>(wd, pb);
    else if constexpr (k >= 28 && k < 36) warm1<(k - 28) * 64>(wd, pc);
  };
  auto ringf = [&](auto FF) {
    constexpr int G = decltype(FF)::v / ND, S = G % 3, I = decltype(FF)::v % ND;
    const u32 sg = (m * 16 + G * 64) * t.ldw;
    if constexpr (I == 4) {
      if (t.ffn) dma<lwb(S) + 8192, G * 64 * 3584>(t.srd_w, vc, sc, d1);
    }
    if constexpr (I < 4) dma<lx(S) + I * 1024, I * 8192 + G * 128>(t.srd_x, (I & 1) ? vx1 : t.vx0, sx, d4);
    else if constexpr (I < 6) dma<lwa(S) + (I & 1) * 1024, 0>(t.srd_w, (I & 1) ? vw1 : t.vw, sg + t.c0, d2);
    else if constexpr (I < 8) dma<lwa(S) + 8192 + (I & 1) * 1024, 0>(t.srd_w, (I & 1) ? vw1 : t.vw, sg + t.c1, d2);
    else dma<lwb(S) + (I & 1) * 1024, 0>(t.srd_w, (I & 1) ? vw1 : t.vw, sg + t.c2, d2);
  };
  constexpr auto rend = [](int G) { return G < 0 ? ND + 3 : G == 0 ? 2 * ND + 5 : G == 1 ? 3 * ND + 8 : G < 6 ? (G + 3) * ND : 8 * ND; };
  sfor<ND, rend(-1)>(ringf);
  load_tag(c, lane < 16 ? blk_of(t.rt, lane) : lane < 32 ? blk_of(lane & 15, t.ct) : lane < 40 ? m * 64 + (lane & 7) * 8 + ga : b,
           lane < 16 ? 64 + t.ct : lane < 32 ? 80 + t.rt : 32 + hd);
  BSrc b2, bo;
  b2.init(c, w2, 1792, 448);
  bo.init(c, wo, 512, 128);
  const u32 hh = lane >> 5, blk = (lane >> 4) & 1, q = (lane & 15) >> 2, p = lane & 3, r = lane & 31;
  init_ab(c);
  u32x4 bf[8][2];
  auto ldb = [&](auto NN) {
    constexpr int N = decltype(NN)::v;
    bf[N - 28][0] = rdb<N>(c, 0);
    bf[N - 28][1] = rdb<N>(c, 1);
  };
  u64 ps = 0, grs = 0;
  f32x4 gc[4];
  __builtin_amdgcn_s_setprio(1);
  WAITV(4);
  BAR();
  constexpr auto wo_in = [](int G) { return G == 3 ? 28 : G == 4 ? 29 : G == 6 ? 30 : G == 7 ? 31 : -1; };
  float sv[4], scs;
  u32 a_cf = L_ST + (m * 64 + (lane & 15) * 4 + (lane >> 4)) * 8, a_fm = L_ST + 4096 + (m * 32 + r) * 8 + hh * 4, a_lr = L_RS + m * 128 + r * 4;
  u32 a_rs = RS_OFF + (t.rt * 128 + m * 32 + r) * 8, a_cs = CS_OFF + t.ct * 16384 + (lane >> 4) * 4096 + m * 256 + (lane & 15) * 16;
  OPAQUE(a_cf); OPAQUE(a_fm); OPAQUE(a_lr); OPAQUE(a_rs); OPAQUE(a_cs);
  sfor<0, 8>([&](auto GG) {
    constexpr int G = decltype(GG)::v;
    constexpr int NRB = G == 6 ? 10 : G == 7 ? 2 * NB7 : 0;
    if constexpr (G == 5 || G == 6) ldb(IC<23 + G>{});
    if constexpr (G == 1) {
      const u32 cv = *(LAS u32*)(L_ST + m * 2048 + lane * 4), rv = *(LAS u32*)(L_ST + ((lane >> 2) & 3) * 2048 + 1024 + (lane & 3) * 4);
      __builtin_amdgcn_raw_buffer_store_b32(cv, c.ws, lane * 4, CS_OFF + (t.ct * 16 + t.rt) * 1024 + m * 256, AUX);
      asm volatile("s_mov_b64 exec, %3\n\tglobal_store_dword %0, %1, %2 sc1\n\ts_mov_b64 exec, -1" ::"v"(RS_OFF + (t.rt * 128 + t.ct * 8) * 8 + (lane & 15) * 4),
                   "v"(rv), "s"(c.wsp), "s"(m == 0 ? 0xffffull : 0ull)
                   : "memory");
      set_tag(c);
    }
    if constexpr (G == 2) publish(c, 0xffull << (8 * m));
    auto gather = [&]() {
      await(c, MB_OFF + b * 1024 + 512, 32, ps);
      gld64<0>(grs, a_rs, c.wsp);
      gld128<0>(gc[0], a_cs, c.wsp); gld128<1024>(gc[1], a_cs, c.wsp); gld128<2048>(gc[2], a_cs, c.wsp); gld128<3072>(gc[3], a_cs, c.wsp);
    };
    if constexpr (G == 4) ps = peek(c, MB_OFF + b * 1024 + 512, 32);
    if constexpr (G == 5) gather();
    sfor<rend(G - 1), rend(G)>([&](auto FF) {
      ringf(FF);
      warm(IC<decltype(FF)::v - rend(-1)>{});
    });
    if constexpr (G >= 6)
      sfor<(G - 6) * 5, (G - 6) * 5 + NRB / 2>([&](auto NN) {
        constexpr int N = decltype(NN)::v;
        b2.issue<N>(c);
        SB();
        if constexpr (N == 0) {
#pragma unroll
          for (int k = 0; k < 4; k++) OPAQUE(gc[k]);
#pragma unroll
          for (int k = 0; k < 4; k++) {
            sv[k] = gc[0][k];
#pragma unroll
            for (int u = 1; u < 4; u++) {
              sv[k] += gc[u][k];
              OPAQUE(sv[k]);
            }
          }
        } else if constexpr (N == 1) {
          swap32(sv[0], sv[2]);
          swap32(sv[1], sv[3]);
          sv[0] += sv[2];
          OPAQUE(sv[0]);
          sv[1] += sv[3];
          auto sw16 = __builtin_amdgcn_permlane16_swap(__float_as_uint(sv[0]), __float_as_uint(sv[1]), false, false);
          scs = __uint_as_float(sw16[0]) + __uint_as_float(sw16[1]);
          OPAQUE(scs);
        } else if constexpr (N == 2) {
          const u32 h2 = pk(scs, scs);
          const float lo = scs - __uint_as_float(h2 & 0xffff0000u);
          *(LAS u32x2*)a_cf = u32x2{h2, pk(lo, lo)};
        } else if constexpr (N == 3) {
          OPAQUE(grs);
          *(LAS u32*)a_fm = (u32)grs;
          *(LAS u32*)a_lr = (u32)(grs >> 32);
        }
      });
    if constexpr (G >= 6) WAITL0();
    if constexpr (wo_in(G) > 0) bo.issue<wo_in(G)>(c);
    if constexpr (G < 7) WAITV(G < 3 ? rend(G) - (G + 2) * ND : rend(G) - rend(G - 1) + NRB + (wo_in(G) > 0 ? 2 : 0));
    if (G == 7 && !t.ffn) {
      const u32 awp = L_WPHI + 1024 + (4 * hh + q) * 32 + p * 8, sg = blk ? 0x80008000u : 0u;
      const u32x4 w0 = wphi_frag(awp, sg), w1 = wphi_frag(awp + 512, sg);
      const float sl = __uint_as_float((u32)(grs >> 32)) * LOG2E;
      sem_wait(SEM_K + m, 1);
      const u32 ak = ((m >> 1) ? L_SP2 : L_SP1) + (m & 1) * 2048 + lane * 16;
      const f32x16 P0 = mfma(w0, lds128(ak), f32x16{}), P1 = mfma(w1, lds128(ak + 1024), f32x16{});
      BAR();
      const u32 apw = lwa(1) + m * 2048 + r * 64 + ((hh ^ ((r >> 1) & 7)) << 3);
      phi_store(apw, P0, sl);
      phi_store(apw + 8192, P1, sl);
      WAITL0();
    }
    BAR();
  });
  __builtin_amdgcn_s_setprio(2);
  const u32 mb = MB_OFF + b * 1024;
  const u32 fk = mb + 128, fo = mb + 256 + 64 * m, fq = mb + 768 + 16 * m;
  u64 pq = peek(c, fq, 2);
  WAITV(1);
  ldb(IC<30>{});
  ldb(IC<31>{});
  sem_post(SEM_B + m);
  WAITL0();
  asm volatile("" : "+s"(wd));
  bo.issue<32>(c);
  bo.issue<33>(c);
  WAITV(4);
  await(c, fq, 2, pq);
  u32x4 p00, p01, p10, p11;
  u64 pkv;
  float rs0, rs1;
  {
    u32x4 qf[2];
#pragma unroll
    for (int u = 0; u < 2; u++) qf[u] = ld128(c.ws, lane * 16, QF_OFF + ((8 * ga + 2 * m + u) * 32 + hd) * 1024);
    rs0 = __uint_as_float(__builtin_amdgcn_raw_buffer_load_b32(c.ws, lane * 8 + 4, RS_OFF + (8 * ga + 2 * m) * 256, AUX));
    OPAQUE(rs0);
    SB();
    ldb(IC<32>{});
    ldb(IC<33>{});
    WAITL0();
    bo.issue<34>(c);
    bo.issue<35>(c);
    rs1 = rs0;
    swap32(rs0, rs1);
    const u32x4 wp = wphi_frag(L_WPA + (hd & 1) * 512 + (4 * hh + q) * 32 + p * 8, blk ? 0x80008000u : 0u);
    const f32x16 Q0 = mfma(wp, qf[0], f32x16{}), Q1 = mfma(wp, qf[1], f32x16{});
    dual_softmax(Q0, rs0 * LOG2E, p00, p01);
    OPAQUE(p00); OPAQUE(p01);
    SB();
    pkv = peek(c, fk, 16);
    dual_softmax(Q1, rs1 * LOG2E, p10, p11);
    OPAQUE(p10); OPAQUE(p11);
  }
  WAITV(0);
  ldb(IC<34>{});
  ldb(IC<35>{});
  sem_post(SEM_B + m);
  await(c, fk, 16, pkv);
  {
    f32x4 part[8];
#pragma unroll
    for (int s = 0; s < 8; s++) part[s] = __builtin_bit_cast(f32x4, ld128(c.ws, hh * 2048 + r * 16, KV_OFF + (hd * 16 + 2 * s) * 2048 + m * 512));
    f32x4 z = __builtin_bit_cast(f32x4, ld128(c.ws, (lane & 15) * 128 + blk * 16, KZ_OFF + hd * 2048 + m * 32));
    SB();
    float v[4] = {part[0][0], part[0][1], part[0][2], part[0][3]};
#pragma unroll
    for (int s = 1; s < 8; s++)
#pragma unroll
      for (int k = 0; k < 4; k++) {
        v[k] += part[s][k];
        OPAQUE(v[k]);
      }
    swap32(v[0], v[2]);
    swap32(v[1], v[3]);
    v[0] += v[2];
    OPAQUE(v[0]);
    *(LAS u32*)(L_WPHI + r * 32 + m * 8 + hh * 4) = pk(v[0], v[1] + v[3]);
    float za = hh ? z[2] : z[0], zb = hh ? z[3] : z[1];
    sfor<0, 4>([&](auto KK) {
      za += __int_as_float(__builtin_amdgcn_update_dpp(0, __float_as_int(za), 0x120 + (8 >> decltype(KK)::v), 0xf, 0xf, false));
      zb += __int_as_float(__builtin_amdgcn_update_dpp(0, __float_as_int(zb), 0x120 + (8 >> decltype(KK)::v), 0xf, 0xf, false));
    });
    *(LAS u32*)(L_WPHI + (32 + blk) * 32 + m * 8 + hh * 4) = pk(za, zb);
    sem_post(SEM_A);
    sem_wait(SEM_A, 4);
    const u32 e = blk == 0 ? hh * 16 + (lane & 15) : 32 + hh;
    const u32x4 k0 = lds128(L_WPHI + e * 32), k1 = lds128(L_WPHI + e * 32 + 16);
    f32x16 Z0 = mfma(k0, p00, f32x16{}), Z1 = mfma(k0, p10, f32x16{});
    Z0 = mfma(k1, p01, Z0);
    Z1 = mfma(k1, p11, Z1);
    const float r0 = __builtin_amdgcn_rcpf(mul1(Z0[8] + 1e-6f, rs0)), r1 = __builtin_amdgcn_rcpf(mul1(Z1[8] + 1e-6f, rs1));
    u32x4 o0, o1;
#pragma unroll
    for (int d = 0; d < 4; d++) {
      o0[d] = pk(mul1(Z0[2 * d], r0), mul1(Z0[2 * d + 1], r0));
      o1[d] = pk(mul1(Z1[2 * d], r1), mul1(Z1[2 * d + 1], r1));
    }
    st128(o0, c.ws, lane * 16, OF_OFF + ((8 * ga + 2 * m) * 32 + hd) * 1024);
    st128(o1, c.ws, lane * 16, OF_OFF + ((8 * ga + 2 * m + 1) * 32 + hd) * 1024);
  }
  WAITV(0);
  publish(c, 0xff00000000ull);
  const u32 orow = 64 * i + 32 * (m >> 1) + r, oidx = orow * 512 + 64 * ((b >> 3) & 7) + 32 * (m & 1) + 8 * hh;
  u32x4 xr[2] = {*(const GAS u32x4*)(x + oidx), *(const GAS u32x4*)(x + oidx + 16)};
  float frs = __uint_as_float(__builtin_amdgcn_raw_buffer_load_b32(c.ws, orow * 8 + 4, RS_OFF, AUX));
  u64 po = peek(c, fo, 8);
  u32x4 sel[2];
  {
    const u32 one = (((r >> 2) & 1) == hh) ? ((r & 1) ? 0x3f800000u : 0x3f80u) : 0u, dw = ((r >> 3) & 1) * 2 + ((r >> 1) & 1);
#pragma unroll
    for (int d = 0; d < 4; d++) {
      sel[0][d] = (dw == (u32)d && blk == 0) ? one : 0u;
      sel[1][d] = (dw == (u32)d && blk == 1) ? one : 0u;
    }
    OPAQUE(sel[0]); OPAQUE(sel[1]);
  }
  WAITV(0);
  OPAQUE(xr[0]); OPAQUE(xr[1]); OPAQUE(frs);
  await(c, fo, 8, po);
  u32x4 af[8][2];
#pragma unroll
  for (int n = 0; n < 8; n++)
#pragma unroll
    for (int u = 0; u < 2; u++) af[n][u] = ld128(c.ws, lane * 16, OF_OFF + ((2 * i + ((m >> 1) ^ u)) * 32 + 8 * m + n) * 1024);
  SB();
  f32x16 acc[4];
  constexpr int NSM = 4;
  sfor<0, NSM>([&](auto NN) {
    constexpr int N = decltype(NN)::v;
    if constexpr (N == 0) {
      mma0(acc[0], bf[N][0], af[N][0]); mma0(acc[1], bf[N][1], af[N][0]); mma0(acc[2], bf[N][0], af[N][1]); mma0(acc[3], bf[N][1], af[N][1]);
    } else {
      mma(acc[0], bf[N][0], af[N][0]); mma(acc[1], bf[N][1], af[N][0]); mma(acc[2], bf[N][0], af[N][1]); mma(acc[3], bf[N][1], af[N][1]);
    }
  });
  sem_wait(SEM_B + m, 3);
  SB();
  sfor<0, 4>([&](auto KK) {
    constexpr int k = 3 - decltype(KK)::v;
    sfor<NSM, 8>([&](auto NN) {
      constexpr int N = decltype(NN)::v;
      mma(acc[k], bf[N][k & 1], af[N][k >> 1]);
      if constexpr (N == NSM + 1 && k < 3) {
        SB();
        put_block<10 + k>(c, acc[k + 1]);
        if constexpr (k == 0) sem_post(SEM_H);
        SB();
      }
    });
  });
  sem_wait(SEM_H, 4);
  {
    u32x4 f[7][2];
    get_block<10>(c, m ^ 1, f[0]);
    get_block<11>(c, m ^ 2, f[1]);
    get_block<12>(c, m ^ 3, f[2]);
#pragma unroll
    for (int k = 0; k < 3; k++) { mma(acc[0], sel[0], f[k][0]); mma(acc[0], sel[1], f[k][1]); }
    sem_wait(SEM_C, 4);
    get_block<0>(c, m, f[3]);
    get_block<1>(c, m ^ 1, f[4]);
    get_block<2>(c, m ^ 2, f[5]);
    get_block<3>(c, m ^ 3, f[6]);
#pragma unroll
    for (int k = 3; k < 7; k++) { mma(acc[0], sel[0], f[k][0]); mma(acc[0], sel[1], f[k][1]); }
  }
  if (threadIdx.x == 319) __hip_atomic_store((GAS u64*)(c.wsp + EP_OFF + b * 8), c.tag, __ATOMIC_RELAXED, __HIP_MEMORY_SCOPE_AGENT);
  asm volatile("s_nop 15" : "+v"(acc[0]));
#pragma unroll
  for (int h = 0; h < 2; h++) {
    float e[8];
#pragma unroll
    for (int k = 0; k < 8; k++) e[k] = acc[0][8 * h + k];
#pragma unroll
    for (int k = 0; k < 4; k++) swap32(e[k], e[4 + k]);
    u32x4 o;
#pragma unroll
    for (int d = 0; d < 4; d++)
      o[d] = pk(__builtin_fmaf(e[2 * d], frs, __uint_as_float(xr[h][d] << 16)), __builtin_fmaf(e[2 * d + 1], frs, __uint_as_float(xr[h][d] & 0xffff0000u)));
    *(GAS u32x4*)(out + oidx + 16 * h) = o;
  }
}

template <bool FFN> struct Slots {
  static constexpr int NBK = FFN ? 7 : 6, N7 = 28 * NBK, NS = N7 + 5 * NBK;
  static constexpr int blk(int n) {
    if (n < N7) return n % NBK;
    const int k = (n - N7) / 5;
    return (!FFN && k < 2) ? 1 - k : k;
  }
  static constexpr int step(int n) { return n < N7 ? n / NBK : ((n - N7) % 5 == 0 ? -1 : 27 + (n - N7) % 5); }
  static constexpr int bar(int G) { return (G < 7 ? (4 * G + 3) * NBK : NS - NBK) + 1; }
};
template <bool FFN>
DI void compute1(Ctx& c, const Tile& t) {
  u32x4 xa, xb;
  using SL = Slots<FFN>;
  constexpr int NBK = SL::NBK;
  const u32 lane = c.lane, m = c.m;
  const int rt = t.rt, j = t.j;
  const u32 hh = lane >> 5, blk = (lane >> 4) & 1, q = (lane & 15) >> 2, p = lane & 3, r = lane & 31;
  u32 ax[4], aw[2][2], ac;
#pragma unroll
  for (int s = 0; s < 4; s++) ax[s] = m * 4096 + r * 128 + (((2 * s + hh) ^ ((r >> 1) & 7)) << 4);
  const u32 at = (((2 * blk + (p >> 1)) ^ ((q >> 1) << 2)) << 4) + 8 * (p & 1);
  {
    const u32 a = (8 * hh + q) * 128 + at;
    aw[0][0] = a + LWA; aw[0][1] = (a ^ 64) + LWA; aw[1][0] = a + LWB; aw[1][1] = (a ^ 64) + LWB;
    ac = (8 * hh + q) * 64 + 32 * blk + 8 * p + LWB + 8192;
    OPAQUE(aw[0][0]); OPAQUE(aw[0][1]); OPAQUE(aw[1][0]); OPAQUE(aw[1][1]); OPAQUE(ac);
  }
  u32 acs = L_ST + r * 8;
  OPAQUE(acs);
  auto rdw = [&](auto NN) -> u32x4 {
    constexpr int T = SL::step(decltype(NN)::v), M = SL::blk(decltype(NN)::v);
    if constexpr (T < 0) {
      const u32x2 v = *(LAS u32x2*)(acs + M * 256);
      return __builtin_shufflevector(v, v, 0, 1, -1, -1);
    }
    else {
      constexpr int S = (T >> 2) % 3, s = T & 3;
      if constexpr (M < 6) {
        const u32 a = (M < 4 ? aw[0][M & 1] + S * 16384 + (M >> 1) * 8192 : aw[1][M & 1] + S * 12288) + s * 2048;
        return trd2(a, a + 512);
      } else {
        const u32 a = ac + S * 12288 + s * 1024;
        return trd2(a, a + 256);
      }
    }
  };
  u32x4 xf[4], wf[2 * NBK];
  u32x2 fm;
  f32x16 acc[NBK], P0, P1;
  float rstd;
  u32x2 sv[8];
  u32x4 eo;
  float sa;
  const u32 tb = rt * 4 + m;
  const u32 obase = FFN ? HF_OFF + tb * 112 * 1024 : QF_OFF + (tb * 32 + 4 * j) * 1024;
  WAITL0();
  WAITV(NCP);
  BAR();
  xf[0] = lds128(ax[0]);
  sfor<0, NBK>([&](auto NN) { wf[decltype(NN)::v] = rdw(NN); });
  SB();
  auto filler = [&](auto NN) {
    constexpr int n = decltype(NN)::v;
    if constexpr (n == SL::bar(0) - 5) {
      WAITV(0);
      xa = lds128(L_ST + m * 2048 + lane * 16);
      xb = lds128(L_ST + m * 2048 + 1024 + lane * 16);
      const u32 a = LBS + m * 4096 + (4 * hh + q) * 128 + at;
#pragma unroll
      for (int k = 0; k < 8; k++) sv[k] = trd((a ^ ((k >> 2) * 64)) + (k & 3) * 1024);
      set_tag(c);
    }
    if constexpr (n == SL::bar(0) - 2) {
      u32 ones = ONES2;
      asm volatile("" : "+s"(ones));
      float st[4] = {0.f, 0.f, 0.f, 0.f}, cs[2] = {0.f, 0.f};
#pragma unroll
      for (int d = 0; d < 4; d++) {
        st[0] = dot2(ones, xa[d], st[0]);
        st[1] = dot2(xa[d], xa[d], st[1]);
        st[2] = dot2(ones, xb[d], st[2]);
        st[3] = dot2(xb[d], xb[d], st[3]);
      }
#pragma unroll
      for (int d = 0; d < 8; d++) {
        cs[0] = dot2(ones, sv[d >> 1][d & 1], cs[0]);
        cs[1] = dot2(ones, sv[4 + (d >> 1)][d & 1], cs[1]);
      }
      swap32(st[0], st[1]);
      swap32(st[2], st[3]);
      swap32(cs[0], cs[1]);
      *(LAS float*)(L_ST + m * 2048 + lane * 4) = cs[0] + cs[1];
      st[0] += st[1];
      OPAQUE(st[0]);
      st[2] += st[3];
      auto sw16 = __builtin_amdgcn_permlane16_swap(__float_as_uint(st[0]), __float_as_uint(st[2]), false, false);
      sa = __uint_as_float(sw16[0]) + __uint_as_float(sw16[1]);
      OPAQUE(sa);
    }
    if constexpr (n == SL::bar(0) - 1) {
      sfor<0, 4>([&](auto KK) { sa += __int_as_float(__builtin_amdgcn_update_dpp(0, __float_as_int(sa), 0x120 + (8 >> decltype(KK)::v), 0xf, 0xf, false)); });
      float mean = sa * (1.f / 512.f), ms = mean;
      swap32(mean, ms);
      ms = __builtin_amdgcn_rsqf(mx(__builtin_fmaf(-mean, mean, ms), 0.f) + 1e-5f);
      const u32 mh = pk(-mean, -mean);
      const float ml = -mean - __uint_as_float(mh & 0xffff0000u);
      *(LAS u32x2*)(L_ST + m * 2048 + 1024 + (((lane >> 4) & 1) | ((lane & 15) << 1) | (lane & 32)) * 8) = u32x2{(mh & 0xffffu) | (pk(ml, ml) & 0xffff0000u), __float_as_uint(ms)};
    }
    if constexpr (n >= SL::N7 + 5) {
      constexpr int g = (n - SL::N7) % 5, M = SL::blk(n - 5);
      if constexpr (g >= 1) {
        constexpr int s = (g - 1) >> 1, h = (g - 1) & 1;
        if constexpr (FFN || M < 4) {
          eo[2 * h] = pk2(pr<8 * s + 4 * h>(acc[M]));
          eo[2 * h + 1] = pk2(pr<8 * s + 4 * h + 2>(acc[M]));
          if constexpr (FFN) {
            eo[2 * h] = relu2(eo[2 * h]);
            eo[2 * h + 1] = relu2(eo[2 * h + 1]);
          }
          if constexpr (h == 0) OPAQUE(eo);
        }
        if constexpr (h == 1) {
          if constexpr (FFN) st128(eo, c.ws, lane * 16, obase + ((t.c0 >> 5) + 2 * M + s) * 1024);
          else if constexpr (M == 1) {
            *(LAS u32x4*)(((m >> 1) ? L_SP2 : L_SP1) + (m & 1) * 2048 + s * 1024 + lane * 16) = eo;
            if constexpr (s == 1) sem_post(SEM_K + m);
          } else if constexpr (M == 0) {
            const u32x4 wp = wphi_frag(L_WPHI + s * 512 + (4 * hh + q) * 32 + p * 8, blk ? 0x80008000u : 0u);
            asm volatile("s_nop 1\n\tv_mfma_f32_32x32x16_bf16 %0, %1, %2, 0" : "=&v"(s ? P1 : P0) : "v"(wp), "v"(eo));
          } else if constexpr (M < 4) st128(eo, c.ws, lane * 16, obase + (2 * (M - 2) + s) * 1024);
        }
      }
    }
  };
  sfor<0, SL::NS>([&](auto NN) {
    constexpr int n = decltype(NN)::v, T = SL::step(n), M = SL::blk(n);
    constexpr bool TR = !FFN && M >= 4;
    const u32x4 xo = xf[T & 3], wo_ = wf[n % (2 * NBK)];
    if constexpr (T < 0) {
      const u32x2 cf = __builtin_shufflevector(wo_, wo_, 0, 1);
      mma8(acc[M], TR ? fm : cf, TR ? cf : fm);
    } else if constexpr (T == 0) mma0(acc[M], TR ? xo : wo_, TR ? wo_ : xo);
    else mma(acc[M], TR ? xo : wo_, TR ? wo_ : xo);
    constexpr int G = n < SL::N7 ? n / (4 * NBK) : 7;
    if constexpr (n == SL::bar(G)) {
      WAITL0();
      BAR();
    }
    if constexpr (n + NBK < SL::NS && n != SL::bar(G) - 1) {
      if constexpr (n < SL::N7 && (n == SL::bar(G) || (M == 0 && (T & 3) != 3))) {
        if constexpr (T == 27) {
          fm = *(LAS u32x2*)(hh ? L_ZERO : L_ST + 4096 + (m * 32 + r) * 8);
          xf[0] = lds128(ax[0] + 16384); xf[1] = lds128(ax[1] + 16384); xf[2] = lds128(ax[2] + 16384);
        } else xf[(T + 1) & 3] = lds128(ax[(T + 1) & 3] + (((T + 1) >> 2) % 3) * 16384);
      }
      if constexpr (n == SL::bar(G)) wf[(n - 1 + NBK) % (2 * NBK)] = rdw(IC<n - 1 + NBK>{});
      wf[(n + NBK) % (2 * NBK)] = rdw(IC<n + NBK>{});
      if constexpr (n == SL::N7) {
        xf[3] = lds128(ax[3] + 16384);
        if constexpr (!FFN) rstd = *(LAS float*)(L_RS + m * 128 + r * 4);
      }
    }
    filler(NN);
    SB();
  });
  __builtin_amdgcn_s_setprio(3);
  if constexpr (FFN) {
    asm volatile("s_nop 15" : "+v"(acc[6]));
    sfor<0, 2>([&](auto SS) {
      u32x4 o = pack8<decltype(SS)::v>(acc[6]);
#pragma unroll
      for (int d = 0; d < 4; d++) o[d] = relu2(o[d]);
      st128(o, c.ws, lane * 16, obase + ((t.cc >> 5) + decltype(SS)::v) * 1024);
    });
  } else {
    const u32 apw = lx(1) + m * 2048 + r * 64 + ((hh ^ ((r >> 1) & 7)) << 3);
    phi_store(apw, P0, rstd * LOG2E);
    SB();
    WAITV(0);
    publish(c, 0xf00ull);
    const u32 av0 = L_ST + m * 2048 + lane * 16, av1 = lwb(1) + m * 2048 + lane * 16;
    sfor<0, 2>([&](auto TT) {
      constexpr int tt = decltype(TT)::v;
      const f32x4 ra = ldsf4(L_RS + m * 128 + (16 * tt + 4 * hh) * 4), rb = ldsf4(L_RS + m * 128 + (16 * tt + 8 + 4 * hh) * 4);
      const f32x4 a0 = q4<8 * tt>(acc[4]) * ra, b0 = q4<8 * tt + 4>(acc[4]) * rb, a1 = q4<8 * tt>(acc[5]) * ra, b1 = q4<8 * tt + 4>(acc[5]) * rb;
      *(LAS u32x4*)(av0 + tt * 1024) = u32x4{pk(a0[0], a0[1]), pk(a0[2], a0[3]), pk(b0[0], b0[1]), pk(b0[2], b0[3])};
      *(LAS u32x4*)(av1 + tt * 1024) = u32x4{pk(a1[0], a1[1]), pk(a1[2], a1[3]), pk(b1[0], b1[1]), pk(b1[2], b1[3])};
    });
    phi_store(apw + 8192, P1, rstd * LOG2E);
    WAITL0();
    BAR();
    const u32 t0 = 4 * hh + q;
    const u32 apr = ((m >> 1) ? lwa(1) : lx(1)) + (m & 1) * 8192 + t0 * 64 + (((4 * blk + p) ^ (t0 >> 1)) << 3);
    const u32 avr = ((m >> 1) ? lwb(1) : L_ST) + lane * 16;
    const bool isv = blk == (m & 1);
    u32x4 pa[8], vb[8];
#pragma unroll
    for (int k = 0; k < 8; k++) {
      pa[k] = trd2(apr + k * 1024, (apr ^ 32) + k * 1024 + 512);
      vb[k] = lds128(avr + k * 1024);
    }
    sem_post(SEM_KV);
    f32x16 X = f32x16{};
#pragma unroll
    for (int k = 0; k < 8; k++) {
#pragma unroll
      for (int d = 0; d < 4; d++) vb[k][d] = isv ? vb[k][d] : ONES2;
      X = mfma(pa[k], vb[k], X);
    }
    const u32 pi = (4 * j + m) * 16 + rt;
    const u32 ve = isv ? KV_OFF + pi * 2048 + (hh * 16 + (lane & 15)) * 16 : KZ_OFF + pi * 128 + hh * 16, vs = isv ? 512 : 32;
    st128(__builtin_bit_cast(u32x4, q4<0>(X)), c.ws, ve, 0);
    st128(__builtin_bit_cast(u32x4, q4<4>(X)), c.ws, ve + vs, 0);
    st128(__builtin_bit_cast(u32x4, q4<8>(X)), c.ws, ve + 2 * vs, 0);
    st128(__builtin_bit_cast(u32x4, q4<12>(X)), c.ws, ve + 3 * vs, 0);
  }
  __builtin_amdgcn_s_setprio(0);
  if constexpr (!FFN) {
    WAITV(0);
    publish(c, 0xffull);
    sem_wait(SEM_KV, 4);
  }
}
DI void compute2(Ctx& c, bool ffn, const u16* w2) {
  const u32 lane = c.lane, m = c.m;
  const int b = blockIdx.x, i = (b & 7) * 4 + (b >> 6);
  init_ab(c);
  BSrc b2;
  b2.init(c, w2, 1792, 448);
  auto issue = [&](auto NN) { b2.issue<decltype(NN)::v>(c); };
  u32x4 af[28][2], bf[28][2];
  auto ldb = [&](auto NN) {
    constexpr int N = decltype(NN)::v;
    bf[N][0] = rdb<N>(c, 0);
    bf[N][1] = rdb<N>(c, 1);
  };
  sfor<5 + NB7, 10>(issue);
  sfor<10, 10 + NB7 + 2>(issue);
  WAITV(14);
  publish(c, ffn ? 0xffull : 0ull);
  sem_wait(SEM_B + m, 1);
  sfor<0, 5>(ldb);
  sfor<10 + NB7 + 2, 16>(issue);
  WAITL0();
  sfor<16, 21>(issue);
  WAITV(22);
  sfor<5, 10>(ldb);
  const u32 fh = MB_OFF + b * 1024 + 32 * m;
  u64 ph = peek(c, fh, 4);
  WAITL0();
  sfor<21, 26>(issue);
  WAITV(10);
  await(c, fh, 4, ph);
  asm volatile("" : "+s"(c.rot));
  const u32 ah = HF_OFF + ((2 * i) * 112 + 28 * m) * 1024;
  const u32 va[2] = {lane * 16 + (m >> 1) * 114688, lane * 16 + ((m >> 1) ^ 1) * 114688};
  auto lda = [&](auto NN, int u) {
    constexpr int N = decltype(NN)::v;
    if constexpr (N < 28) af[N][u] = ld128(c.ws, va[u], ah + hstep<N>(c) * 1024);
  };
  sfor<0, CW>([&](auto NN) { lda(NN, 0); lda(NN, 1); });
  SB();
  f32x16 ac2[4];
  sfor<0, 24>([&](auto NN) {
    constexpr int n = decltype(NN)::v;
    sfor<0, 4>([&](auto KK) {
      constexpr int k = decltype(KK)::v;
      if constexpr (n == 0) mma0(ac2[k], bf[n][k & 1], af[n][k >> 1]);
      else mma(ac2[k], bf[n][k & 1], af[n][k >> 1]);
      SB();
      if constexpr ((k & 1) == 1 && n + CW < 28) lda(IC<n + CW>{}, k >> 1);
      if constexpr ((k & 1) == 0 && n >= 6) bf[n + 4][k >> 1] = rdb<n + 4>(c, k >> 1);
      if constexpr (k == 3 && n == 8) {
        sem_wait(SEM_B + m, 2);
        sfor<26, 28>(issue);
      }
      if constexpr (k == 3 && n == 11) sem_post(SEM_B + m);
      SB();
    });
  });
  sfor<0, 4>([&](auto KK) {
    constexpr int k = decltype(KK)::v;
    sfor<24, 28>([&](auto NN) {
      constexpr int n = decltype(NN)::v;
      mma(ac2[k], bf[n][k & 1], af[n][k >> 1]);
      if constexpr (n == 25 && k > 0) {
        SB();
        put_block<k - 1>(c, ac2[k - 1]);
        SB();
      }
    });
  });
  asm volatile("s_nop 15" : "+v"(ac2[3]));
  put_block<3>(c, ac2[3]);
  sem_post(SEM_C);
}

__global__ __launch_bounds__(512, 2) void fused_k(const u16* x, const u16* wqkv, const u16* wphi, const u16* wo, const u16* w1, const u16* w2, u16* out,
                                                  char* ws) {
  __shared__ __attribute__((aligned(16))) char smem[163840];
  asm volatile("" ::"v"((LAS char*)smem));
  const int b = blockIdx.x, xcd = b & 7, r = b >> 3;
  u64 pc0;
  asm volatile("s_getpc_b64 %0" : "=s"(pc0));
  {
    u32 off = ((r * 8 + (threadIdx.x >> 6)) * 3 + (threadIdx.x & 3)) * 64;
    off = off < CODE_BYTES ? off : 0;
    asm volatile("s_mov_b32 m0, %2\n\ts_mov_b64 exec, 7\n\tglobal_load_lds_dword %0, %1\n\ts_mov_b64 exec, -1" ::"v"(off), "s"(pc0), "i"(L_RS) : "memory");
  }
  Ctx c;
  c.lane = threadIdx.x & 63;
  const u32 w = __builtin_amdgcn_readfirstlane(threadIdx.x >> 6);
  c.m = w & 3;
  c.ws = __builtin_amdgcn_make_buffer_rsrc(ws, 0, WS_BYTES, 0x00020000);
  c.wsp = ws;
  c.rot = (b >> 6) * 7;
  const int rt = (xcd >> 2) * 8 + (r >> 2), ct = (xcd & 3) * 4 + (r & 3), j = ct >> 1;
  Tile t;
  t.rt = rt;
  t.j = j;
  t.ct = ct;
  t.ffn = ct & 1;
  t.ldw = t.ffn ? 3584 : 3072;
  const u32 lane = c.lane, m = c.m;
  t.srd_x = make_srd(x, 2048 * 512 * 2);
  t.srd_w = make_srd(t.ffn ? w1 : wqkv, 512 * t.ldw);
  t.vx0 = (lane >> 3) * 1024 + (((lane & 7) ^ (lane >> 4)) << 4);
  {
    const u32 sx = (rt * 128 + m * 32 + (w >> 2) * 16) * 1024, dx = m * 4096 + (w >> 2) * 2048;
    dma<LX, 0>(t.srd_x, t.vx0, sx, dx);
    dma<LX + 1024, 8192>(t.srd_x, t.vx0 ^ 64, sx, dx);
    t.vw = (lane >> 3) * t.ldw + (((lane & 7) ^ (((lane >> 4) & 1) << 2)) << 4);
    t.c0 = t.ffn ? 448 * j + (j & 1) * 64 : 1024 + 128 * j;
    t.c1 = t.ffn ? t.c0 + 128 : 128 * j;
    t.c2 = t.ffn ? t.c0 + 256 : 2048 + 128 * j;
    t.cc = t.ffn ? 448 * j + ((j & 1) ? 0 : 384) : 0;
    dma<LWA, 0>(t.srd_w, t.vw, w * 8 * t.ldw + t.c0, w * 1024);
    dma<LWA + 8192, 0>(t.srd_w, t.vw, w * 8 * t.ldw + t.c1, w * 1024);
    dma<LWB, 0>(t.srd_w, t.vw, w * 8 * t.ldw + t.c2, w * 1024);
    if (t.ffn && w >= 4) dma<LWB + 8192, 0>(t.srd_w, (lane >> 2) * 3584 + ((lane & 3) << 4), m * 16 * 3584 + t.cc, m * 1024);
    BAR();
    SB();
  }
  if (__builtin_expect(w >= 4, 1)) {
    helper(c, t, x, wo, w2, out);
  } else {
    {
      const u32 vs = m < 3 ? t.vw : (lane >> 3) * t.ldw + ((lane & 3) << 4);
      const u32 so = rt * 32 * t.ldw + (m == 0 ? t.c0 : m == 1 ? t.c1 : m == 2 ? t.c2 : t.cc);
      dma<LBS, 0>(t.srd_w, vs, so, m * 4096);
      dma<LBS + 1024, 0>(t.srd_w, vs, so + 8 * t.ldw, m * 4096);
      dma<LBS + 2048, 0>(t.srd_w, vs, so + 16 * t.ldw, m * 4096);
      dma<LBS + 3072, 0>(t.srd_w, vs, so + 24 * t.ldw, m * 4096);
      dma<L_ST, 0>(t.srd_x, lane * 16, (rt * 128 + ct * 8 + m * 2) * 1024, m * 2048);
      dma<L_ST + 1024, 1024>(t.srd_x, lane * 16, (rt * 128 + ct * 8 + m * 2) * 1024, m * 2048);
      dma<L_WPHI, 0>(make_srd(wphi, 32 * 512), lane * 16, m < 2 ? (u32)(j * 2048 + m * 1024) : (u32)(((b & 7) * 2 + ((b >> 4) & 1)) * 1024), (m < 2 ? m : 2) * 1024);
      const u32 ii = 2 * rt + (m >> 1), l7 = lane & 7;
      const u32 t1 = t.ffn ? (ii & 3) * 64 + (ii >> 2) + l7 * 8 : l7 * 32 + m * 8 + j, f1 = t.ffn ? j * 2 + (m & 1) : 16 + rt;
      load_tag(c, lane < 8 ? t1 : lane < 12 ? (rt >> 1) * 32 + (lane & 3) * 8 + j : b, lane < 8 ? f1 : 96 + (rt & 1) * 4 + m);
      *(LAS u32*)(L_SEM + (lane & 31) * 4) = 0;
    }
    if (t.ffn) compute1<true>(c, t);
    else compute1<false>(c, t);
    compute2(c, t.ffn, w2);
  }
}

extern "C" void kernel_launch(void* const* d_in, const int* in_sizes, int n_in, void* d_out, int out_size, void* d_ws, size_t ws_size,
                              hipStream_t stream) {
  const u16 *x = (const u16*)d_in[0], *wqkv = (const u16*)d_in[1], *wphi = (const u16*)d_in[2], *wo = (const u16*)d_in[3];
  const u16 *w1 = (const u16*)d_in[5], *w2 = (const u16*)d_in[6];
  fused_k<<<256, 512, 0, stream>>>(x, wqkv, wphi, wo, w1, w2, (u16*)d_out, (char*)d_ws);
}
```

```cpp
#include <hip/hip_runtime.h>
#pragma clang diagnostic ignored "-Wint-to-pointer-cast"
typedef unsigned short u16;
typedef unsigned u32;
typedef unsigned long long u64;
typedef u32 u32x2 __attribute__((ext_vector_type(2)));
typedef u32 u32x4 __attribute__((ext_vector_type(4)));
typedef short s16x2 __attribute__((ext_vector_type(2)));
typedef short s16x4 __attribute__((ext_vector_type(4)));
typedef float f32x2 __attribute__((ext_vector_type(2)));
typedef float f32x4 __attribute__((ext_vector_type(4)));
typedef float f32x16 __attribute__((ext_vector_type(16)));
typedef __bf16 b16x2 __attribute__((ext_vector_type(2)));
typedef __bf16 b16x8 __attribute__((ext_vector_type(8)));
#define LAS __attribute__((address_space(3)))
#define GAS __attribute__((address_space(1)))
#define DI __device__ __forceinline__
#define SB() __builtin_amdgcn_sched_barrier(0)
#define BAR() __builtin_amdgcn_s_barrier()
#define WAITV(n) do { asm volatile("s_waitcnt vmcnt(%0)" ::"i"(n) : "memory"); SB(); } while (0)
#define WAITL0() asm volatile("s_waitcnt lgkmcnt(0)" ::: "memory")
#define OPAQUE(x) asm volatile("" : "+v"(x))

#define NTX 0
#define NTW 0
#define NTS 0
#define NTSX ""
#define SLP 2
#define P7 2
constexpr u32 HF_OFF = 0;
constexpr u32 QF_OFF = 64u * 112u * 1024u;
constexpr u32 OF_OFF = QF_OFF + 64u * 32u * 1024u;
constexpr u32 KV_OFF = OF_OFF + 64u * 32u * 1024u;
constexpr u32 KZ_OFF = KV_OFF + 32u * 16u * 2048u;
constexpr u32 CS_OFF = KZ_OFF + 32u * 16u * 128u;
constexpr u32 RS_OFF = CS_OFF + 256u * 1024u;
constexpr u32 MB_OFF = RS_OFF + 2048u * 8u;
constexpr u32 EP_OFF = MB_OFF + 256u * 1024u;
constexpr u32 XP_OFF = EP_OFF + 2048u;
constexpr u32 WS_BYTES = XP_OFF + 256u * 32768u;
constexpr u32 LX = 0, LWA = 49152, LWB = 98304, LBS = 135168, LSCR = 159744;
constexpr u32 lx(int s) { return LX + s * 16384; }
constexpr u32 lwa(int s) { return LWA + s * 16384; }
constexpr u32 lwb(int s) { return LWB + s * 12288; }
constexpr u32 L_WPHI = LSCR, L_WPA = LSCR + 2048, L_RS = LSCR + 3072, L_SEM = LSCR + 3584, L_ZERO = LSCR + 3648;
constexpr u32 L_SP0 = lwb(0) + 8192, L_SP1 = lwb(1) + 8192, L_SP2 = lwb(2) + 8192;
constexpr u32 L_ST = LBS + 16384;
constexpr int AUX = 16;

DI f32x16 mfma(u32x4 a, u32x4 b, f32x16 c) {
  return __builtin_amdgcn_mfma_f32_32x32x16_bf16(__builtin_bit_cast(b16x8, a), __builtin_bit_cast(b16x8, b), c, 0, 0, 0);
}
DI void mma(f32x16& d, u32x4 a, u32x4 b) { asm volatile("v_mfma_f32_32x32x16_bf16 %0, %1, %2, %0" : "+v"(d) : "v"(a), "v"(b)); }
DI void mma0(f32x16& d, u32x4 a, u32x4 b) { asm volatile("v_mfma_f32_32x32x16_bf16 %0, %1, %2, 0" : "=&v"(d) : "v"(a), "v"(b)); }
DI void mma8(f32x16& d, u32x2 a, u32x2 b) { asm volatile("v_mfma_f32_32x32x8_bf16 %0, %1, %2, %0" : "+v"(d) : "v"(a), "v"(b)); }
DI u32x2 trd(u32 addr) { return __builtin_bit_cast(u32x2, __builtin_amdgcn_ds_read_tr16_b64_v4i16((LAS s16x4*)addr)); }
DI u32x4 trd2(u32 a0, u32 a1) {
  u32x2 lo = trd(a0), hi = trd(a1);
  return __builtin_shufflevector(lo, hi, 0, 1, 2, 3);
}
DI u32x4 lds128(u32 addr) { return *(LAS u32x4*)addr; }
DI f32x4 ldsf4(u32 addr) { return *(LAS f32x4*)addr; }
DI u32 pk(float a, float b) {
  f32x2 v = {a, b};
  return __builtin_bit_cast(u32, __builtin_convertvector(v, b16x2));
}
DI u32 pk2(f32x2 v) { return __builtin_bit_cast(u32, __builtin_convertvector(v, b16x2)); }
DI u32 relu2(u32 v) { return __builtin_bit_cast(u32, __builtin_elementwise_max(__builtin_bit_cast(s16x2, v), s16x2{0, 0})); }
extern "C" __device__ float fmul_legacy_(float, float) __asm("llvm.amdgcn.fmul.legacy");
DI float mul1(float a, float b) { return fmul_legacy_(a, b); }
DI float mx(float a, float b) { return __builtin_elementwise_maximum(a, b); }
DI float dot2(u32 a, u32 b, float c) {
  return __builtin_amdgcn_fdot2_f32_bf16(__builtin_bit_cast(b16x2, a), __builtin_bit_cast(b16x2, b), c, false);
}
DI void swap32(float& a, float& b) {
  auto r = __builtin_amdgcn_permlane32_swap(__float_as_uint(a), __float_as_uint(b), false, false);
  a = __uint_as_float(r[0]);
  b = __uint_as_float(r[1]);
}
template <int I> DI f32x2 pr(const f32x16& a) { return __builtin_shufflevector(a, a, I, I + 1); }
template <int I> DI f32x4 q4(const f32x16& a) { return __builtin_shufflevector(a, a, I, I + 1, I + 2, I + 3); }
DI u32x4 make_srd(const void* p, u32 bytes) {
  u64 a = (u64)p;
  u32x4 r = {(u32)a, (u32)(a >> 32) & 0xffffu, bytes, 0x00020000u};
  return r;
}
template <u32 LI, u32 SI, bool NT = false>
DI void dma(u32x4 srd, u32 voff, u32 sbase, u32 lbase) {
  u32 t;
  if constexpr (NT) {
    asm volatile("s_add_u32 m0, %4, %6\n\ts_add_u32 %0, %3, %5\n\tbuffer_load_dwordx4 %1, %2, %0 offen nt lds"
                 : "=&s"(t)
                 : "v"(voff), "s"(srd), "s"(sbase), "s"(lbase), "i"(SI), "i"(LI)
                 : "memory", "scc");
  } else
  asm volatile("s_add_u32 m0, %4, %6\n\ts_add_u32 %0, %3, %5\n\tbuffer_load_dwordx4 %1, %2, %0 offen lds"
               : "=&s"(t)
               : "v"(voff), "s"(srd), "s"(sbase), "s"(lbase), "i"(SI), "i"(LI)
               : "memory", "scc");
}
DI void st128(u32x4 v, __amdgpu_buffer_rsrc_t rs, u32 voff, u32 soff) {
  __builtin_amdgcn_raw_buffer_store_b128(v, rs, voff, soff, AUX);
  asm volatile("s_nop 1" ::"v"(v) : "memory");
}
#define LDAUX AUX
DI u32x4 ld128(__amdgpu_buffer_rsrc_t rs, u32 voff, u32 soff) { return __builtin_amdgcn_raw_buffer_load_b128(rs, voff, soff, LDAUX); }
template <int OFF> DI void gld64(u64& d, u32 voff, const char* base) {
  asm volatile("global_load_dwordx2 %0, %1, %2 offset:%3 sc1" : "=v"(d) : "v"(voff), "s"(base), "i"(OFF) : "memory");
}
template <int OFF> DI void gld128(f32x4& d, u32 voff, const char* base) {
  asm volatile("global_load_dwordx4 %0, %1, %2 offset:%3 sc1" : "=v"(d) : "v"(voff), "s"(base), "i"(OFF) : "memory");
}
template <int OFF> DI void warm1(u32& d, const void* base) { asm volatile("s_load_dword %0, %1, %2" : "+s"(d) : "s"(base), "i"(OFF)); }
template <int N> struct IC { static constexpr int v = N; };
template <int B, int E, class F> DI void sfor(F&& f) {
  if constexpr (B < E) {
    f(IC<B>{});
    sfor<B + 1, E>(f);
  }
}
constexpr u32 ONES2 = 0x3f803f80u;
constexpr float LOG2E = 1.4426950408889634f;

DI void dual_softmax(const f32x16& P, float sl, u32x4& o0, u32x4& o1) {
  float m0 = mx(mx(mx(mx(mx(mx(mx(P[0], P[1]), P[2]), P[3]), P[4]), P[5]), P[6]), P[7]);
  float m1 = mx(mx(mx(mx(mx(mx(mx(P[8], P[9]), P[10]), P[11]), P[12]), P[13]), P[14]), P[15]);
  swap32(m0, m1);
  m0 = mx(m0, m1) * -sl;
  m1 = m0;
  swap32(m0, m1);
  f32x2 e[8];
  sfor<0, 8>([&](auto II) {
    constexpr int I = decltype(II)::v;
    const float m = I < 4 ? m0 : m1;
    e[I] = __builtin_elementwise_fma(pr<2 * I>(P), f32x2{sl, sl}, f32x2{m, m});
  });
#pragma unroll
  for (int i = 0; i < 8; i++) e[i] = f32x2{__builtin_amdgcn_exp2f(e[i][0]), __builtin_amdgcn_exp2f(e[i][1])};
  const f32x2 s0 = (e[0] + e[1]) + (e[2] + e[3]), s1 = (e[4] + e[5]) + (e[6] + e[7]);
  m0 = s0[0] + s0[1];
  OPAQUE(m0);
  m1 = s1[0] + s1[1];
  swap32(m0, m1);
  m0 = __builtin_amdgcn_rcpf(m0 + m1);
  m1 = m0;
  swap32(m0, m1);
#pragma unroll
  for (int i = 0; i < 4; i++) {
    o0[i] = pk2(e[i] * m0);
    o1[i] = pk2(e[4 + i] * m1);
  }
}
DI u32x4 wphi_frag(u32 a, u32 sg) {
  u32x4 f = trd2(a, a + 256);
  f[0] ^= sg; f[1] ^= sg; f[2] ^= sg; f[3] ^= sg;
  return f;
}
DI void dual_softmax1(const f32x16& P, float sl, u32x4& o0, u32x4& o1) {
  float m0 = mx(mx(mx(mx(mx(mx(mx(P[0], P[1]), P[2]), P[3]), P[4]), P[5]), P[6]), P[7]);
  float m1 = mx(mx(mx(mx(mx(mx(mx(P[8], P[9]), P[10]), P[11]), P[12]), P[13]), P[14]), P[15]);
  swap32(m0, m1);
  m0 = mx(m0, m1) * -sl;
  m1 = m0;
  swap32(m0, m1);
  float e[16];
#pragma unroll
  for (int i = 0; i < 16; i++) {
    e[i] = __builtin_fmaf(P[i], sl, i < 8 ? m0 : m1);
    OPAQUE(e[i]);
  }
#pragma unroll
  for (int i = 0; i < 16; i++) e[i] = __builtin_amdgcn_exp2f(e[i]);
  m0 = e[0];
  m1 = e[8];
#pragma unroll
  for (int i = 1; i < 8; i++) {
    m0 += e[i];
    OPAQUE(m0);
    m1 += e[8 + i];
    OPAQUE(m1);
  }
  swap32(m0, m1);
  m0 = __builtin_amdgcn_rcpf(m0 + m1);
  m1 = m0;
  swap32(m0, m1);
#pragma unroll
  for (int i = 0; i < 4; i++) {
    o0[i] = pk(mul1(e[2 * i], m0), mul1(e[2 * i + 1], m0));
    o1[i] = pk(mul1(e[8 + 2 * i], m1), mul1(e[9 + 2 * i], m1));
  }
}
DI void phi_put(u32 apw, u32x4 o0, u32x4 o1) {
  *(LAS u32x2*)(apw) = u32x2{o0[0], o0[1]};
  *(LAS u32x2*)(apw ^ 16) = u32x2{o0[2], o0[3]};
  *(LAS u32x2*)(apw ^ 32) = u32x2{o1[0], o1[1]};
  *(LAS u32x2*)(apw ^ 48) = u32x2{o1[2], o1[3]};
}
DI void phi_store(u32 apw, const f32x16& P, float sl) {
  u32x4 o0, o1;
  dual_softmax(P, sl, o0, o1);
  phi_put(apw, o0, o1);
}
template <int S> DI u32x4 pack8(const f32x16& a) { return u32x4{pk2(pr<8 * S>(a)), pk2(pr<8 * S + 2>(a)), pk2(pr<8 * S + 4>(a)), pk2(pr<8 * S + 6>(a))}; }

constexpr u32 slot_loc(int s, int k) { return k < 2 ? lx(s) + k * 8192 : k < 4 ? lwa(s) + (k - 2) * 8192 : lwb(s); }
constexpr u32 bloc(int n) {
  return n < 5 ? slot_loc(2, n) : n < 10 ? slot_loc(0, n - 5) : n < 15 ? slot_loc(1, n - 10) : n < 40 ? LBS + (n & 1) * 8192 : LBS + (n - 40) * 8192;
}

#define STAMP(c, i) do {} while (0)
#define HSTAMP(c, i) do {} while (0)
struct Ctx {
  mutable u32 dbg;
  __amdgpu_buffer_rsrc_t ws;
  char* wsp;
  u32 m, lane;
  u32 ab[3][2];
  u32 par;
  u64 tag;
  u64 own;
  u32 fl;
};
template <int N> DI u32x4 rdb(const Ctx& c, int cb) {
  constexpr u32 W = bloc(N) / 57344;
  const u32 a = c.ab[W][cb] + (bloc(N) - W * 57344);
  return trd2(a, a + 1024);
}
DI void init_ab(Ctx& c) {
  const u32 lane = c.lane, m = c.m, hh = lane >> 5, blk = (lane >> 4) & 1, q = (lane & 15) >> 2, p = lane & 3;
#pragma unroll
  for (int cb = 0; cb < 2; cb++) {
    const u32 t = m * 2048 + (4 * hh + q) * 128 + (((4 * (cb ^ (m & 1)) + 2 * blk + (p >> 1)) ^ ((q >> 1) << 2)) << 4) + 8 * (p & 1);
#pragma unroll
    for (int k = 0; k < 3; k++) {
      c.ab[k][cb] = t + k * 57344;
      OPAQUE(c.ab[k][cb]);
    }
  }
}
DI void load_tag(Ctx& c, u32 tgt, u32 slot) {
  c.fl = MB_OFF + tgt * 1024 + slot * 8;
  asm volatile("global_load_dwordx2 %0, %1, %2 sc1" : "=v"(c.tag) : "v"(EP_OFF + tgt * 8), "s"(c.wsp) : "memory");
}
DI void set_tag(Ctx& c) {
  OPAQUE(c.tag);
  c.tag += 1;
  c.own = ((u64)(u32)__builtin_amdgcn_readlane((u32)(c.tag >> 32), 63) << 32) | (u32)__builtin_amdgcn_readlane((u32)c.tag, 63);
}
DI void publish(const Ctx& c, u64 mask) {
  asm volatile("s_mov_b64 exec, %3\n\tglobal_atomic_swap_x2 %0, %1, %2 sc1\n\ts_mov_b64 exec, -1" ::"v"(c.fl), "v"(c.tag), "s"(c.wsp), "s"(mask) : "memory");
}
DI u64 peek(const Ctx& c, u32 off, int n) {
  u64 v = c.own;
  const u64 z = 0;
  asm volatile("s_mov_b64 exec, %4\n\tglobal_atomic_or_x2 %0, %1, %2, %3 sc0 sc1\n\ts_mov_b64 exec, -1"
               : "+v"(v)
               : "v"(off + c.lane * 8), "v"(z), "s"(c.wsp), "s"((1ull << n) - 1)
               : "memory");
  return v;
}
DI void await(const Ctx& c, u32 off, int n, u64 v) {
  OPAQUE(v);
#pragma unroll 1
  for (int it = 0; it < (1 << 16) && !__all(v == c.own); it++) {
    if (SLP) __builtin_amdgcn_s_sleep(SLP);
    v = peek(c, off, n);
    asm volatile("s_waitcnt vmcnt(0)" : "+v"(v)::"memory");
  }
}
DI void sem_post(u32 idx) {
  asm volatile("s_mov_b64 exec, 1\n\tds_add_u32 %0, %1\n\ts_mov_b64 exec, -1" ::"v"(L_SEM + idx * 4), "v"(1u) : "memory");
}
DI void sem_wait(u32 idx, u32 n) {
  u32 v, sv, left = 1u << 20;
  asm volatile(
      "1:\n\tds_read_b32 %0, %3\n\ts_sub_u32 %2, %2, 1\n\ts_waitcnt lgkmcnt(0)\n\ts_cbranch_scc1 2f\n\tv_readfirstlane_b32 %1, %0\n\ts_nop 0\n\t"
      "s_cmp_lt_u32 %1, %4\n\ts_cbranch_scc1 1b\n2:"
      : "=&v"(v), "=&s"(sv), "+s"(left)
      : "v"(L_SEM + idx * 4), "s"(n)
      : "memory", "scc");
}
DI u32 sem_peek(u32 idx) { return *(volatile LAS u32*)(L_SEM + idx * 4); }
DI void sem_check(u32 v, u32 idx, u32 n) {
  if (__builtin_amdgcn_readfirstlane(v) < n) sem_wait(idx, n);
  asm volatile("" ::: "memory");
}
constexpr u32 SEM_B = 0, SEM_B2 = 1, SEM_B0 = 2, SEM_7 = 4, SEM_P = 5, SEM_S = 6, SEM_X = 7, SEM_K = 8, SEM_A = 12, SEM_KV = 13, SEM_C = 14, SEM_H = 15;
template <int N> DI u32 aset(const Ctx& c) {
  constexpr int e = N % 7, o = 7 + (e + 1) % 7;
  return c.par ? (N < 7 ? o : e) : (N < 7 ? e : o);
}
struct BSrc {
  u32x4 srd;
  u32 vb, sb, d2;
  DI void init(const Ctx& c, const u16* w, u32 rows, u32 sb_) {
    srd = make_srd(w, rows * 1024);
    vb = (c.lane >> 3) * 1024 + (((c.lane & 7) ^ (((c.lane >> 4) & 1) << 2)) << 4);
    sb = __builtin_amdgcn_readfirstlane(sb_);
    d2 = __builtin_amdgcn_readfirstlane((threadIdx.x & 192) * 32);
  }
  DI void init2(const Ctx& c, const u16* w2) {
    init(c, w2, 1792, ((blockIdx.x & 3) * 448 + (c.m >> 1) * 16) * 1024 + ((blockIdx.x >> 3) & 3) * 256 + (c.m & 1) * 128);
  }
  template <int N, int H> DI void issue1(const Ctx& c) const {
    dma<bloc(N) + H * 1024, H * 8192>(srd, vb, sb + (N < 28 ? aset<N>(c) * 32768 : (N - 28) * 16384), d2);
  }
  template <int N> DI void issue(const Ctx& c) const {
    issue1<N, 0>(c);
    issue1<N, 1>(c);
  }
};

struct Tile {
  int rt, j, ct;
  bool ffn;
  u32 ldw;
  u32 c0, c1, c2, cc;
  u32x4 srd_x, srd_w;
  u32 vx0, vw;
};
DI u32 blk_of(u32 rt, u32 ct) { return (((rt & 7) * 4 + (ct & 3)) << 3) + (rt >> 3) * 4 + (ct >> 2); }

template <int N> DI void put_block(const Ctx& c, const f32x16& a) {
  const u32 p = c.m * 2048 + c.lane * 16 + bloc(N);
  *(LAS u32x4*)p = pack8<0>(a);
  *(LAS u32x4*)(p + 1024) = pack8<1>(a);
}
template <int N> DI void get_block(const Ctx& c, u32 src, u32x4 (&f)[2]) {
  const u32 p = src * 2048 + c.lane * 16 + bloc(N);
  f[0] = lds128(p);
  f[1] = lds128(p + 1024);
}

#define PFCODE 40960
#define TOUCH 0
#define WARM 1
#define WN 1
#define WD 1
#define TLBW 0
#define OAUX 0
#define WARMO 0
#define PUBU 16
#define PKU 34
#define CKU 46
#define PAUSE 0
#define QK 14
#define QW 8
#define SMXA dual_softmax1
#define SG 0
#define WOFF 14
#define CW 8
#define NB7 4
constexpr int NCP = 8;
constexpr u32 CODE_BYTES = PFCODE;
DI void helper(Ctx& c, const Tile& t, const u16* x, const u16* wo, const u16* w2, u16* out) {
  constexpr int ND = 10;
  const u32 lane = c.lane, m = c.m;
  const int b = blockIdx.x, i = 2 * t.rt + ((b >> 1) & 1), jc = ((b >> 3) & 3) * 2 + (b & 1);
  const int hd = (b & 7) * 4 + ((b >> 3) & 3), ga = b >> 5;
  const u32 vx1 = t.vx0 ^ 64, vw1 = t.vw + 8 * t.ldw;
  const u32 vc = (lane >> 2) * 3584 + ((lane & 3) << 4);
  const u32 sx = (t.rt * 128 + m * 32) * 1024, sc = m * 16 * 3584 + t.cc;
  const u32 d4 = m * 4096, d2 = m * 2048, d1 = m * 1024;
  u32 wd = 0;
  const char *pb = (const char*)w2 + (b * 4 + m) * 1792, *pc = (const char*)wo + (b * 4 + m) * 512;
  auto warm = [&](auto KK) {
    constexpr int k = decltype(KK)::v - WOFF;
    if constexpr (!WARM || k < 0) {
    } else if constexpr (k < 28) warm1<k * 64>(wd, pb);
    else if constexpr (k < 36) warm1<(k - 28) * 64>(wd, pc);
  };
  auto ringf = [&](auto FF) {
    constexpr int G = decltype(FF)::v / ND, S = G % 3, I = decltype(FF)::v % ND;
    const u32 sg = (m * 16 + G * 64) * t.ldw;
    if constexpr (I == 4) {
      if (t.ffn) dma<lwb(S) + 8192, G * 64 * 3584, NTW>(t.srd_w, vc, sc, d1);
    }
    if constexpr (I < 4) dma<lx(S) + I * 1024, I * 8192 + G * 128, NTX>(t.srd_x, (I & 1) ? vx1 : t.vx0, sx, d4);
    else if constexpr (I < 6) dma<lwa(S) + (I & 1) * 1024, 0, NTW>(t.srd_w, (I & 1) ? vw1 : t.vw, sg + t.c0, d2);
    else if constexpr (I < 8) dma<lwa(S) + 8192 + (I & 1) * 1024, 0, NTW>(t.srd_w, (I & 1) ? vw1 : t.vw, sg + t.c1, d2);
    else dma<lwb(S) + (I & 1) * 1024, 0, NTW>(t.srd_w, (I & 1) ? vw1 : t.vw, sg + t.c2, d2);
  };
  constexpr auto rend = [](int G) { return G < 0 ? ND + 3 : G == 0 ? 2 * ND + 5 : G == 1 ? 3 * ND + 8 : G < 6 ? (G + 3) * ND : 8 * ND; };
  sfor<ND, rend(-1)>(ringf);
  load_tag(c, lane < 16 ? blk_of(t.rt, lane) : lane < 32 ? blk_of(lane & 15, t.ct) : lane < 40 ? blk_of(2 * ga + (m >> 1), (m & 1) * 8 + (lane & 1) * 4 + ((lane >> 1) & 3)) : b,
           lane < 16 ? 64 + t.ct : lane < 32 ? 80 + t.rt : 32 + hd);
  BSrc b2, bo;
  b2.init2(c, w2);
  bo.init(c, wo, 512, m * 128 * 1024 + jc * 128);
  const u32 hh = lane >> 5, blk = (lane >> 4) & 1, q = (lane & 15) >> 2, p = lane & 3, r = lane & 31;
  init_ab(c);
  u32x4 bf[8][2];
  auto ldb = [&](auto NN) {
    constexpr int N = decltype(NN)::v;
    bf[N - 28][0] = rdb<N>(c, 0);
    bf[N - 28][1] = rdb<N>(c, 1);
  };
  u64 ps = 0, grs = 0;
  f32x4 gc[4];
  __builtin_amdgcn_s_setprio(1);
  HSTAMP(c, 1);
  WAITV(4);
  BAR();
  HSTAMP(c, 2);
  constexpr auto wo_in = [](int G) { return G == 3 ? 28 : G == 4 ? 29 : G == 6 ? 30 : G == 7 ? 31 : -1; };
  float sv[4], scs;
  u32 a_cf = L_ST + (m * 64 + (lane & 15) * 4 + (lane >> 4)) * 8, a_fm = L_ST + 4096 + (m * 32 + r) * 8 + hh * 4, a_lr = L_RS + m * 128 + r * 4;
  u32 a_rs = RS_OFF + (t.rt * 128 + m * 32 + r) * 8, a_cs = CS_OFF + t.ct * 16384 + (lane >> 4) * 4096 + m * 256 + (lane & 15) * 16;
  OPAQUE(a_cf); OPAQUE(a_fm); OPAQUE(a_lr); OPAQUE(a_rs); OPAQUE(a_cs);
  sfor<0, 8>([&](auto GG) {
    constexpr int G = decltype(GG)::v;
    constexpr int NRB = G == 6 ? 10 : G == 7 ? 10 : 0;
    if constexpr (G == 5 || G == 6) ldb(IC<23 + G>{});
    if constexpr (G == SG + 1) {
      const u32 cv = *(LAS u32*)(L_ST + m * 2048 + lane * 4), rv = *(LAS u32*)(L_ST + ((lane >> 2) & 3) * 2048 + 1024 + (lane & 3) * 4);
      __builtin_amdgcn_raw_buffer_store_b32(cv, c.ws, lane * 4, CS_OFF + (t.ct * 16 + t.rt) * 1024 + m * 256, AUX);
      asm volatile("s_mov_b64 exec, %3\n\tglobal_store_dword %0, %1, %2 sc1\n\ts_mov_b64 exec, -1" ::"v"(RS_OFF + (t.rt * 128 + t.ct * 8) * 8 + (lane & 15) * 4),
                   "v"(rv), "s"(c.wsp), "s"(m == 0 ? 0xffffull : 0ull)
                   : "memory");
      set_tag(c);
    }
    if constexpr (G == SG + 2) publish(c, 0xffull << (8 * m));
    auto gather = [&]() {
      await(c, MB_OFF + b * 1024 + 512, 32, ps);
      gld64<0>(grs, a_rs, c.wsp);
      gld128<0>(gc[0], a_cs, c.wsp); gld128<1024>(gc[1], a_cs, c.wsp); gld128<2048>(gc[2], a_cs, c.wsp); gld128<3072>(gc[3], a_cs, c.wsp);
    };
    if constexpr (SG == 0 && G == 4) ps = peek(c, MB_OFF + b * 1024 + 512, 32);
    if constexpr (SG == 0 && G == 5) gather();
    sfor<rend(G - 1), rend(G)>([&](auto FF) {
      ringf(FF);
      constexpr int u = decltype(FF)::v - rend(-1);
      sfor<u * WN / WD, (u + 1) * WN / WD>(warm);
    });
    if constexpr (SG == 1 && G == 5) {
      WAITV(ND);
      gather();
    }
    if constexpr (G >= 6)
      sfor<(G - 6) * 5, (G - 6) * 5 + NRB / 2>([&](auto NN) {
        constexpr int N = decltype(NN)::v - SG;
        b2.issue<N + SG>(c);
        SB();
        if constexpr (N == 0) {
          if constexpr (SG == 1) WAITV(4);
#pragma unroll
          for (int k = 0; k < 4; k++) OPAQUE(gc[k]);
#pragma unroll
          for (int k = 0; k < 4; k++) {
            sv[k] = gc[0][k];
#pragma unroll
            for (int u = 1; u < 4; u++) {
              sv[k] += gc[u][k];
              OPAQUE(sv[k]);
            }
          }
        } else if constexpr (N == 1) {
          swap32(sv[0], sv[2]);
          swap32(sv[1], sv[3]);
          sv[0] += sv[2];
          OPAQUE(sv[0]);
          sv[1] += sv[3];
          auto sw16 = __builtin_amdgcn_permlane16_swap(__float_as_uint(sv[0]), __float_as_uint(sv[1]), false, false);
          scs = __uint_as_float(sw16[0]) + __uint_as_float(sw16[1]);
          OPAQUE(scs);
        } else if constexpr (N == 2) {
          const u32 h2 = pk(scs, scs);
          const float lo = scs - __uint_as_float(h2 & 0xffff0000u);
          *(LAS u32x2*)a_cf = u32x2{h2, pk(lo, lo)};
        } else if constexpr (N == 3) {
          OPAQUE(grs);
          *(LAS u32*)a_fm = (u32)grs;
          *(LAS u32*)a_lr = (u32)(grs >> 32);
        }
      });
    if constexpr (G >= 6) WAITL0();
    if constexpr (wo_in(G) > 0) bo.issue<wo_in(G)>(c);
    if constexpr (SG == 1 && G == 4) ps = peek(c, MB_OFF + b * 1024 + 512, 32);
    HSTAMP(c, 3 + 2 * G);
    WAITV(G < 3 ? rend(G) - (G + 2) * ND : rend(G) - rend(G - 1) + NRB + (wo_in(G) > 0 ? 2 : 0) + (SG == 1 && G == 4 ? 1 : SG == 1 && G == 5 ? 5 : 0));
    if constexpr (G < 7) BAR();
    else sem_post(SEM_B0);
    HSTAMP(c, 4 + 2 * G);
  });
  const u32 mb = MB_OFF + b * 1024;
  const u32 fk = mb + 128, fo = mb + 256 + 64 * m, fq = mb + 768 + 16 * m;
  const u32 awp = (4 * hh + q) * 32 + p * 8, sg = blk ? 0x80008000u : 0u;
  asm volatile("" : "+s"(wd));
  u64 pq;
  if (!t.ffn) {
    const u32x4 w0 = wphi_frag(L_WPHI + 1024 + awp, sg), w1 = wphi_frag(L_WPHI + 1536 + awp, sg);
    const float sl = __uint_as_float((u32)(grs >> 32)) * LOG2E;
    sem_wait(SEM_K + m, 1);
    const u32 ak = ((m >> 1) ? L_SP2 : L_SP1) + (m & 1) * 2048 + lane * 16;
    const f32x16 P0 = mfma(w0, lds128(ak), f32x16{}), P1 = mfma(w1, lds128(ak + 1024), f32x16{});
    u32x4 o0, o1;
    dual_softmax1(P0, sl, o0, o1);
    OPAQUE(o0); OPAQUE(o1);
    sem_wait(SEM_7, 4);
    const u32 apw = lwa(1) + m * 2048 + r * 64 + ((hh ^ ((r >> 1) & 7)) << 3);
    phi_put(apw, o0, o1);
    phi_store(apw + 8192, P1, sl);
    sem_post(SEM_P);
  } else {
    sem_wait(SEM_K + m, 1);
  }
  __builtin_amdgcn_s_setprio(2);
  pq = peek(c, fq, 2);
  const u32x4 wp = wphi_frag(L_WPA + (hd & 1) * 512 + awp, sg);
  WAITV(0);
  sem_post(SEM_B);
  STAMP(c, 20);
  await(c, fq, 2, pq);
  STAMP(c, 24);
  u32x4 p00, p01, p10, p11;
  u64 pkv;
  float rs0, rs1;
  {
    u32x4 qf[2];
#pragma unroll
    for (int u = 0; u < 2; u++) qf[u] = ld128(c.ws, lane * 16, QF_OFF + ((8 * ga + 2 * m + u) * 32 + hd) * 1024);
    rs0 = __uint_as_float(__builtin_amdgcn_raw_buffer_load_b32(c.ws, lane * 8 + 4, RS_OFF + (8 * ga + 2 * m) * 256, AUX));
    ldb(IC<30>{});
    ldb(IC<31>{});
    rs1 = rs0;
    swap32(rs0, rs1);
    const f32x16 Q0 = mfma(wp, qf[0], f32x16{}), Q1 = mfma(wp, qf[1], f32x16{});
    SMXA(Q0, rs0 * LOG2E, p00, p01);
    OPAQUE(p00); OPAQUE(p01);
    SB();
    pkv = peek(c, fk, 16);
    SMXA(Q1, rs1 * LOG2E, p10, p11);
    OPAQUE(p10); OPAQUE(p11);
  }
  STAMP(c, 21);
  WAITV(0);
  OPAQUE(pkv);
#pragma unroll 1
  for (int it = 0; it < (1 << 16); it++) {
    if (__all(pkv == c.own)) {
      sem_post(SEM_S);
      break;
    }
    if (__builtin_amdgcn_readfirstlane(sem_peek(SEM_S))) break;
    pkv = peek(c, fk, 16);
    asm volatile("s_waitcnt vmcnt(0)" : "+v"(pkv)::"memory");
  }
  STAMP(c, 25);
  {
    f32x4 part[8];
#pragma unroll
    for (int s = 0; s < 8; s++) part[s] = __builtin_bit_cast(f32x4, ld128(c.ws, hh * 2048 + r * 16, KV_OFF + (hd * 16 + 2 * s) * 2048 + m * 512));
    f32x4 z = __builtin_bit_cast(f32x4, ld128(c.ws, (lane & 15) * 128 + blk * 16, KZ_OFF + hd * 2048 + m * 32));
    SB();
    float v[4] = {part[0][0], part[0][1], part[0][2], part[0][3]};
#pragma unroll
    for (int s = 1; s < 8; s++)
#pragma unroll
      for (int k = 0; k < 4; k++) {
        v[k] += part[s][k];
        OPAQUE(v[k]);
      }
    swap32(v[0], v[2]);
    swap32(v[1], v[3]);
    v[0] += v[2];
    OPAQUE(v[0]);
    *(LAS u32*)(L_SP0 + r * 32 + m * 8 + hh * 4) = pk(v[0], v[1] + v[3]);
    float za = hh ? z[2] : z[0], zb = hh ? z[3] : z[1];
    sfor<0, 4>([&](auto KK) {
      za += __int_as_float(__builtin_amdgcn_update_dpp(0, __float_as_int(za), 0x120 + (8 >> decltype(KK)::v), 0xf, 0xf, false));
      zb += __int_as_float(__builtin_amdgcn_update_dpp(0, __float_as_int(zb), 0x120 + (8 >> decltype(KK)::v), 0xf, 0xf, false));
    });
    *(LAS u32*)(L_SP0 + (32 + blk) * 32 + m * 8 + hh * 4) = pk(za, zb);
    sem_post(SEM_A);
    sem_wait(SEM_A, 4);
    const u32 e = blk == 0 ? hh * 16 + (lane & 15) : 32 + hh;
    const u32x4 k0 = lds128(L_SP0 + e * 32), k1 = lds128(L_SP0 + e * 32 + 16);
    f32x16 Z0 = mfma(k0, p00, f32x16{}), Z1 = mfma(k0, p10, f32x16{});
    Z0 = mfma(k1, p01, Z0);
    Z1 = mfma(k1, p11, Z1);
    constexpr int NO = 0;
    const float r0 = __builtin_amdgcn_rcpf(mul1(Z0[8 - NO] + 1e-6f, rs0)), r1 = __builtin_amdgcn_rcpf(mul1(Z1[8 - NO] + 1e-6f, rs1));
    u32x4 o0, o1;
#pragma unroll
    for (int d = 0; d < 4; d++) {
      o0[d] = pk(mul1(Z0[NO + 2 * d], r0), mul1(Z0[NO + 2 * d + 1], r0));
      o1[d] = pk(mul1(Z1[NO + 2 * d], r1), mul1(Z1[NO + 2 * d + 1], r1));
    }
    st128(o0, c.ws, lane * 16, OF_OFF + ((8 * ga + 2 * m) * 32 + hd) * 1024);
    st128(o1, c.ws, lane * 16, OF_OFF + ((8 * ga + 2 * m + 1) * 32 + hd) * 1024);
  }
  STAMP(c, 26);
  bo.issue<32>(c);
  bo.issue<33>(c);
  WAITV(4);
  publish(c, 0xff00000000ull);
  sem_post(SEM_X);
  STAMP(c, 27);
  WAITV(1);
  ldb(IC<32>{});
  ldb(IC<33>{});
  WAITL0();
  bo.issue<34>(c);
  bo.issue<35>(c);
  const u32 orow = 64 * i + 32 * (m >> 1) + r, oidx = orow * 512 + 64 * jc + 32 * (m & 1) + 8 * hh;
  u32x4 xr[2] = {*(const GAS u32x4*)(x + oidx), *(const GAS u32x4*)(x + oidx + 16)};
  float frs = __uint_as_float(__builtin_amdgcn_raw_buffer_load_b32(c.ws, orow * 8 + 4, RS_OFF, AUX));
  u64 po = peek(c, fo, 8);
  const u32 fx = mb + 832 + (m >> 1) * 32;
  u32x4 sel[2];
  {
    const u32 one = (((r >> 2) & 1) == hh) ? ((r & 1) ? 0x3f800000u : 0x3f80u) : 0u, dw = ((r >> 3) & 1) * 2 + ((r >> 1) & 1);
#pragma unroll
    for (int d = 0; d < 4; d++) {
      sel[0][d] = (dw == (u32)d && blk == 0) ? one : 0u;
      sel[1][d] = (dw == (u32)d && blk == 1) ? one : 0u;
    }
    OPAQUE(sel[0]); OPAQUE(sel[1]);
  }
  WAITV(0);
  OPAQUE(xr[0]); OPAQUE(xr[1]); OPAQUE(frs);
  ldb(IC<34>{});
  ldb(IC<35>{});
  await(c, fo, 8, po);
  STAMP(c, 30);
  u64 px = peek(c, fx, 4);
  u32x4 af[8][2];
#pragma unroll
  for (int n = 0; n < 8; n++)
#pragma unroll
    for (int u = 0; u < 2; u++) af[n][u] = ld128(c.ws, lane * 16, OF_OFF + ((2 * i + ((m >> 1) ^ u)) * 32 + 8 * m + n) * 1024);
  WAITV(16);
  OPAQUE(px);
  auto finish = [&](auto EE) {
  constexpr bool EARLY = decltype(EE)::v;
  u32x4 f[7][2];
  auto ldx = [&]() {
#pragma unroll
    for (int k = 0; k < 4; k++)
#pragma unroll
      for (int u = 0; u < 2; u++) f[3 + k][u] = ld128(c.ws, lane * 16, XP_OFF + b * 32768 + k * 8192 + m * 2048 + u * 1024);
    SB();
  };
  if constexpr (EARLY) ldx();
  f32x16 acc[4];
  constexpr int NSM = 4;
  sfor<0, NSM>([&](auto NN) {
    constexpr int N = decltype(NN)::v;
    if constexpr (N == 0) {
      mma0(acc[0], bf[N][0], af[N][0]); mma0(acc[1], bf[N][1], af[N][0]); mma0(acc[2], bf[N][0], af[N][1]); mma0(acc[3], bf[N][1], af[N][1]);
    } else {
      mma(acc[0], bf[N][0], af[N][0]); mma(acc[1], bf[N][1], af[N][0]); mma(acc[2], bf[N][0], af[N][1]); mma(acc[3], bf[N][1], af[N][1]);
    }
  });

  sfor<0, 4>([&](auto KK) {
    constexpr int k = 3 - decltype(KK)::v;
    sfor<NSM, 8>([&](auto NN) {
      constexpr int N = decltype(NN)::v;
      mma(acc[k], bf[N][k & 1], af[N][k >> 1]);
      if constexpr (N == NSM + 1 && k < 3) {
        SB();
        put_block<40 + k>(c, acc[k + 1]);
        if constexpr (k == 0) sem_post(SEM_H);
        SB();
      }
    });
  });
  STAMP(c, 31);
  {
    if constexpr (!EARLY) {
      await(c, fx, 4, px);
      ldx();
    }
    STAMP(c, 32);
    sem_wait(SEM_H, 4);
    get_block<40>(c, m ^ 1, f[0]);
    get_block<41>(c, m ^ 2, f[1]);
    get_block<42>(c, m ^ 3, f[2]);
    sem_wait(SEM_C, 4);
#pragma unroll
    for (int k = 0; k < 7; k++) { mma(acc[0], sel[0], f[k][0]); mma(acc[0], sel[1], f[k][1]); }
    STAMP(c, 33);
  }
  if (threadIdx.x == 319) __hip_atomic_store((GAS u64*)(c.wsp + EP_OFF + b * 8), c.tag, __ATOMIC_RELAXED, __HIP_MEMORY_SCOPE_AGENT);
  asm volatile("s_nop 15" : "+v"(acc[0]));
#pragma unroll
  for (int h = 0; h < 2; h++) {
    float e[8];
#pragma unroll
    for (int k = 0; k < 8; k++) e[k] = acc[0][8 * h + k];
#pragma unroll
    for (int k = 0; k < 4; k++) swap32(e[k], e[4 + k]);
    u32x4 o;
#pragma unroll
    for (int d = 0; d < 4; d++)
      o[d] = pk(__builtin_fmaf(e[2 * d], frs, __uint_as_float(xr[h][d] << 16)), __builtin_fmaf(e[2 * d + 1], frs, __uint_as_float(xr[h][d] & 0xffff0000u)));
    __builtin_amdgcn_raw_buffer_store_b128(o, __builtin_amdgcn_make_buffer_rsrc(out, 0, 2048 * 1024, 0x00020000), oidx * 2 + 32 * h, 0, OAUX);
  }
  };
  if (__all(px == c.own)) finish(IC<1>{});
  else finish(IC<0>{});
}

template <bool FFN> struct Slots {
  static constexpr int NBK = FFN ? 7 : 6, N7 = 28 * NBK, NS = N7 + 5 * NBK;
  static constexpr int blk(int n) {
    if (n < N7) return n % NBK;
    const int k = (n - N7) / 5;
    return (!FFN && k < 4) ? (k + 1) & 3 : k;
  }
  static constexpr int step(int n) { return n < N7 ? n / NBK : ((n - N7) % 5 == 0 ? -1 : 27 + (n - N7) % 5); }
  static constexpr int bar(int G) { return (G < 7 ? (4 * G + 3) * NBK : NS - NBK) + 1; }
};
template <bool FFN>
DI void compute1(Ctx& c, const Tile& t, const u16* w2) {
  u32x4 xa, xb;
  using SL = Slots<FFN>;
  constexpr int NBK = SL::NBK;
  const u32 lane = c.lane, m = c.m;
  const int rt = t.rt, j = t.j;
  const u32 hh = lane >> 5, blk = (lane >> 4) & 1, q = (lane & 15) >> 2, p = lane & 3, r = lane & 31;
  u32 ax[4], aw[2][2], ac;
#pragma unroll
  for (int s = 0; s < 4; s++) ax[s] = m * 4096 + r * 128 + (((2 * s + hh) ^ ((r >> 1) & 7)) << 4);
  const u32 at = (((2 * blk + (p >> 1)) ^ ((q >> 1) << 2)) << 4) + 8 * (p & 1);
  {
    const u32 a = (8 * hh + q) * 128 + at;
    aw[0][0] = a + LWA; aw[0][1] = (a ^ 64) + LWA; aw[1][0] = a + LWB; aw[1][1] = (a ^ 64) + LWB;
    ac = (8 * hh + q) * 64 + 32 * blk + 8 * p + LWB + 8192;
    OPAQUE(aw[0][0]); OPAQUE(aw[0][1]); OPAQUE(aw[1][0]); OPAQUE(aw[1][1]); OPAQUE(ac);
  }
  u32 acs = L_ST + r * 8;
  OPAQUE(acs);
  auto rdw = [&](auto NN) -> u32x4 {
    constexpr int T = SL::step(decltype(NN)::v), M = SL::blk(decltype(NN)::v);
    if constexpr (T < 0) {
      const u32x2 v = *(LAS u32x2*)(acs + M * 256);
      return __builtin_shufflevector(v, v, 0, 1, -1, -1);
    }
    else {
      constexpr int S = (T >> 2) % 3, s = T & 3;
      if constexpr (M < 6) {
        const u32 a = (M < 4 ? aw[0][M & 1] + S * 16384 + (M >> 1) * 8192 : aw[1][M & 1] + S * 12288) + s * 2048;
        return trd2(a, a + 512);
      } else {
        const u32 a = ac + S * 12288 + s * 1024;
        return trd2(a, a + 256);
      }
    }
  };
  u32x4 xf[4], wf[2 * NBK];
  u32x2 fm;
  f32x16 acc[NBK], P0, P1;
  float rstd;
  u32x2 sv[8];
  u32x4 eo, af[28];
  float sa;
  const u32 tb = rt * 4 + m;
  const u32 obase = FFN ? HF_OFF + tb * 112 * 1024 : QF_OFF + (tb * 32 + 4 * j) * 1024;
  WAITL0();
  STAMP(c, 1);
  WAITV(NCP);
  BAR();
  STAMP(c, 2);
  xf[0] = lds128(ax[0]);
  sfor<0, NBK>([&](auto NN) { wf[decltype(NN)::v] = rdw(NN); });
  SB();
  auto filler = [&](auto NN) {
    constexpr int n = decltype(NN)::v;
    if constexpr (n == SL::bar(SG) - 5) {
      WAITV(0);
      xa = lds128(L_ST + m * 2048 + lane * 16);
      xb = lds128(L_ST + m * 2048 + 1024 + lane * 16);
      const u32 a = LBS + m * 4096 + (4 * hh + q) * 128 + at;
#pragma unroll
      for (int k = 0; k < 8; k++) sv[k] = trd((a ^ ((k >> 2) * 64)) + (k & 3) * 1024);
      set_tag(c);
    }
    if constexpr (n == SL::bar(SG) - 2) {
      u32 ones = ONES2;
      asm volatile("" : "+s"(ones));
      float st[4] = {0.f, 0.f, 0.f, 0.f}, cs[2] = {0.f, 0.f};
#pragma unroll
      for (int d = 0; d < 4; d++) {
        st[0] = dot2(ones, xa[d], st[0]);
        st[1] = dot2(xa[d], xa[d], st[1]);
        st[2] = dot2(ones, xb[d], st[2]);
        st[3] = dot2(xb[d], xb[d], st[3]);
      }
#pragma unroll
      for (int d = 0; d < 8; d++) {
        cs[0] = dot2(ones, sv[d >> 1][d & 1], cs[0]);
        cs[1] = dot2(ones, sv[4 + (d >> 1)][d & 1], cs[1]);
      }
      swap32(st[0], st[1]);
      swap32(st[2], st[3]);
      swap32(cs[0], cs[1]);
      *(LAS float*)(L_ST + m * 2048 + lane * 4) = cs[0] + cs[1];
      st[0] += st[1];
      OPAQUE(st[0]);
      st[2] += st[3];
      auto sw16 = __builtin_amdgcn_permlane16_swap(__float_as_uint(st[0]), __float_as_uint(st[2]), false, false);
      sa = __uint_as_float(sw16[0]) + __uint_as_float(sw16[1]);
      OPAQUE(sa);
    }
    if constexpr (n == SL::bar(SG) - 1) {
      sfor<0, 4>([&](auto KK) { sa += __int_as_float(__builtin_amdgcn_update_dpp(0, __float_as_int(sa), 0x120 + (8 >> decltype(KK)::v), 0xf, 0xf, false)); });
      float mean = sa * (1.f / 512.f), ms = mean;
      swap32(mean, ms);
      ms = __builtin_amdgcn_rsqf(mx(__builtin_fmaf(-mean, mean, ms), 0.f) + 1e-5f);
      const u32 mh = pk(-mean, -mean);
      const float ml = -mean - __uint_as_float(mh & 0xffff0000u);
      *(LAS u32x2*)(L_ST + m * 2048 + 1024 + (((lane >> 4) & 1) | ((lane & 15) << 1) | (lane & 32)) * 8) = u32x2{(mh & 0xffffu) | (pk(ml, ml) & 0xffff0000u), __float_as_uint(ms)};
    }
    if constexpr (n >= SL::N7 + 5) {
      constexpr int g = (n - SL::N7) % 5, M = SL::blk(n - 5);
      if constexpr (g >= 1) {
        constexpr int s = (g - 1) >> 1, h = (g - 1) & 1;
        if constexpr (FFN || M < 4) {
          eo[2 * h] = pk2(pr<8 * s + 4 * h>(acc[M]));
          eo[2 * h + 1] = pk2(pr<8 * s + 4 * h + 2>(acc[M]));
          if constexpr (FFN) {
            eo[2 * h] = relu2(eo[2 * h]);
            eo[2 * h + 1] = relu2(eo[2 * h + 1]);
          }
          if constexpr (h == 0) OPAQUE(eo);
        }
        if constexpr (h == 1) {
          if constexpr (FFN) {
            af[2 * M + s] = eo;
            st128(eo, c.ws, lane * 16, obase + ((t.c0 >> 5) + 2 * M + s) * 1024);
          }
          else if constexpr (M == 1) {
            *(LAS u32x4*)(((m >> 1) ? L_SP2 : L_SP1) + (m & 1) * 2048 + s * 1024 + lane * 16) = eo;
            if constexpr (s == 1) sem_post(SEM_K + m);
          } else if constexpr (M == 0) {
            const u32x4 wp = wphi_frag(L_WPHI + s * 512 + (4 * hh + q) * 32 + p * 8, blk ? 0x80008000u : 0u);
            asm volatile("s_nop 1\n\tv_mfma_f32_32x32x16_bf16 %0, %1, %2, 0" : "=&v"(s ? P1 : P0) : "v"(wp), "v"(eo));
          } else if constexpr (M < 4) st128(eo, c.ws, lane * 16, obase + (2 * (M - 2) + s) * 1024);
        }
      }
    }
  };
  sfor<0, SL::NS>([&](auto NN) {
    constexpr int n = decltype(NN)::v, T = SL::step(n), M = SL::blk(n);
    constexpr bool TR = !FFN && M >= 4;
    const u32x4 xo = xf[T & 3], wo_ = wf[n % (2 * NBK)];
    if constexpr (T < 0) {
      const u32x2 cf = __builtin_shufflevector(wo_, wo_, 0, 1);
      mma8(acc[M], TR ? fm : cf, TR ? cf : fm);
    } else if constexpr (T == 0) mma0(acc[M], TR ? xo : wo_, TR ? wo_ : xo);
    else mma(acc[M], TR ? xo : wo_, TR ? wo_ : xo);
    constexpr int G = n < SL::N7 ? n / (4 * NBK) : 7;
    if constexpr (n == SL::bar(G) && G < 7) {
      WAITL0();
      STAMP(c, 3 + 2 * G);
      BAR();
      STAMP(c, 4 + 2 * G);
    }
    if constexpr (n + NBK < SL::NS && n != SL::bar(G) - 1) {
      if constexpr (n < SL::N7 && (n == SL::bar(G) || (M == 0 && (T & 3) != 3))) {
        if constexpr (T == 27) {
          fm = *(LAS u32x2*)(hh ? L_ZERO : L_ST + 4096 + (m * 32 + r) * 8);
          xf[0] = lds128(ax[0] + 16384); xf[1] = lds128(ax[1] + 16384); xf[2] = lds128(ax[2] + 16384);
        } else xf[(T + 1) & 3] = lds128(ax[(T + 1) & 3] + (((T + 1) >> 2) % 3) * 16384);
      }
      if constexpr (n == SL::bar(G)) wf[(n - 1 + NBK) % (2 * NBK)] = rdw(IC<n - 1 + NBK>{});
      wf[(n + NBK) % (2 * NBK)] = rdw(IC<n + NBK>{});
      if constexpr (n == SL::N7) {
        xf[3] = lds128(ax[3] + 16384);
        if constexpr (!FFN) rstd = *(LAS float*)(L_RS + m * 128 + r * 4);
      }
    }
    if constexpr (n == SL::N7) __builtin_amdgcn_s_setprio(P7);
    if constexpr (n == SL::NS - NBK - 1) sem_post(SEM_7);
    if constexpr (FFN && n == SL::N7 + QK) sem_post(SEM_K + m);
    filler(NN);
    SB();
  });
  STAMP(c, 18);
  __builtin_amdgcn_s_setprio(3);
  u32 ab[3][2];
#pragma unroll
  for (int k = 0; k < 6; k++) {
    ab[k >> 1][k & 1] = (((4 * hh + q) * 128 + at) ^ ((k & 1) * 64)) + (k >> 1) * 57344;
    OPAQUE(ab[k >> 1][k & 1]);
  }
  constexpr auto stp = [](int u) { return u < 96 ? u >> 2 : 24 + (u & 3); };
  constexpr auto blo = [](int u) { return u < 96 ? u & 3 : (u - 96) >> 2; };
  auto rd2 = [&](auto UU) -> u32x4 {
    constexpr int n = stp(decltype(UU)::v), k = blo(decltype(UU)::v);
    constexpr u32 L = bloc(n >> 1), W = L / 57344;
    const u32 a = ab[W][k & 1] + (L - W * 57344) + ((n & 1) * 2 + (k >> 1)) * 2048;
    return trd2(a, a + 1024);
  };
  u32x4 w2f[8];
  if constexpr (FFN) {
    sem_wait(SEM_B0, 4);
    sfor<0, 4>([&](auto UU) { w2f[decltype(UU)::v] = rd2(UU); });
    asm volatile("s_nop 15" : "+v"(acc[6]));
    sfor<0, 2>([&](auto SS) {
      u32x4 o = pack8<decltype(SS)::v>(acc[6]);
#pragma unroll
      for (int d = 0; d < 4; d++) o[d] = relu2(o[d]);
      af[12 + decltype(SS)::v] = o;
      st128(o, c.ws, lane * 16, obase + ((t.cc >> 5) + decltype(SS)::v) * 1024);
    });
  } else {
    const u32 apw = lx(1) + m * 2048 + r * 64 + ((hh ^ ((r >> 1) & 7)) << 3);
    WAITV(0);
    publish(c, 0xf00ull);
    sem_wait(SEM_7, 4);
    phi_store(apw, P0, rstd * LOG2E);
    SB();
    const u32 av0 = L_ST + m * 2048 + lane * 16, av1 = lwb(1) + m * 2048 + lane * 16;
    sfor<0, 2>([&](auto TT) {
      constexpr int tt = decltype(TT)::v;
      const f32x4 ra = ldsf4(L_RS + m * 128 + (16 * tt + 4 * hh) * 4), rb = ldsf4(L_RS + m * 128 + (16 * tt + 8 + 4 * hh) * 4);
      const f32x4 a0 = q4<8 * tt>(acc[4]) * ra, b0 = q4<8 * tt + 4>(acc[4]) * rb, a1 = q4<8 * tt>(acc[5]) * ra, b1 = q4<8 * tt + 4>(acc[5]) * rb;
      *(LAS u32x4*)(av0 + tt * 1024) = u32x4{pk(a0[0], a0[1]), pk(a0[2], a0[3]), pk(b0[0], b0[1]), pk(b0[2], b0[3])};
      *(LAS u32x4*)(av1 + tt * 1024) = u32x4{pk(a1[0], a1[1]), pk(a1[2], a1[3]), pk(b1[0], b1[1]), pk(b1[2], b1[3])};
    });
    phi_store(apw + 8192, P1, rstd * LOG2E);
    sem_post(SEM_P);
    STAMP(c, 19);
    sem_wait(SEM_P, 8);
    const u32 t0 = 4 * hh + q;
    const u32 apr = ((m >> 1) ? lwa(1) : lx(1)) + (m & 1) * 8192 + t0 * 64 + (((4 * blk + p) ^ (t0 >> 1)) << 3);
    const u32 avr = ((m >> 1) ? lwb(1) : L_ST) + lane * 16;
    const bool isv = blk == (m & 1);
    u32x4 pa[8], vb[8];
#pragma unroll
    for (int k = 0; k < 8; k++) {
      pa[k] = trd2(apr + k * 1024, (apr ^ 32) + k * 1024 + 512);
      vb[k] = lds128(avr + k * 1024);
    }
    sem_post(SEM_KV);
    f32x16 X = f32x16{};
#pragma unroll
    for (int k = 0; k < 8; k++) {
#pragma unroll
      for (int d = 0; d < 4; d++) vb[k][d] = isv ? vb[k][d] : ONES2;
      X = mfma(pa[k], vb[k], X);
    }
    const u32 pi = (4 * j + m) * 16 + rt;
    const u32 ve = isv ? KV_OFF + pi * 2048 + (hh * 16 + (lane & 15)) * 16 : KZ_OFF + pi * 128 + hh * 16, vs = isv ? 512 : 32;
    st128(__builtin_bit_cast(u32x4, q4<0>(X)), c.ws, ve, 0);
    st128(__builtin_bit_cast(u32x4, q4<4>(X)), c.ws, ve + vs, 0);
    st128(__builtin_bit_cast(u32x4, q4<8>(X)), c.ws, ve + 2 * vs, 0);
    st128(__builtin_bit_cast(u32x4, q4<12>(X)), c.ws, ve + 3 * vs, 0);
  }
  STAMP(c, 20);
  __builtin_amdgcn_s_setprio(0);
  const int b = blockIdx.x;
  BSrc b2;
  b2.init2(c, w2);
  const u32 fh = MB_OFF + b * 1024 + 16 * m + (FFN ? 8 - 8 * c.par : 0);
  const u32 ah = HF_OFF + (tb * 112 + 28 * (b & 3)) * 1024;
  auto lda = [&](auto NN) {
    constexpr int N = decltype(NN)::v;
    af[N] = ld128(c.ws, lane * 16, ah + (2 * aset<(N >> 1)>(c) + (N & 1)) * 1024);
  };
  u64 ph;
  if constexpr (!FFN) {
    ph = peek(c, fh, 2);
    WAITV(1);
    publish(c, 0xffull);
    STAMP(c, 23);
    sem_wait(SEM_KV, 4);
    sfor<10, 14>([&](auto NN) { b2.issue<decltype(NN)::v>(c); });
    sfor<0, 4>([&](auto UU) { w2f[decltype(UU)::v] = rd2(UU); });
    WAITV(9);
    await(c, fh, 2, ph);
    STAMP(c, 25);
    sfor<0, QW>(lda);
    SB();
  } else sem_wait(SEM_7, 4);
  const u32 xb_ = XP_OFF + (b & 3) * 8192 + (m & 1) * 4096;
  const u32 xo0 = xb_ + blk_of(rt, (m >> 1) * 8 + ((b >> 3) & 3)) * 32768, xo1 = xb_ + blk_of(rt, (m >> 1) * 8 + 4 + ((b >> 3) & 3)) * 32768;
  auto send = [&](const f32x16& a, int k) {
    st128(pack8<0>(a), c.ws, lane * 16, (k >> 1 ? xo1 : xo0) + (k & 1) * 2048);
    st128(pack8<1>(a), c.ws, lane * 16, (k >> 1 ? xo1 : xo0) + (k & 1) * 2048 + 1024);
  };
  f32x16 ac2[4];
  u32 sp = 0, sx = 0, pz = 0;
  STAMP(c, 27);
  SB();
  sfor<0, 112>([&](auto UU) {
    constexpr int u = decltype(UU)::v, n = stp(u), k = blo(u);
    if constexpr (u < 4) mma0(ac2[k], w2f[u & 7], af[n]);
    else mma(ac2[k], w2f[u & 7], af[n]);
    SB();
    if constexpr (u == 32 || u == 72) sp = sem_peek(u == 32 ? SEM_B : SEM_B2);
    if constexpr (u == 35 || u == 75) sem_check(sp, u == 35 ? SEM_B : SEM_B2, 4);
    if constexpr ((PAUSE & (FFN ? 1 : 2)) && u < 96) {
      if constexpr ((u & 7) == 5) sx = sem_peek(SEM_S);
      if constexpr ((u & 7) == 0 && u > 0) {
        if (__builtin_amdgcn_readfirstlane(sx) > pz) {
          sem_wait(SEM_X, 4);
          pz = 4;
        }
      }
    }
    if constexpr (u + 4 < 112) w2f[(u + 4) & 7] = rd2(IC<(u + 4 < 112 ? u + 4 : 0)>{});
    if constexpr (FFN) {
      if constexpr (u < 8) b2.template issue1<10 + (u < 8 ? u >> 1 : 0), (u & 1)>(c);
      if constexpr (u == PUBU) {
        WAITV(8);
        publish(c, 0xfull);
        STAMP(c, 23);
      }
      if constexpr (u == PKU) ph = peek(c, fh, 1);
      if constexpr (u == CKU) {
        WAITV(0);
        sem_post(SEM_B2);
        STAMP(c, 24);
        await(c, fh, 1, ph);
        STAMP(c, 25);
        sfor<14, 28>(lda);
      }
    } else {
      if constexpr (u == 0) sem_post(SEM_B2);
      if constexpr ((u & 3) == 1 && (u >> 2) + QW < 28) lda(IC<((u >> 2) + QW < 28 ? (u >> 2) + QW : 0)>{});
    }
    if constexpr (u >= 100 && (u & 3) == 1) send(ac2[k - 1], k - 1);
    SB();
  });
  STAMP(c, 31);
  asm volatile("s_nop 15" : "+v"(ac2[3]));
  send(ac2[3], 3);
  WAITV(0);
  publish(c, 0x3000ull);
  sem_post(SEM_C);
  STAMP(c, 32);
}

#define ROTB 1
__global__ __launch_bounds__(512, 2) void fused_k(const u16* x, const u16* wqkv, const u16* wphi, const u16* wo, const u16* w1, const u16* w2, u16* out,
                                                  char* ws) {
  __shared__ __attribute__((aligned(16))) char smem[163840];
  asm volatile("" ::"v"((LAS char*)smem));
  const int b = blockIdx.x, xcd = b & 7, r = b >> 3;
  u64 pc0;
  asm volatile("s_getpc_b64 %0" : "=s"(pc0));
  {
    u32 off = ((r * 8 + (threadIdx.x >> 6)) * 3 + (threadIdx.x & 3)) * 64;
    off = off < CODE_BYTES ? off : 0;
    asm volatile("s_mov_b32 m0, %2\n\ts_mov_b64 exec, 7\n\tglobal_load_lds_dword %0, %1\n\ts_mov_b64 exec, -1" ::"v"(off), "s"(pc0), "i"(L_RS) : "memory");
  }
  Ctx c;
  c.dbg = 0;
  STAMP(c, 0);
  c.lane = threadIdx.x & 63;
  const u32 w = __builtin_amdgcn_readfirstlane(threadIdx.x >> 6);
  c.m = w & 3;
  c.ws = __builtin_amdgcn_make_buffer_rsrc(ws, 0, WS_BYTES, 0x00020000);
  c.wsp = ws;
  c.par = (b >> 4) & 1;
  const int rt = (xcd >> 2) * 8 + (r >> 2), ct = (xcd & 3) * 4 + (r & 3), j = ct >> 1;
  Tile t;
  t.rt = rt;
  t.j = j;
  t.ct = ct;
  t.ffn = ct & 1;
  t.ldw = t.ffn ? 3584 : 3072;
  const u32 lane = c.lane, m = c.m;
  t.srd_x = make_srd(x, 2048 * 512 * 2);
  t.srd_w = make_srd(t.ffn ? w1 : wqkv, 512 * t.ldw);
  t.vx0 = (lane >> 3) * 1024 + (((lane & 7) ^ (lane >> 4)) << 4);
  {
    const u32 sx = (rt * 128 + m * 32 + (w >> 2) * 16) * 1024, dx = m * 4096 + (w >> 2) * 2048;
    dma<LX, 0, NTX>(t.srd_x, t.vx0, sx, dx);
    dma<LX + 1024, 8192, NTX>(t.srd_x, t.vx0 ^ 64, sx, dx);
    t.vw = (lane >> 3) * t.ldw + (((lane & 7) ^ (((lane >> 4) & 1) << 2)) << 4);
    t.c0 = t.ffn ? 448 * j + (j & 1) * 64 : 1024 + 128 * j;
    t.c1 = t.ffn ? t.c0 + 128 : 128 * j;
    t.c2 = t.ffn ? t.c0 + 256 : 2048 + 128 * j;
    t.cc = t.ffn ? 448 * j + ((j & 1) ? 0 : 384) : 0;
    dma<LWA, 0, NTW>(t.srd_w, t.vw, w * 8 * t.ldw + t.c0, w * 1024);
    dma<LWA + 8192, 0, NTW>(t.srd_w, t.vw, w * 8 * t.ldw + t.c1, w * 1024);
    dma<LWB, 0, NTW>(t.srd_w, t.vw, w * 8 * t.ldw + t.c2, w * 1024);
    if (t.ffn && w >= 4) dma<LWB + 8192, 0, NTW>(t.srd_w, (lane >> 2) * 3584 + ((lane & 3) << 4), m * 16 * 3584 + t.cc, m * 1024);
    BAR();
    SB();
  }
  if (__builtin_expect(w >= 4, 1)) {
    helper(c, t, x, wo, w2, out);
  } else {
    {
      const u32 vs = m < 3 ? t.vw : (lane >> 3) * t.ldw + ((lane & 3) << 4);
      const u32 so = rt * 32 * t.ldw + (m == 0 ? t.c0 : m == 1 ? t.c1 : m == 2 ? t.c2 : t.cc);
      dma<LBS, 0, NTS>(t.srd_w, vs, so, m * 4096);
      dma<LBS + 1024, 0, NTS>(t.srd_w, vs, so + 8 * t.ldw, m * 4096);
      dma<LBS + 2048, 0, NTS>(t.srd_w, vs, so + 16 * t.ldw, m * 4096);
      dma<LBS + 3072, 0, NTS>(t.srd_w, vs, so + 24 * t.ldw, m * 4096);
      dma<L_ST, 0>(t.srd_x, lane * 16, (rt * 128 + ct * 8 + m * 2) * 1024, m * 2048);
      dma<L_ST + 1024, 1024>(t.srd_x, lane * 16, (rt * 128 + ct * 8 + m * 2) * 1024, m * 2048);
      dma<L_WPHI, 0>(make_srd(wphi, 32 * 512), lane * 16, m < 2 ? (u32)(j * 2048 + m * 1024) : (u32)(((b & 7) * 2 + ((b >> 4) & 1)) * 1024), (m < 2 ? m : 2) * 1024);
      const u32 t1 = t.ffn ? blk_of(rt, (ct & 12) + (lane & 3)) : (lane & 7) * 32 + m * 8 + j, f1 = t.ffn ? m * 2 + (j & 1) : 16 + rt;
      load_tag(c, lane < 8 ? t1 : lane < 12 ? (rt >> 1) * 32 + (lane & 3) * 8 + j : lane < 14 ? blk_of(rt, (m >> 1) * 8 + (lane & 1) * 4 + (ct & 3)) : b,
               lane < 8 ? f1 : lane < 12 ? 96 + (rt & 1) * 4 + m : 104 + (m & 1) * 4 + (ct >> 2));
      *(LAS u32*)(L_SEM + (lane & 31) * 4) = 0;
    }
    if (t.ffn) compute1<true>(c, t, w2);
    else compute1<false>(c, t, w2);
  }
}

extern "C" void kernel_launch(void* const* d_in, const int* in_sizes, int n_in, void* d_out, int out_size, void* d_ws, size_t ws_size,
                              hipStream_t stream) {
  const u16 *x = (const u16*)d_in[0], *wqkv = (const u16*)d_in[1], *wphi = (const u16*)d_in[2], *wo = (const u16*)d_in[3];
  const u16 *w1 = (const u16*)d_in[5], *w2 = (const u16*)d_in[6];
  fused_k<<<256, 512, 0, stream>>>(x, wqkv, wphi, wo, w1, w2, (u16*)d_out, (char*)d_ws);
}
```

```cpp
#include <hip/hip_runtime.h>
#pragma clang diagnostic ignored "-Wint-to-pointer-cast"
typedef unsigned short u16;
typedef unsigned u32;
typedef unsigned long long u64;
typedef u32 u32x2 __attribute__((ext_vector_type(2)));
typedef u32 u32x4 __attribute__((ext_vector_type(4)));
typedef short s16x2 __attribute__((ext_vector_type(2)));
typedef short s16x4 __attribute__((ext_vector_type(4)));
typedef float f32x2 __attribute__((ext_vector_type(2)));
typedef float f32x4 __attribute__((ext_vector_type(4)));
typedef float f32x16 __attribute__((ext_vector_type(16)));
typedef __bf16 b16x2 __attribute__((ext_vector_type(2)));
typedef __bf16 b16x8 __attribute__((ext_vector_type(8)));
#define LAS __attribute__((address_space(3)))
#define GAS __attribute__((address_space(1)))
#define DI __device__ __forceinline__
#define SB() __builtin_amdgcn_sched_barrier(0)
#define BAR() __builtin_amdgcn_s_barrier()
#define WAITV(n) do { asm volatile("s_waitcnt vmcnt(%0)" ::"i"(n) : "memory"); SB(); } while (0)
#define WAITL0() asm volatile("s_waitcnt lgkmcnt(0)" ::: "memory")
#define OPAQUE(x) asm volatile("" : "+v"(x))

#define NTX 0
#define NTW 0
#define NTS 0
#define NTSX ""
#define SLP 2
#define P7 2
constexpr u32 HF_OFF = 0;
constexpr u32 QF_OFF = 64u * 112u * 1024u;
constexpr u32 OF_OFF = QF_OFF + 64u * 32u * 1024u;
constexpr u32 KV_OFF = OF_OFF + 64u * 32u * 1024u;
constexpr u32 KZ_OFF = KV_OFF + 32u * 16u * 2048u;
constexpr u32 CS_OFF = KZ_OFF + 32u * 16u * 128u;
constexpr u32 RS_OFF = CS_OFF + 256u * 1024u;
constexpr u32 MB_OFF = RS_OFF + 2048u * 8u;
constexpr u32 EP_OFF = MB_OFF + 256u * 1024u;
constexpr u32 XP_OFF = EP_OFF + 2048u;
constexpr u32 WS_BYTES = XP_OFF + 256u * 32768u;
constexpr u32 LX = 0, LWA = 49152, LWB = 98304, LBS = 135168, LSCR = 159744;
constexpr u32 lx(int s) { return LX + s * 16384; }
constexpr u32 lwa(int s) { return LWA + s * 16384; }
constexpr u32 lwb(int s) { return LWB + s * 12288; }
constexpr u32 L_WPHI = LSCR, L_WPA = LSCR + 2048, L_RS = LSCR + 3072, L_SEM = LSCR + 3584, L_ZERO = LSCR + 3648;
constexpr u32 L_SP0 = lwb(0) + 8192, L_SP1 = lwb(1) + 8192, L_SP2 = lwb(2) + 8192;
constexpr u32 L_ST = LBS + 16384;
constexpr int AUX = 16;

DI f32x16 mfma(u32x4 a, u32x4 b, f32x16 c) {
  return __builtin_amdgcn_mfma_f32_32x32x16_bf16(__builtin_bit_cast(b16x8, a), __builtin_bit_cast(b16x8, b), c, 0, 0, 0);
}
DI void mma(f32x16& d, u32x4 a, u32x4 b) { asm volatile("v_mfma_f32_32x32x16_bf16 %0, %1, %2, %0" : "+v"(d) : "v"(a), "v"(b)); }
DI void mma0(f32x16& d, u32x4 a, u32x4 b) { asm volatile("v_mfma_f32_32x32x16_bf16 %0, %1, %2, 0" : "=&v"(d) : "v"(a), "v"(b)); }
DI void mma8(f32x16& d, u32x2 a, u32x2 b) { asm volatile("v_mfma_f32_32x32x8_bf16 %0, %1, %2, %0" : "+v"(d) : "v"(a), "v"(b)); }
DI u32x2 trd(u32 addr) { return __builtin_bit_cast(u32x2, __builtin_amdgcn_ds_read_tr16_b64_v4i16((LAS s16x4*)addr)); }
DI u32x4 trd2(u32 a0, u32 a1) {
  u32x2 lo = trd(a0), hi = trd(a1);
  return __builtin_shufflevector(lo, hi, 0, 1, 2, 3);
}
DI u32x4 lds128(u32 addr) { return *(LAS u32x4*)addr; }
DI f32x4 ldsf4(u32 addr) { return *(LAS f32x4*)addr; }
DI u32 pk(float a, float b) {
  f32x2 v = {a, b};
  return __builtin_bit_cast(u32, __builtin_convertvector(v, b16x2));
}
DI u32 pk2(f32x2 v) { return __builtin_bit_cast(u32, __builtin_convertvector(v, b16x2)); }
DI u32 relu2(u32 v) { return __builtin_bit_cast(u32, __builtin_elementwise_max(__builtin_bit_cast(s16x2, v), s16x2{0, 0})); }
extern "C" __device__ float fmul_legacy_(float, float) __asm("llvm.amdgcn.fmul.legacy");
DI float mul1(float a, float b) { return fmul_legacy_(a, b); }
DI float mx(float a, float b) { return __builtin_elementwise_maximum(a, b); }
DI float dot2(u32 a, u32 b, float c) {
  return __builtin_amdgcn_fdot2_f32_bf16(__builtin_bit_cast(b16x2, a), __builtin_bit_cast(b16x2, b), c, false);
}
DI void swap32(float& a, float& b) {
  auto r = __builtin_amdgcn_permlane32_swap(__float_as_uint(a), __float_as_uint(b), false, false);
  a = __uint_as_float(r[0]);
  b = __uint_as_float(r[1]);
}
template <int I> DI f32x2 pr(const f32x16& a) { return __builtin_shufflevector(a, a, I, I + 1); }
template <int I> DI f32x4 q4(const f32x16& a) { return __builtin_shufflevector(a, a, I, I + 1, I + 2, I + 3); }
DI u32x4 make_srd(const void* p, u32 bytes) {
  u64 a = (u64)p;
  u32x4 r = {(u32)a, (u32)(a >> 32) & 0xffffu, bytes, 0x00020000u};
  return r;
}
template <u32 LI, u32 SI, bool NT = false>
DI void dma(u32x4 srd, u32 voff, u32 sbase, u32 lbase) {
  u32 t;
  if constexpr (NT) {
    asm volatile("s_add_u32 m0, %4, %6\n\ts_add_u32 %0, %3, %5\n\tbuffer_load_dwordx4 %1, %2, %0 offen nt lds"
                 : "=&s"(t)
                 : "v"(voff), "s"(srd), "s"(sbase), "s"(lbase), "i"(SI), "i"(LI)
                 : "memory", "scc");
  } else
  asm volatile("s_add_u32 m0, %4, %6\n\ts_add_u32 %0, %3, %5\n\tbuffer_load_dwordx4 %1, %2, %0 offen lds"
               : "=&s"(t)
               : "v"(voff), "s"(srd), "s"(sbase), "s"(lbase), "i"(SI), "i"(LI)
               : "memory", "scc");
}
DI void st128(u32x4 v, __amdgpu_buffer_rsrc_t rs, u32 voff, u32 soff) {
  __builtin_amdgcn_raw_buffer_store_b128(v, rs, voff, soff, AUX);
  asm volatile("s_nop 1" ::"v"(v) : "memory");
}
#define LDAUX AUX
DI u32x4 ld128(__amdgpu_buffer_rsrc_t rs, u32 voff, u32 soff) { return __builtin_amdgcn_raw_buffer_load_b128(rs, voff, soff, LDAUX); }
template <int OFF> DI void gld64(u64& d, u32 voff, const char* base) {
  asm volatile("global_load_dwordx2 %0, %1, %2 offset:%3 sc1" : "=v"(d) : "v"(voff), "s"(base), "i"(OFF) : "memory");
}
template <int OFF> DI void gld128(f32x4& d, u32 voff, const char* base) {
  asm volatile("global_load_dwordx4 %0, %1, %2 offset:%3 sc1" : "=v"(d) : "v"(voff), "s"(base), "i"(OFF) : "memory");
}
template <int OFF> DI void warm1(u32& d, const void* base) { asm volatile("s_load_dword %0, %1, %2" : "+s"(d) : "s"(base), "i"(OFF)); }
template <int N> struct IC { static constexpr int v = N; };
template <int B, int E, class F> DI void sfor(F&& f) {
  if constexpr (B < E) {
    f(IC<B>{});
    sfor<B + 1, E>(f);
  }
}
constexpr u32 ONES2 = 0x3f803f80u;
constexpr float LOG2E = 1.4426950408889634f;

DI void dual_softmax(const f32x16& P, float sl, u32x4& o0, u32x4& o1) {
  float m0 = mx(mx(mx(mx(mx(mx(mx(P[0], P[1]), P[2]), P[3]), P[4]), P[5]), P[6]), P[7]);
  float m1 = mx(mx(mx(mx(mx(mx(mx(P[8], P[9]), P[10]), P[11]), P[12]), P[13]), P[14]), P[15]);
  swap32(m0, m1);
  m0 = mx(m0, m1) * -sl;
  m1 = m0;
  swap32(m0, m1);
  f32x2 e[8];
  sfor<0, 8>([&](auto II) {
    constexpr int I = decltype(II)::v;
    const float m = I < 4 ? m0 : m1;
    e[I] = __builtin_elementwise_fma(pr<2 * I>(P), f32x2{sl, sl}, f32x2{m, m});
  });
#pragma unroll
  for (int i = 0; i < 8; i++) e[i] = f32x2{__builtin_amdgcn_exp2f(e[i][0]), __builtin_amdgcn_exp2f(e[i][1])};
  const f32x2 s0 = (e[0] + e[1]) + (e[2] + e[3]), s1 = (e[4] + e[5]) + (e[6] + e[7]);
  m0 = s0[0] + s0[1];
  OPAQUE(m0);
  m1 = s1[0] + s1[1];
  swap32(m0, m1);
  m0 = __builtin_amdgcn_rcpf(m0 + m1);
  m1 = m0;
  swap32(m0, m1);
#pragma unroll
  for (int i = 0; i < 4; i++) {
    o0[i] = pk2(e[i] * m0);
    o1[i] = pk2(e[4 + i] * m1);
  }
}
DI u32x4 wphi_frag(u32 a, u32 sg) {
  u32x4 f = trd2(a, a + 256);
  f[0] ^= sg; f[1] ^= sg; f[2] ^= sg; f[3] ^= sg;
  return f;
}
DI void dual_softmax1(const f32x16& P, float sl, u32x4& o0, u32x4& o1) {
  float m0 = mx(mx(mx(mx(mx(mx(mx(P[0], P[1]), P[2]), P[3]), P[4]), P[5]), P[6]), P[7]);
  float m1 = mx(mx(mx(mx(mx(mx(mx(P[8], P[9]), P[10]), P[11]), P[12]), P[13]), P[14]), P[15]);
  swap32(m0, m1);
  m0 = mx(m0, m1) * -sl;
  m1 = m0;
  swap32(m0, m1);
  float e[16];
#pragma unroll
  for (int i = 0; i < 16; i++) {
    e[i] = __builtin_fmaf(P[i], sl, i < 8 ? m0 : m1);
    OPAQUE(e[i]);
  }
#pragma unroll
  for (int i = 0; i < 16; i++) e[i] = __builtin_amdgcn_exp2f(e[i]);
  m0 = e[0];
  m1 = e[8];
#pragma unroll
  for (int i = 1; i < 8; i++) {
    m0 += e[i];
    OPAQUE(m0);
    m1 += e[8 + i];
    OPAQUE(m1);
  }
  swap32(m0, m1);
  m0 = __builtin_amdgcn_rcpf(m0 + m1);
  m1 = m0;
  swap32(m0, m1);
#pragma unroll
  for (int i = 0; i < 4; i++) {
    o0[i] = pk(mul1(e[2 * i], m0), mul1(e[2 * i + 1], m0));
    o1[i] = pk(mul1(e[8 + 2 * i], m1), mul1(e[9 + 2 * i], m1));
  }
}
DI void phi_put(u32 apw, u32x4 o0, u32x4 o1) {
  *(LAS u32x2*)(apw) = u32x2{o0[0], o0[1]};
  *(LAS u32x2*)(apw ^ 16) = u32x2{o0[2], o0[3]};
  *(LAS u32x2*)(apw ^ 32) = u32x2{o1[0], o1[1]};
  *(LAS u32x2*)(apw ^ 48) = u32x2{o1[2], o1[3]};
}
DI void phi_store(u32 apw, const f32x16& P, float sl) {
  u32x4 o0, o1;
  dual_softmax(P, sl, o0, o1);
  phi_put(apw, o0, o1);
}
template <int S> DI u32x4 pack8(const f32x16& a) { return u32x4{pk2(pr<8 * S>(a)), pk2(pr<8 * S + 2>(a)), pk2(pr<8 * S + 4>(a)), pk2(pr<8 * S + 6>(a))}; }

constexpr u32 slot_loc(int s, int k) { return k < 2 ? lx(s) + k * 8192 : k < 4 ? lwa(s) + (k - 2) * 8192 : lwb(s); }
constexpr u32 bloc(int n) {
  return n < 5 ? slot_loc(2, n) : n < 10 ? slot_loc(0, n - 5) : n < 15 ? slot_loc(1, n - 10) : n < 40 ? LBS + (n & 1) * 8192 : LBS + (n - 40) * 8192;
}

#define STAMP(c, i) do {} while (0)
#define HSTAMP(c, i) do {} while (0)
struct Ctx {
  mutable u32 dbg;
  __amdgpu_buffer_rsrc_t ws;
  char* wsp;
  u32 m, lane;
  u32 ab[3][2];
  u32 par;
  u64 tag;
  u64 own;
  u32 fl;
};
template <int N> DI u32x4 rdb(const Ctx& c, int cb) {
  constexpr u32 W = bloc(N) / 57344;
  const u32 a = c.ab[W][cb] + (bloc(N) - W * 57344);
  return trd2(a, a + 1024);
}
DI void init_ab(Ctx& c) {
  const u32 lane = c.lane, m = c.m, hh = lane >> 5, blk = (lane >> 4) & 1, q = (lane & 15) >> 2, p = lane & 3;
#pragma unroll
  for (int cb = 0; cb < 2; cb++) {
    const u32 t = m * 2048 + (4 * hh + q) * 128 + (((4 * (cb ^ (m & 1)) + 2 * blk + (p >> 1)) ^ ((q >> 1) << 2)) << 4) + 8 * (p & 1);
#pragma unroll
    for (int k = 0; k < 3; k++) {
      c.ab[k][cb] = t + k * 57344;
      OPAQUE(c.ab[k][cb]);
    }
  }
}
DI void load_tag(Ctx& c, u32 tgt, u32 slot) {
  c.fl = MB_OFF + tgt * 1024 + slot * 8;
  asm volatile("global_load_dwordx2 %0, %1, %2 sc1" : "=v"(c.tag) : "v"(EP_OFF + tgt * 8), "s"(c.wsp) : "memory");
}
DI void set_tag(Ctx& c) {
  OPAQUE(c.tag);
  c.tag += 1;
  c.own = ((u64)(u32)__builtin_amdgcn_readlane((u32)(c.tag >> 32), 63) << 32) | (u32)__builtin_amdgcn_readlane((u32)c.tag, 63);
}
DI void publish(const Ctx& c, u64 mask) {
  asm volatile("s_mov_b64 exec, %3\n\tglobal_atomic_swap_x2 %0, %1, %2 sc1\n\ts_mov_b64 exec, -1" ::"v"(c.fl), "v"(c.tag), "s"(c.wsp), "s"(mask) : "memory");
}
DI u64 peek(const Ctx& c, u32 off, int n) {
  u64 v = c.own;
  const u64 z = 0;
  asm volatile("s_mov_b64 exec, %4\n\tglobal_atomic_or_x2 %0, %1, %2, %3 sc0 sc1\n\ts_mov_b64 exec, -1"
               : "+v"(v)
               : "v"(off + c.lane * 8), "v"(z), "s"(c.wsp), "s"((1ull << n) - 1)
               : "memory");
  return v;
}
DI void await(const Ctx& c, u32 off, int n, u64 v) {
  OPAQUE(v);
#pragma unroll 1
  for (int it = 0; it < (1 << 16) && !__all(v == c.own); it++) {
    if (SLP) __builtin_amdgcn_s_sleep(SLP);
    v = peek(c, off, n);
    asm volatile("s_waitcnt vmcnt(0)" : "+v"(v)::"memory");
  }
}
DI void sem_post(u32 idx) {
  asm volatile("s_mov_b64 exec, 1\n\tds_add_u32 %0, %1\n\ts_mov_b64 exec, -1" ::"v"(L_SEM + idx * 4), "v"(1u) : "memory");
}
DI void sem_wait(u32 idx, u32 n) {
  u32 v, sv, left = 1u << 20;
  asm volatile(
      "1:\n\tds_read_b32 %0, %3\n\ts_sub_u32 %2, %2, 1\n\ts_waitcnt lgkmcnt(0)\n\ts_cbranch_scc1 2f\n\tv_readfirstlane_b32 %1, %0\n\ts_nop 0\n\t"
      "s_cmp_lt_u32 %1, %4\n\ts_cbranch_scc1 1b\n2:"
      : "=&v"(v), "=&s"(sv), "+s"(left)
      : "v"(L_SEM + idx * 4), "s"(n)
      : "memory", "scc");
}
DI u32 sem_peek(u32 idx) { return *(volatile LAS u32*)(L_SEM + idx * 4); }
DI void sem_check(u32 v, u32 idx, u32 n) {
  if (__builtin_amdgcn_readfirstlane(v) < n) sem_wait(idx, n);
  asm volatile("" ::: "memory");
}
constexpr u32 SEM_B = 0, SEM_B2 = 1, SEM_B0 = 2, SEM_7 = 4, SEM_P = 5, SEM_S = 6, SEM_X = 7, SEM_K = 8, SEM_A = 12, SEM_KV = 13, SEM_C = 14, SEM_H = 15;
template <int N> DI u32 aset(const Ctx& c) {
  constexpr int e = N % 7, o = 7 + (e + 1) % 7;
  return c.par ? (N < 7 ? o : e) : (N < 7 ? e : o);
}
struct BSrc {
  u32x4 srd;
  u32 vb, sb, d2;
  DI void init(const Ctx& c, const u16* w, u32 rows, u32 sb_) {
    srd = make_srd(w, rows * 1024);
    vb = (c.lane >> 3) * 1024 + (((c.lane & 7) ^ (((c.lane >> 4) & 1) << 2)) << 4);
    sb = __builtin_amdgcn_readfirstlane(sb_);
    d2 = __builtin_amdgcn_readfirstlane((threadIdx.x & 192) * 32);
  }
  DI void init2(const Ctx& c, const u16* w2) {
    init(c, w2, 1792, ((blockIdx.x & 3) * 448 + (c.m >> 1) * 16) * 1024 + ((blockIdx.x >> 3) & 3) * 256 + (c.m & 1) * 128);
  }
  template <int N, int H> DI void issue1(const Ctx& c) const {
    dma<bloc(N) + H * 1024, H * 8192>(srd, vb, sb + (N < 28 ? aset<N>(c) * 32768 : (N - 28) * 16384), d2);
  }
  template <int N> DI void issue(const Ctx& c) const {
    issue1<N, 0>(c);
    issue1<N, 1>(c);
  }
};

struct Tile {
  int rt, j, ct;
  bool ffn;
  u32 ldw;
  u32 c0, c1, c2, cc;
  u32x4 srd_x, srd_w;
  u32 vx0, vw;
};
DI u32 blk_of(u32 rt, u32 ct) { return (((rt & 7) * 4 + (ct & 3)) << 3) + (rt >> 3) * 4 + (ct >> 2); }

template <int N> DI void put_block(const Ctx& c, const f32x16& a) {
  const u32 p = c.m * 2048 + c.lane * 16 + bloc(N);
  *(LAS u32x4*)p = pack8<0>(a);
  *(LAS u32x4*)(p + 1024) = pack8<1>(a);
}
template <int N> DI void get_block(const Ctx& c, u32 src, u32x4 (&f)[2]) {
  const u32 p = src * 2048 + c.lane * 16 + bloc(N);
  f[0] = lds128(p);
  f[1] = lds128(p + 1024);
}

#define PFCODE 40960
#define TOUCH 0
#define WARM 2
#define WN 1
#define WD 1
#define TLBW 0
#define OAUX 0
#define WARMO 0
#define PUBU 16
#define PKU 34
#define CKU 46
#define PAUSE 0
#define QK 14
#define QW 8
#define SMXA dual_softmax1
#define SG 0
#define WOFF 10
#define CW 8
#define NB7 4
constexpr int NCP = 8;
constexpr u32 CODE_BYTES = PFCODE;
DI void helper(Ctx& c, const Tile& t, const u16* x, const u16* wo, const u16* w2, u16* out) {
  constexpr int ND = 10;
  const u32 lane = c.lane, m = c.m;
  const int b = blockIdx.x, i = 2 * t.rt + ((b >> 1) & 1), jc = ((b >> 3) & 3) * 2 + (b & 1);
  const int hd = (b & 7) * 4 + ((b >> 3) & 3), ga = b >> 5;
  const u32 vx1 = t.vx0 ^ 64, vw1 = t.vw + 8 * t.ldw;
  const u32 vc = (lane >> 2) * 3584 + ((lane & 3) << 4);
  const u32 sx = (t.rt * 128 + m * 32) * 1024, sc = m * 16 * 3584 + t.cc;
  const u32 d4 = m * 4096, d2 = m * 2048, d1 = m * 1024;
  u32 wd = 0;
  const char *pb = (const char*)w2 + (b * 4 + m) * 1792, *pc = (const char*)wo + (b * 4 + m) * 512;
  const char* pm = c.wsp + MB_OFF + b * 1024 + m * 256;
  const char* pq_ = c.wsp + QF_OFF + ((t.rt * 4 + m) * 32 + 4 * t.j + (t.ffn ? 2 : 0)) * 1024;
  const char* pd = c.wsp + OF_OFF + ((8 * ga + 2 * m) * 32 + hd) * 1024;
  const char* pf = c.wsp + KV_OFF + ((4 * t.j + m) * 16 + t.rt) * 2048 + (t.ffn ? 1024 : 0);
  const char* pz = c.wsp + KZ_OFF + ((4 * t.j + m) * 16 + t.rt) * 128 + (t.ffn ? 64 : 0);
  auto warm = [&](auto KK) {
    constexpr int k = decltype(KK)::v - WOFF;
    if constexpr (k < 0) {
    } else if constexpr (k < 4) warm1<k * 64>(wd, pm);
    else if constexpr (k < 32) warm1<(k - 4) * 64>(wd, pb);
    else if constexpr (k < 40) warm1<(k - 32) * 64>(wd, pc);
    else if constexpr (WARM >= 2 && k == 40) warm1<0>(wd, pz);
    else if constexpr (WARM >= 2 && k < 49) warm1<(k - 41) * 128>(wd, pf);
    else if constexpr (WARM >= 3 && k < 65) warm1<((k - 49) >> 3) * 32768 + ((k - 49) & 7) * 128>(wd, pd);
    else if constexpr (WARM >= 4 && k < 81) warm1<(k - 65) * 128>(wd, pq_);
  };
  auto ringf = [&](auto FF) {
    constexpr int G = decltype(FF)::v / ND, S = G % 3, I = decltype(FF)::v % ND;
    const u32 sg = (m * 16 + G * 64) * t.ldw;
    if constexpr (I == 4) {
      if (t.ffn) dma<lwb(S) + 8192, G * 64 * 3584, NTW>(t.srd_w, vc, sc, d1);
    }
    if constexpr (I < 4) dma<lx(S) + I * 1024, I * 8192 + G * 128, NTX>(t.srd_x, (I & 1) ? vx1 : t.vx0, sx, d4);
    else if constexpr (I < 6) dma<lwa(S) + (I & 1) * 1024, 0, NTW>(t.srd_w, (I & 1) ? vw1 : t.vw, sg + t.c0, d2);
    else if constexpr (I < 8) dma<lwa(S) + 8192 + (I & 1) * 1024, 0, NTW>(t.srd_w, (I & 1) ? vw1 : t.vw, sg + t.c1, d2);
    else dma<lwb(S) + (I & 1) * 1024, 0, NTW>(t.srd_w, (I & 1) ? vw1 : t.vw, sg + t.c2, d2);
  };
  constexpr auto rend = [](int G) { return G < 0 ? ND + 3 : G == 0 ? 2 * ND + 5 : G == 1 ? 3 * ND + 8 : G < 6 ? (G + 3) * ND : 8 * ND; };
  sfor<ND, rend(-1)>(ringf);
  load_tag(c, lane < 16 ? blk_of(t.rt, lane) : lane < 32 ? blk_of(lane & 15, t.ct) : lane < 40 ? blk_of(2 * ga + (m >> 1), (m & 1) * 8 + (lane & 1) * 4 + ((lane >> 1) & 3)) : b,
           lane < 16 ? 64 + t.ct : lane < 32 ? 80 + t.rt : 32 + hd);
  BSrc b2, bo;
  b2.init2(c, w2);
  bo.init(c, wo, 512, m * 128 * 1024 + jc * 128);
  const u32 hh = lane >> 5, blk = (lane >> 4) & 1, q = (lane & 15) >> 2, p = lane & 3, r = lane & 31;
  init_ab(c);
  u32x4 bf[8][2];
  auto ldb = [&](auto NN) {
    constexpr int N = decltype(NN)::v;
    bf[N - 28][0] = rdb<N>(c, 0);
    bf[N - 28][1] = rdb<N>(c, 1);
  };
  u64 ps = 0, grs = 0;
  f32x4 gc[4];
  __builtin_amdgcn_s_setprio(1);
  HSTAMP(c, 1);
  WAITV(4);
  BAR();
  HSTAMP(c, 2);
  constexpr auto wo_in = [](int G) { return G == 3 ? 28 : G == 4 ? 29 : G == 6 ? 30 : G == 7 ? 31 : -1; };
  float sv[4], scs;
  u32 a_cf = L_ST + (m * 64 + (lane & 15) * 4 + (lane >> 4)) * 8, a_fm = L_ST + 4096 + (m * 32 + r) * 8 + hh * 4, a_lr = L_RS + m * 128 + r * 4;
  u32 a_rs = RS_OFF + (t.rt * 128 + m * 32 + r) * 8, a_cs = CS_OFF + t.ct * 16384 + (lane >> 4) * 4096 + m * 256 + (lane & 15) * 16;
  OPAQUE(a_cf); OPAQUE(a_fm); OPAQUE(a_lr); OPAQUE(a_rs); OPAQUE(a_cs);
  sfor<0, 8>([&](auto GG) {
    constexpr int G = decltype(GG)::v;
    constexpr int NRB = G == 6 ? 10 : G == 7 ? 10 : 0;
    if constexpr (G == 5 || G == 6) ldb(IC<23 + G>{});
    if constexpr (G == SG + 1) {
      const u32 cv = *(LAS u32*)(L_ST + m * 2048 + lane * 4), rv = *(LAS u32*)(L_ST + ((lane >> 2) & 3) * 2048 + 1024 + (lane & 3) * 4);
      __builtin_amdgcn_raw_buffer_store_b32(cv, c.ws, lane * 4, CS_OFF + (t.ct * 16 + t.rt) * 1024 + m * 256, AUX);
      asm volatile("s_mov_b64 exec, %3\n\tglobal_store_dword %0, %1, %2 sc1\n\ts_mov_b64 exec, -1" ::"v"(RS_OFF + (t.rt * 128 + t.ct * 8) * 8 + (lane & 15) * 4),
                   "v"(rv), "s"(c.wsp), "s"(m == 0 ? 0xffffull : 0ull)
                   : "memory");
      set_tag(c);
    }
    if constexpr (G == SG + 2) publish(c, 0xffull << (8 * m));
    auto gather = [&]() {
      await(c, MB_OFF + b * 1024 + 512, 32, ps);
      gld64<0>(grs, a_rs, c.wsp);
      gld128<0>(gc[0], a_cs, c.wsp); gld128<1024>(gc[1], a_cs, c.wsp); gld128<2048>(gc[2], a_cs, c.wsp); gld128<3072>(gc[3], a_cs, c.wsp);
    };
    if constexpr (SG == 0 && G == 4) ps = peek(c, MB_OFF + b * 1024 + 512, 32);
    if constexpr (SG == 0 && G == 5) gather();
    sfor<rend(G - 1), rend(G)>([&](auto FF) {
      ringf(FF);
      constexpr int u = decltype(FF)::v - rend(-1);
      sfor<u * WN / WD, (u + 1) * WN / WD>(warm);
    });
    if constexpr (SG == 1 && G == 5) {
      WAITV(ND);
      gather();
    }
    if constexpr (G >= 6)
      sfor<(G - 6) * 5, (G - 6) * 5 + NRB / 2>([&](auto NN) {
        constexpr int N = decltype(NN)::v - SG;
        b2.issue<N + SG>(c);
        SB();
        if constexpr (N == 0) {
          if constexpr (SG == 1) WAITV(4);
#pragma unroll
          for (int k = 0; k < 4; k++) OPAQUE(gc[k]);
#pragma unroll
          for (int k = 0; k < 4; k++) {
            sv[k] = gc[0][k];
#pragma unroll
            for (int u = 1; u < 4; u++) {
              sv[k] += gc[u][k];
              OPAQUE(sv[k]);
            }
          }
        } else if constexpr (N == 1) {
          swap32(sv[0], sv[2]);
          swap32(sv[1], sv[3]);
          sv[0] += sv[2];
          OPAQUE(sv[0]);
          sv[1] += sv[3];
          auto sw16 = __builtin_amdgcn_permlane16_swap(__float_as_uint(sv[0]), __float_as_uint(sv[1]), false, false);
          scs = __uint_as_float(sw16[0]) + __uint_as_float(sw16[1]);
          OPAQUE(scs);
        } else if constexpr (N == 2) {
          const u32 h2 = pk(scs, scs);
          const float lo = scs - __uint_as_float(h2 & 0xffff0000u);
          *(LAS u32x2*)a_cf = u32x2{h2, pk(lo, lo)};
        } else if constexpr (N == 3) {
          OPAQUE(grs);
          *(LAS u32*)a_fm = (u32)grs;
          *(LAS u32*)a_lr = (u32)(grs >> 32);
        }
      });
    if constexpr (G >= 6) WAITL0();
    if constexpr (wo_in(G) > 0) bo.issue<wo_in(G)>(c);
    if constexpr (SG == 1 && G == 4) ps = peek(c, MB_OFF + b * 1024 + 512, 32);
    HSTAMP(c, 3 + 2 * G);
    WAITV(G < 3 ? rend(G) - (G + 2) * ND : rend(G) - rend(G - 1) + NRB + (wo_in(G) > 0 ? 2 : 0) + (SG == 1 && G == 4 ? 1 : SG == 1 && G == 5 ? 5 : 0));
    if constexpr (G < 7) BAR();
    else sem_post(SEM_B0);
    HSTAMP(c, 4 + 2 * G);
  });
  const u32 mb = MB_OFF + b * 1024;
  const u32 fk = mb + 128, fo = mb + 256 + 64 * m, fq = mb + 768 + 16 * m;
  const u32 awp = (4 * hh + q) * 32 + p * 8, sg = blk ? 0x80008000u : 0u;
  asm volatile("" : "+s"(wd));
  u64 pq;
  if (!t.ffn) {
    const u32x4 w0 = wphi_frag(L_WPHI + 1024 + awp, sg), w1 = wphi_frag(L_WPHI + 1536 + awp, sg);
    const float sl = __uint_as_float((u32)(grs >> 32)) * LOG2E;
    sem_wait(SEM_K + m, 1);
    const u32 ak = ((m >> 1) ? L_SP2 : L_SP1) + (m & 1) * 2048 + lane * 16;
    const f32x16 P0 = mfma(w0, lds128(ak), f32x16{}), P1 = mfma(w1, lds128(ak + 1024), f32x16{});
    u32x4 o0, o1;
    dual_softmax1(P0, sl, o0, o1);
    OPAQUE(o0); OPAQUE(o1);
    sem_wait(SEM_7, 4);
    const u32 apw = lwa(1) + m * 2048 + r * 64 + ((hh ^ ((r >> 1) & 7)) << 3);
    phi_put(apw, o0, o1);
    phi_store(apw + 8192, P1, sl);
    sem_post(SEM_P);
  } else {
    sem_wait(SEM_K + m, 1);
  }
  __builtin_amdgcn_s_setprio(2);
  pq = peek(c, fq, 2);
  const u32x4 wp = wphi_frag(L_WPA + (hd & 1) * 512 + awp, sg);
  WAITV(0);
  sem_post(SEM_B);
  STAMP(c, 20);
  await(c, fq, 2, pq);
  STAMP(c, 24);
  u32x4 p00, p01, p10, p11;
  u64 pkv;
  float rs0, rs1;
  {
    u32x4 qf[2];
#pragma unroll
    for (int u = 0; u < 2; u++) qf[u] = ld128(c.ws, lane * 16, QF_OFF + ((8 * ga + 2 * m + u) * 32 + hd) * 1024);
    rs0 = __uint_as_float(__builtin_amdgcn_raw_buffer_load_b32(c.ws, lane * 8 + 4, RS_OFF + (8 * ga + 2 * m) * 256, AUX));
    ldb(IC<30>{});
    ldb(IC<31>{});
    rs1 = rs0;
    swap32(rs0, rs1);
    const f32x16 Q0 = mfma(wp, qf[0], f32x16{}), Q1 = mfma(wp, qf[1], f32x16{});
    SMXA(Q0, rs0 * LOG2E, p00, p01);
    OPAQUE(p00); OPAQUE(p01);
    SB();
    pkv = peek(c, fk, 16);
    SMXA(Q1, rs1 * LOG2E, p10, p11);
    OPAQUE(p10); OPAQUE(p11);
  }
  STAMP(c, 21);
  WAITV(0);
  OPAQUE(pkv);
#pragma unroll 1
  for (int it = 0; it < (1 << 16); it++) {
    if (__all(pkv == c.own)) {
      sem_post(SEM_S);
      break;
    }
    if (__builtin_amdgcn_readfirstlane(sem_peek(SEM_S))) break;
    pkv = peek(c, fk, 16);
    asm volatile("s_waitcnt vmcnt(0)" : "+v"(pkv)::"memory");
  }
  STAMP(c, 25);
  {
    f32x4 part[8];
#pragma unroll
    for (int s = 0; s < 8; s++) part[s] = __builtin_bit_cast(f32x4, ld128(c.ws, hh * 2048 + r * 16, KV_OFF + (hd * 16 + 2 * s) * 2048 + m * 512));
    f32x4 z = __builtin_bit_cast(f32x4, ld128(c.ws, (lane & 15) * 128 + blk * 16, KZ_OFF + hd * 2048 + m * 32));
    SB();
    float v[4] = {part[0][0], part[0][1], part[0][2], part[0][3]};
#pragma unroll
    for (int s = 1; s < 8; s++)
#pragma unroll
      for (int k = 0; k < 4; k++) {
        v[k] += part[s][k];
        OPAQUE(v[k]);
      }
    swap32(v[0], v[2]);
    swap32(v[1], v[3]);
    v[0] += v[2];
    OPAQUE(v[0]);
    *(LAS u32*)(L_SP0 + r * 32 + m * 8 + hh * 4) = pk(v[0], v[1] + v[3]);
    float za = hh ? z[2] : z[0], zb = hh ? z[3] : z[1];
    sfor<0, 4>([&](auto KK) {
      za += __int_as_float(__builtin_amdgcn_update_dpp(0, __float_as_int(za), 0x120 + (8 >> decltype(KK)::v), 0xf, 0xf, false));
      zb += __int_as_float(__builtin_amdgcn_update_dpp(0, __float_as_int(zb), 0x120 + (8 >> decltype(KK)::v), 0xf, 0xf, false));
    });
    *(LAS u32*)(L_SP0 + (32 + blk) * 32 + m * 8 + hh * 4) = pk(za, zb);
    sem_post(SEM_A);
    sem_wait(SEM_A, 4);
    const u32 e = blk == 0 ? hh * 16 + (lane & 15) : 32 + hh;
    const u32x4 k0 = lds128(L_SP0 + e * 32), k1 = lds128(L_SP0 + e * 32 + 16);
    f32x16 Z0 = mfma(k0, p00, f32x16{}), Z1 = mfma(k0, p10, f32x16{});
    Z0 = mfma(k1, p01, Z0);
    Z1 = mfma(k1, p11, Z1);
    constexpr int NO = 0;
    const float r0 = __builtin_amdgcn_rcpf(mul1(Z0[8 - NO] + 1e-6f, rs0)), r1 = __builtin_amdgcn_rcpf(mul1(Z1[8 - NO] + 1e-6f, rs1));
    u32x4 o0, o1;
#pragma unroll
    for (int d = 0; d < 4; d++) {
      o0[d] = pk(mul1(Z0[NO + 2 * d], r0), mul1(Z0[NO + 2 * d + 1], r0));
      o1[d] = pk(mul1(Z1[NO + 2 * d], r1), mul1(Z1[NO + 2 * d + 1], r1));
    }
    st128(o0, c.ws, lane * 16, OF_OFF + ((8 * ga + 2 * m) * 32 + hd) * 1024);
    st128(o1, c.ws, lane * 16, OF_OFF + ((8 * ga + 2 * m + 1) * 32 + hd) * 1024);
  }
  STAMP(c, 26);
  bo.issue<32>(c);
  bo.issue<33>(c);
  WAITV(4);
  publish(c, 0xff00000000ull);
  sem_post(SEM_X);
  STAMP(c, 27);
  WAITV(1);
  ldb(IC<32>{});
  ldb(IC<33>{});
  WAITL0();
  bo.issue<34>(c);
  bo.issue<35>(c);
  const u32 orow = 64 * i + 32 * (m >> 1) + r, oidx = orow * 512 + 64 * jc + 32 * (m & 1) + 8 * hh;
  u32x4 xr[2] = {*(const GAS u32x4*)(x + oidx), *(const GAS u32x4*)(x + oidx + 16)};
  float frs = __uint_as_float(__builtin_amdgcn_raw_buffer_load_b32(c.ws, orow * 8 + 4, RS_OFF, AUX));
  u64 po = peek(c, fo, 8);
  const u32 fx = mb + 832 + (m >> 1) * 32;
  u32x4 sel[2];
  {
    const u32 one = (((r >> 2) & 1) == hh) ? ((r & 1) ? 0x3f800000u : 0x3f80u) : 0u, dw = ((r >> 3) & 1) * 2 + ((r >> 1) & 1);
#pragma unroll
    for (int d = 0; d < 4; d++) {
      sel[0][d] = (dw == (u32)d && blk == 0) ? one : 0u;
      sel[1][d] = (dw == (u32)d && blk == 1) ? one : 0u;
    }
    OPAQUE(sel[0]); OPAQUE(sel[1]);
  }
  WAITV(0);
  OPAQUE(xr[0]); OPAQUE(xr[1]); OPAQUE(frs);
  ldb(IC<34>{});
  ldb(IC<35>{});
  await(c, fo, 8, po);
  STAMP(c, 30);
  u64 px = peek(c, fx, 4);
  u32x4 af[8][2];
#pragma unroll
  for (int n = 0; n < 8; n++)
#pragma unroll
    for (int u = 0; u < 2; u++) af[n][u] = ld128(c.ws, lane * 16, OF_OFF + ((2 * i + ((m >> 1) ^ u)) * 32 + 8 * m + n) * 1024);
  WAITV(16);
  OPAQUE(px);
  auto finish = [&](auto EE) {
  constexpr bool EARLY = decltype(EE)::v;
  u32x4 f[7][2];
  auto ldx = [&]() {
#pragma unroll
    for (int k = 0; k < 4; k++)
#pragma unroll
      for (int u = 0; u < 2; u++) f[3 + k][u] = ld128(c.ws, lane * 16, XP_OFF + b * 32768 + k * 8192 + m * 2048 + u * 1024);
    SB();
  };
  if constexpr (EARLY) ldx();
  f32x16 acc[4];
  constexpr int NSM = 4;
  sfor<0, NSM>([&](auto NN) {
    constexpr int N = decltype(NN)::v;
    if constexpr (N == 0) {
      mma0(acc[0], bf[N][0], af[N][0]); mma0(acc[1], bf[N][1], af[N][0]); mma0(acc[2], bf[N][0], af[N][1]); mma0(acc[3], bf[N][1], af[N][1]);
    } else {
      mma(acc[0], bf[N][0], af[N][0]); mma(acc[1], bf[N][1], af[N][0]); mma(acc[2], bf[N][0], af[N][1]); mma(acc[3], bf[N][1], af[N][1]);
    }
  });

  sfor<0, 4>([&](auto KK) {
    constexpr int k = 3 - decltype(KK)::v;
    sfor<NSM, 8>([&](auto NN) {
      constexpr int N = decltype(NN)::v;
      mma(acc[k], bf[N][k & 1], af[N][k >> 1]);
      if constexpr (N == NSM + 1 && k < 3) {
        SB();
        put_block<40 + k>(c, acc[k + 1]);
        if constexpr (k == 0) sem_post(SEM_H);
        SB();
      }
    });
  });
  STAMP(c, 31);
  {
    if constexpr (!EARLY) {
      await(c, fx, 4, px);
      ldx();
    }
    STAMP(c, 32);
    sem_wait(SEM_H, 4);
    get_block<40>(c, m ^ 1, f[0]);
    get_block<41>(c, m ^ 2, f[1]);
    get_block<42>(c, m ^ 3, f[2]);
    sem_wait(SEM_C, 4);
#pragma unroll
    for (int k = 0; k < 7; k++) { mma(acc[0], sel[0], f[k][0]); mma(acc[0], sel[1], f[k][1]); }
    STAMP(c, 33);
  }
  if (threadIdx.x == 319) __hip_atomic_store((GAS u64*)(c.wsp + EP_OFF + b * 8), c.tag, __ATOMIC_RELAXED, __HIP_MEMORY_SCOPE_AGENT);
  asm volatile("s_nop 15" : "+v"(acc[0]));
#pragma unroll
  for (int h = 0; h < 2; h++) {
    float e[8];
#pragma unroll
    for (int k = 0; k < 8; k++) e[k] = acc[0][8 * h + k];
#pragma unroll
    for (int k = 0; k < 4; k++) swap32(e[k], e[4 + k]);
    u32x4 o;
#pragma unroll
    for (int d = 0; d < 4; d++)
      o[d] = pk(__builtin_fmaf(e[2 * d], frs, __uint_as_float(xr[h][d] << 16)), __builtin_fmaf(e[2 * d + 1], frs, __uint_as_float(xr[h][d] & 0xffff0000u)));
    __builtin_amdgcn_raw_buffer_store_b128(o, __builtin_amdgcn_make_buffer_rsrc(out, 0, 2048 * 1024, 0x00020000), oidx * 2 + 32 * h, 0, OAUX);
  }
  };
  if (__all(px == c.own)) finish(IC<1>{});
  else finish(IC<0>{});
}

template <bool FFN> struct Slots {
  static constexpr int NBK = FFN ? 7 : 6, N7 = 28 * NBK, NS = N7 + 5 * NBK;
  static constexpr int blk(int n) {
    if (n < N7) return n % NBK;
    const int k = (n - N7) / 5;
    return (!FFN && k < 4) ? (k + 1) & 3 : k;
  }
  static constexpr int step(int n) { return n < N7 ? n / NBK : ((n - N7) % 5 == 0 ? -1 : 27 + (n - N7) % 5); }
  static constexpr int bar(int G) { return (G < 7 ? (4 * G + 3) * NBK : NS - NBK) + 1; }
};
template <bool FFN>
DI void compute1(Ctx& c, const Tile& t, const u16* w2) {
  u32x4 xa, xb;
  using SL = Slots<FFN>;
  constexpr int NBK = SL::NBK;
  const u32 lane = c.lane, m = c.m;
  const int rt = t.rt, j = t.j;
  const u32 hh = lane >> 5, blk = (lane >> 4) & 1, q = (lane & 15) >> 2, p = lane & 3, r = lane & 31;
  u32 ax[4], aw[2][2], ac;
#pragma unroll
  for (int s = 0; s < 4; s++) ax[s] = m * 4096 + r * 128 + (((2 * s + hh) ^ ((r >> 1) & 7)) << 4);
  const u32 at = (((2 * blk + (p >> 1)) ^ ((q >> 1) << 2)) << 4) + 8 * (p & 1);
  {
    const u32 a = (8 * hh + q) * 128 + at;
    aw[0][0] = a + LWA; aw[0][1] = (a ^ 64) + LWA; aw[1][0] = a + LWB; aw[1][1] = (a ^ 64) + LWB;
    ac = (8 * hh + q) * 64 + 32 * blk + 8 * p + LWB + 8192;
    OPAQUE(aw[0][0]); OPAQUE(aw[0][1]); OPAQUE(aw[1][0]); OPAQUE(aw[1][1]); OPAQUE(ac);
  }
  u32 acs = L_ST + r * 8;
  OPAQUE(acs);
  auto rdw = [&](auto NN) -> u32x4 {
    constexpr int T = SL::step(decltype(NN)::v), M = SL::blk(decltype(NN)::v);
    if constexpr (T < 0) {
      const u32x2 v = *(LAS u32x2*)(acs + M * 256);
      return __builtin_shufflevector(v, v, 0, 1, -1, -1);
    }
    else {
      constexpr int S = (T >> 2) % 3, s = T & 3;
      if constexpr (M < 6) {
        const u32 a = (M < 4 ? aw[0][M & 1] + S * 16384 + (M >> 1) * 8192 : aw[1][M & 1] + S * 12288) + s * 2048;
        return trd2(a, a + 512);
      } else {
        const u32 a = ac + S * 12288 + s * 1024;
        return trd2(a, a + 256);
      }
    }
  };
  u32x4 xf[4], wf[2 * NBK];
  u32x2 fm;
  f32x16 acc[NBK], P0, P1;
  float rstd;
  u32x2 sv[8];
  u32x4 eo, af[28];
  float sa;
  const u32 tb = rt * 4 + m;
  const u32 obase = FFN ? HF_OFF + tb * 112 * 1024 : QF_OFF + (tb * 32 + 4 * j) * 1024;
  WAITL0();
  STAMP(c, 1);
  WAITV(NCP);
  BAR();
  STAMP(c, 2);
  xf[0] = lds128(ax[0]);
  sfor<0, NBK>([&](auto NN) { wf[decltype(NN)::v] = rdw(NN); });
  SB();
  auto filler = [&](auto NN) {
    constexpr int n = decltype(NN)::v;
    if constexpr (n == SL::bar(SG) - 5) {
      WAITV(0);
      xa = lds128(L_ST + m * 2048 + lane * 16);
      xb = lds128(L_ST + m * 2048 + 1024 + lane * 16);
      const u32 a = LBS + m * 4096 + (4 * hh + q) * 128 + at;
#pragma unroll
      for (int k = 0; k < 8; k++) sv[k] = trd((a ^ ((k >> 2) * 64)) + (k & 3) * 1024);
      set_tag(c);
    }
    if constexpr (n == SL::bar(SG) - 2) {
      u32 ones = ONES2;
      asm volatile("" : "+s"(ones));
      float st[4] = {0.f, 0.f, 0.f, 0.f}, cs[2] = {0.f, 0.f};
#pragma unroll
      for (int d = 0; d < 4; d++) {
        st[0] = dot2(ones, xa[d], st[0]);
        st[1] = dot2(xa[d], xa[d], st[1]);
        st[2] = dot2(ones, xb[d], st[2]);
        st[3] = dot2(xb[d], xb[d], st[3]);
      }
#pragma unroll
      for (int d = 0; d < 8; d++) {
        cs[0] = dot2(ones, sv[d >> 1][d & 1], cs[0]);
        cs[1] = dot2(ones, sv[4 + (d >> 1)][d & 1], cs[1]);
      }
      swap32(st[0], st[1]);
      swap32(st[2], st[3]);
      swap32(cs[0], cs[1]);
      *(LAS float*)(L_ST + m * 2048 + lane * 4) = cs[0] + cs[1];
      st[0] += st[1];
      OPAQUE(st[0]);
      st[2] += st[3];
      auto sw16 = __builtin_amdgcn_permlane16_swap(__float_as_uint(st[0]), __float_as_uint(st[2]), false, false);
      sa = __uint_as_float(sw16[0]) + __uint_as_float(sw16[1]);
      OPAQUE(sa);
    }
    if constexpr (n == SL::bar(SG) - 1) {
      sfor<0, 4>([&](auto KK) { sa += __int_as_float(__builtin_amdgcn_update_dpp(0, __float_as_int(sa), 0x120 + (8 >> decltype(KK)::v), 0xf, 0xf, false)); });
      float mean = sa * (1.f / 512.f), ms = mean;
      swap32(mean, ms);
      ms = __builtin_amdgcn_rsqf(mx(__builtin_fmaf(-mean, mean, ms), 0.f) + 1e-5f);
      const u32 mh = pk(-mean, -mean);
      const float ml = -mean - __uint_as_float(mh & 0xffff0000u);
      *(LAS u32x2*)(L_ST + m * 2048 + 1024 + (((lane >> 4) & 1) | ((lane & 15) << 1) | (lane & 32)) * 8) = u32x2{(mh & 0xffffu) | (pk(ml, ml) & 0xffff0000u), __float_as_uint(ms)};
    }
    if constexpr (n >= SL::N7 + 5) {
      constexpr int g = (n - SL::N7) % 5, M = SL::blk(n - 5);
      if constexpr (g >= 1) {
        constexpr int s = (g - 1) >> 1, h = (g - 1) & 1;
        if constexpr (FFN || M < 4) {
          eo[2 * h] = pk2(pr<8 * s + 4 * h>(acc[M]));
          eo[2 * h + 1] = pk2(pr<8 * s + 4 * h + 2>(acc[M]));
          if constexpr (FFN) {
            eo[2 * h] = relu2(eo[2 * h]);
            eo[2 * h + 1] = relu2(eo[2 * h + 1]);
          }
          if constexpr (h == 0) OPAQUE(eo);
        }
        if constexpr (h == 1) {
          if constexpr (FFN) {
            af[2 * M + s] = eo;
            st128(eo, c.ws, lane * 16, obase + ((t.c0 >> 5) + 2 * M + s) * 1024);
          }
          else if constexpr (M == 1) {
            *(LAS u32x4*)(((m >> 1) ? L_SP2 : L_SP1) + (m & 1) * 2048 + s * 1024 + lane * 16) = eo;
            if constexpr (s == 1) sem_post(SEM_K + m);
          } else if constexpr (M == 0) {
            const u32x4 wp = wphi_frag(L_WPHI + s * 512 + (4 * hh + q) * 32 + p * 8, blk ? 0x80008000u : 0u);
            asm volatile("s_nop 1\n\tv_mfma_f32_32x32x16_bf16 %0, %1, %2, 0" : "=&v"(s ? P1 : P0) : "v"(wp), "v"(eo));
          } else if constexpr (M < 4) st128(eo, c.ws, lane * 16, obase + (2 * (M - 2) + s) * 1024);
        }
      }
    }
  };
  sfor<0, SL::NS>([&](auto NN) {
    constexpr int n = decltype(NN)::v, T = SL::step(n), M = SL::blk(n);
    constexpr bool TR = !FFN && M >= 4;
    const u32x4 xo = xf[T & 3], wo_ = wf[n % (2 * NBK)];
    if constexpr (T < 0) {
      const u32x2 cf = __builtin_shufflevector(wo_, wo_, 0, 1);
      mma8(acc[M], TR ? fm : cf, TR ? cf : fm);
    } else if constexpr (T == 0) mma0(acc[M], TR ? xo : wo_, TR ? wo_ : xo);
    else mma(acc[M], TR ? xo : wo_, TR ? wo_ : xo);
    constexpr int G = n < SL::N7 ? n / (4 * NBK) : 7;
    if constexpr (n == SL::bar(G) && G < 7) {
      WAITL0();
      STAMP(c, 3 + 2 * G);
      BAR();
      STAMP(c, 4 + 2 * G);
    }
    if constexpr (n + NBK < SL::NS && n != SL::bar(G) - 1) {
      if constexpr (n < SL::N7 && (n == SL::bar(G) || (M == 0 && (T & 3) != 3))) {
        if constexpr (T == 27) {
          fm = *(LAS u32x2*)(hh ? L_ZERO : L_ST + 4096 + (m * 32 + r) * 8);
          xf[0] = lds128(ax[0] + 16384); xf[1] = lds128(ax[1] + 16384); xf[2] = lds128(ax[2] + 16384);
        } else xf[(T + 1) & 3] = lds128(ax[(T + 1) & 3] + (((T + 1) >> 2) % 3) * 16384);
      }
      if constexpr (n == SL::bar(G)) wf[(n - 1 + NBK) % (2 * NBK)] = rdw(IC<n - 1 + NBK>{});
      wf[(n + NBK) % (2 * NBK)] = rdw(IC<n + NBK>{});
      if constexpr (n == SL::N7) {
        xf[3] = lds128(ax[3] + 16384);
        if constexpr (!FFN) rstd = *(LAS float*)(L_RS + m * 128 + r * 4);
      }
    }
    if constexpr (n == SL::N7) __builtin_amdgcn_s_setprio(P7);
    if constexpr (n == SL::NS - NBK - 1) sem_post(SEM_7);
    if constexpr (FFN && n == SL::N7 + QK) sem_post(SEM_K + m);
    filler(NN);
    SB();
  });
  STAMP(c, 18);
  __builtin_amdgcn_s_setprio(3);
  u32 ab[3][2];
#pragma unroll
  for (int k = 0; k < 6; k++) {
    ab[k >> 1][k & 1] = (((4 * hh + q) * 128 + at) ^ ((k & 1) * 64)) + (k >> 1) * 57344;
    OPAQUE(ab[k >> 1][k & 1]);
  }
  constexpr auto stp = [](int u) { return u < 96 ? u >> 2 : 24 + (u & 3); };
  constexpr auto blo = [](int u) { return u < 96 ? u & 3 : (u - 96) >> 2; };
  auto rd2 = [&](auto UU) -> u32x4 {
    constexpr int n = stp(decltype(UU)::v), k = blo(decltype(UU)::v);
    constexpr u32 L = bloc(n >> 1), W = L / 57344;
    const u32 a = ab[W][k & 1] + (L - W * 57344) + ((n & 1) * 2 + (k >> 1)) * 2048;
    return trd2(a, a + 1024);
  };
  u32x4 w2f[8];
  if constexpr (FFN) {
    sem_wait(SEM_B0, 4);
    sfor<0, 4>([&](auto UU) { w2f[decltype(UU)::v] = rd2(UU); });
    asm volatile("s_nop 15" : "+v"(acc[6]));
    sfor<0, 2>([&](auto SS) {
      u32x4 o = pack8<decltype(SS)::v>(acc[6]);
#pragma unroll
      for (int d = 0; d < 4; d++) o[d] = relu2(o[d]);
      af[12 + decltype(SS)::v] = o;
      st128(o, c.ws, lane * 16, obase + ((t.cc >> 5) + decltype(SS)::v) * 1024);
    });
  } else {
    const u32 apw = lx(1) + m * 2048 + r * 64 + ((hh ^ ((r >> 1) & 7)) << 3);
    WAITV(0);
    publish(c, 0xf00ull);
    sem_wait(SEM_7, 4);
    phi_store(apw, P0, rstd * LOG2E);
    SB();
    const u32 av0 = L_ST + m * 2048 + lane * 16, av1 = lwb(1) + m * 2048 + lane * 16;
    sfor<0, 2>([&](auto TT) {
      constexpr int tt = decltype(TT)::v;
      const f32x4 ra = ldsf4(L_RS + m * 128 + (16 * tt + 4 * hh) * 4), rb = ldsf4(L_RS + m * 128 + (16 * tt + 8 + 4 * hh) * 4);
      const f32x4 a0 = q4<8 * tt>(acc[4]) * ra, b0 = q4<8 * tt + 4>(acc[4]) * rb, a1 = q4<8 * tt>(acc[5]) * ra, b1 = q4<8 * tt + 4>(acc[5]) * rb;
      *(LAS u32x4*)(av0 + tt * 1024) = u32x4{pk(a0[0], a0[1]), pk(a0[2], a0[3]), pk(b0[0], b0[1]), pk(b0[2], b0[3])};
      *(LAS u32x4*)(av1 + tt * 1024) = u32x4{pk(a1[0], a1[1]), pk(a1[2], a1[3]), pk(b1[0], b1[1]), pk(b1[2], b1[3])};
    });
    phi_store(apw + 8192, P1, rstd * LOG2E);
    sem_post(SEM_P);
    STAMP(c, 19);
    sem_wait(SEM_P, 8);
    const u32 t0 = 4 * hh + q;
    const u32 apr = ((m >> 1) ? lwa(1) : lx(1)) + (m & 1) * 8192 + t0 * 64 + (((4 * blk + p) ^ (t0 >> 1)) << 3);
    const u32 avr = ((m >> 1) ? lwb(1) : L_ST) + lane * 16;
    const bool isv = blk == (m & 1);
    u32x4 pa[8], vb[8];
#pragma unroll
    for (int k = 0; k < 8; k++) {
      pa[k] = trd2(apr + k * 1024, (apr ^ 32) + k * 1024 + 512);
      vb[k] = lds128(avr + k * 1024);
    }
    sem_post(SEM_KV);
    f32x16 X = f32x16{};
#pragma unroll
    for (int k = 0; k < 8; k++) {
#pragma unroll
      for (int d = 0; d < 4; d++) vb[k][d] = isv ? vb[k][d] : ONES2;
      X = mfma(pa[k], vb[k], X);
    }
    const u32 pi = (4 * j + m) * 16 + rt;
    const u32 ve = isv ? KV_OFF + pi * 2048 + (hh * 16 + (lane & 15)) * 16 : KZ_OFF + pi * 128 + hh * 16, vs = isv ? 512 : 32;
    st128(__builtin_bit_cast(u32x4, q4<0>(X)), c.ws, ve, 0);
    st128(__builtin_bit_cast(u32x4, q4<4>(X)), c.ws, ve + vs, 0);
    st128(__builtin_bit_cast(u32x4, q4<8>(X)), c.ws, ve + 2 * vs, 0);
    st128(__builtin_bit_cast(u32x4, q4<12>(X)), c.ws, ve + 3 * vs, 0);
  }
  STAMP(c, 20);
  __builtin_amdgcn_s_setprio(0);
  const int b = blockIdx.x;
  BSrc b2;
  b2.init2(c, w2);
  const u32 fh = MB_OFF + b * 1024 + 16 * m + (FFN ? 8 - 8 * c.par : 0);
  const u32 ah = HF_OFF + (tb * 112 + 28 * (b & 3)) * 1024;
  auto lda = [&](auto NN) {
    constexpr int N = decltype(NN)::v;
    af[N] = ld128(c.ws, lane * 16, ah + (2 * aset<(N >> 1)>(c) + (N & 1)) * 1024);
  };
  u64 ph;
  if constexpr (!FFN) {
    ph = peek(c, fh, 2);
    WAITV(1);
    publish(c, 0xffull);
    STAMP(c, 23);
    sem_wait(SEM_KV, 4);
    sfor<10, 14>([&](auto NN) { b2.issue<decltype(NN)::v>(c); });
    sfor<0, 4>([&](auto UU) { w2f[decltype(UU)::v] = rd2(UU); });
    WAITV(9);
    await(c, fh, 2, ph);
    STAMP(c, 25);
    sfor<0, QW>(lda);
    SB();
  } else sem_wait(SEM_7, 4);
  const u32 xb_ = XP_OFF + (b & 3) * 8192 + (m & 1) * 4096;
  const u32 xo0 = xb_ + blk_of(rt, (m >> 1) * 8 + ((b >> 3) & 3)) * 32768, xo1 = xb_ + blk_of(rt, (m >> 1) * 8 + 4 + ((b >> 3) & 3)) * 32768;
  auto send = [&](const f32x16& a, int k) {
    st128(pack8<0>(a), c.ws, lane * 16, (k >> 1 ? xo1 : xo0) + (k & 1) * 2048);
    st128(pack8<1>(a), c.ws, lane * 16, (k >> 1 ? xo1 : xo0) + (k & 1) * 2048 + 1024);
  };
  f32x16 ac2[4];
  u32 sp = 0, sx = 0, pz = 0;
  STAMP(c, 27);
  SB();
  sfor<0, 112>([&](auto UU) {
    constexpr int u = decltype(UU)::v, n = stp(u), k = blo(u);
    if constexpr (u < 4) mma0(ac2[k], w2f[u & 7], af[n]);
    else mma(ac2[k], w2f[u & 7], af[n]);
    SB();
    if constexpr (u == 32 || u == 72) sp = sem_peek(u == 32 ? SEM_B : SEM_B2);
    if constexpr (u == 35 || u == 75) sem_check(sp, u == 35 ? SEM_B : SEM_B2, 4);
    if constexpr ((PAUSE & (FFN ? 1 : 2)) && u < 96) {
      if constexpr ((u & 7) == 5) sx = sem_peek(SEM_S);
      if constexpr ((u & 7) == 0 && u > 0) {
        if (__builtin_amdgcn_readfirstlane(sx) > pz) {
          sem_wait(SEM_X, 4);
          pz = 4;
        }
      }
    }
    if constexpr (u + 4 < 112) w2f[(u + 4) & 7] = rd2(IC<(u + 4 < 112 ? u + 4 : 0)>{});
    if constexpr (FFN) {
      if constexpr (u < 8) b2.template issue1<10 + (u < 8 ? u >> 1 : 0), (u & 1)>(c);
      if constexpr (u == PUBU) {
        WAITV(8);
        publish(c, 0xfull);
        STAMP(c, 23);
      }
      if constexpr (u == PKU) ph = peek(c, fh, 1);
      if constexpr (u == CKU) {
        WAITV(0);
        sem_post(SEM_B2);
        STAMP(c, 24);
        await(c, fh, 1, ph);
        STAMP(c, 25);
        sfor<14, 28>(lda);
      }
    } else {
      if constexpr (u == 0) sem_post(SEM_B2);
      if constexpr ((u & 3) == 1 && (u >> 2) + QW < 28) lda(IC<((u >> 2) + QW < 28 ? (u >> 2) + QW : 0)>{});
    }
    if constexpr (u >= 100 && (u & 3) == 1) send(ac2[k - 1], k - 1);
    SB();
  });
  STAMP(c, 31);
  asm volatile("s_nop 15" : "+v"(ac2[3]));
  send(ac2[3], 3);
  WAITV(0);
  publish(c, 0x3000ull);
  sem_post(SEM_C);
  STAMP(c, 32);
}

#define ROTB 1
__global__ __launch_bounds__(512, 2) void fused_k(const u16* x, const u16* wqkv, const u16* wphi, const u16* wo, const u16* w1, const u16* w2, u16* out,
                                                  char* ws) {
  __shared__ __attribute__((aligned(16))) char smem[163840];
  asm volatile("" ::"v"((LAS char*)smem));
  const int b = blockIdx.x, xcd = b & 7, r = b >> 3;
  u64 pc0;
  asm volatile("s_getpc_b64 %0" : "=s"(pc0));
  {
    u32 off = ((r * 8 + (threadIdx.x >> 6)) * 3 + (threadIdx.x & 3)) * 64;
    off = off < CODE_BYTES ? off : 0;
    asm volatile("s_mov_b32 m0, %2\n\ts_mov_b64 exec, 7\n\tglobal_load_lds_dword %0, %1\n\ts_mov_b64 exec, -1" ::"v"(off), "s"(pc0), "i"(L_RS) : "memory");
  }
  Ctx c;
  c.dbg = 0;
  STAMP(c, 0);
  c.lane = threadIdx.x & 63;
  const u32 w = __builtin_amdgcn_readfirstlane(threadIdx.x >> 6);
  c.m = w & 3;
  c.ws = __builtin_amdgcn_make_buffer_rsrc(ws, 0, WS_BYTES, 0x00020000);
  c.wsp = ws;
  c.par = (b >> 4) & 1;
  const int rt = (xcd >> 2) * 8 + (r >> 2), ct = (xcd & 3) * 4 + (r & 3), j = ct >> 1;
  Tile t;
  t.rt = rt;
  t.j = j;
  t.ct = ct;
  t.ffn = ct & 1;
  t.ldw = t.ffn ? 3584 : 3072;
  const u32 lane = c.lane, m = c.m;
  t.srd_x = make_srd(x, 2048 * 512 * 2);
  t.srd_w = make_srd(t.ffn ? w1 : wqkv, 512 * t.ldw);
  t.vx0 = (lane >> 3) * 1024 + (((lane & 7) ^ (lane >> 4)) << 4);
  {
    const u32 sx = (rt * 128 + m * 32 + (w >> 2) * 16) * 1024, dx = m * 4096 + (w >> 2) * 2048;
    dma<LX, 0, NTX>(t.srd_x, t.vx0, sx, dx);
    dma<LX + 1024, 8192, NTX>(t.srd_x, t.vx0 ^ 64, sx, dx);
    t.vw = (lane >> 3) * t.ldw + (((lane & 7) ^ (((lane >> 4) & 1) << 2)) << 4);
    t.c0 = t.ffn ? 448 * j + (j & 1) * 64 : 1024 + 128 * j;
    t.c1 = t.ffn ? t.c0 + 128 : 128 * j;
    t.c2 = t.ffn ? t.c0 + 256 : 2048 + 128 * j;
    t.cc = t.ffn ? 448 * j + ((j & 1) ? 0 : 384) : 0;
    dma<LWA, 0, NTW>(t.srd_w, t.vw, w * 8 * t.ldw + t.c0, w * 1024);
    dma<LWA + 8192, 0, NTW>(t.srd_w, t.vw, w * 8 * t.ldw + t.c1, w * 1024);
    dma<LWB, 0, NTW>(t.srd_w, t.vw, w * 8 * t.ldw + t.c2, w * 1024);
    if (t.ffn && w >= 4) dma<LWB + 8192, 0, NTW>(t.srd_w, (lane >> 2) * 3584 + ((lane & 3) << 4), m * 16 * 3584 + t.cc, m * 1024);
    BAR();
    SB();
  }
  if (__builtin_expect(w >= 4, 1)) {
    helper(c, t, x, wo, w2, out);
  } else {
    {
      const u32 vs = m < 3 ? t.vw : (lane >> 3) * t.ldw + ((lane & 3) << 4);
      const u32 so = rt * 32 * t.ldw + (m == 0 ? t.c0 : m == 1 ? t.c1 : m == 2 ? t.c2 : t.cc);
      dma<LBS, 0, NTS>(t.srd_w, vs, so, m * 4096);
      dma<LBS + 1024, 0, NTS>(t.srd_w, vs, so + 8 * t.ldw, m * 4096);
      dma<LBS + 2048, 0, NTS>(t.srd_w, vs, so + 16 * t.ldw, m * 4096);
      dma<LBS + 3072, 0, NTS>(t.srd_w, vs, so + 24 * t.ldw, m * 4096);
      dma<L_ST, 0>(t.srd_x, lane * 16, (rt * 128 + ct * 8 + m * 2) * 1024, m * 2048);
      dma<L_ST + 1024, 1024>(t.srd_x, lane * 16, (rt * 128 + ct * 8 + m * 2) * 1024, m * 2048);
      dma<L_WPHI, 0>(make_srd(wphi, 32 * 512), lane * 16, m < 2 ? (u32)(j * 2048 + m * 1024) : (u32)(((b & 7) * 2 + ((b >> 4) & 1)) * 1024), (m < 2 ? m : 2) * 1024);
      const u32 t1 = t.ffn ? blk_of(rt, (ct & 12) + (lane & 3)) : (lane & 7) * 32 + m * 8 + j, f1 = t.ffn ? m * 2 + (j & 1) : 16 + rt;
      load_tag(c, lane < 8 ? t1 : lane < 12 ? (rt >> 1) * 32 + (lane & 3) * 8 + j : lane < 14 ? blk_of(rt, (m >> 1) * 8 + (lane & 1) * 4 + (ct & 3)) : b,
               lane < 8 ? f1 : lane < 12 ? 96 + (rt & 1) * 4 + m : 104 + (m & 1) * 4 + (ct >> 2));
      *(LAS u32*)(L_SEM + (lane & 31) * 4) = 0;
    }
    if (t.ffn) compute1<true>(c, t, w2);
    else compute1<false>(c, t, w2);
  }
}

extern "C" void kernel_launch(void* const* d_in, const int* in_sizes, int n_in, void* d_out, int out_size, void* d_ws, size_t ws_size,
                              hipStream_t stream) {
  const u16 *x = (const u16*)d_in[0], *wqkv = (const u16*)d_in[1], *wphi = (const u16*)d_in[2], *wo = (const u16*)d_in[3];
  const u16 *w1 = (const u16*)d_in[5], *w2 = (const u16*)d_in[6];
  fused_k<<<256, 512, 0, stream>>>(x, wqkv, wphi, wo, w1, w2, (u16*)d_out, (char*)d_ws);
}
```

```cpp
#include <hip/hip_runtime.h>
#pragma clang diagnostic ignored "-Wint-to-pointer-cast"
typedef unsigned short u16;
typedef unsigned u32;
typedef unsigned long long u64;
typedef u32 u32x2 __attribute__((ext_vector_type(2)));
typedef u32 u32x4 __attribute__((ext_vector_type(4)));
typedef short s16x2 __attribute__((ext_vector_type(2)));
typedef short s16x4 __attribute__((ext_vector_type(4)));
typedef float f32x2 __attribute__((ext_vector_type(2)));
typedef float f32x4 __attribute__((ext_vector_type(4)));
typedef float f32x16 __attribute__((ext_vector_type(16)));
typedef __bf16 b16x2 __attribute__((ext_vector_type(2)));
typedef __bf16 b16x8 __attribute__((ext_vector_type(8)));
#define LAS __attribute__((address_space(3)))
#define GAS __attribute__((address_space(1)))
#define DI __device__ __forceinline__
#define SB() __builtin_amdgcn_sched_barrier(0)
#define BAR() __builtin_amdgcn_s_barrier()
#define WAITV(n) do { asm volatile("s_waitcnt vmcnt(%0)" ::"i"(n) : "memory"); SB(); } while (0)
#define WAITL0() asm volatile("s_waitcnt lgkmcnt(0)" ::: "memory")
#define OPAQUE(x) asm volatile("" : "+v"(x))

constexpr u32 HF_OFF = 0;
constexpr u32 QF_OFF = 64u * 112u * 1024u;
constexpr u32 OF_OFF = QF_OFF + 64u * 32u * 1024u;
constexpr u32 KV_OFF = OF_OFF + 64u * 32u * 1024u;
constexpr u32 KZ_OFF = KV_OFF + 32u * 16u * 2048u;
constexpr u32 CS_OFF = KZ_OFF + 32u * 16u * 128u;
constexpr u32 RS_OFF = CS_OFF + 256u * 1024u;
constexpr u32 MB_OFF = RS_OFF + 2048u * 8u;
constexpr u32 EP_OFF = MB_OFF + 256u * 1024u;
constexpr u32 XP_OFF = EP_OFF + 2048u;
constexpr u32 WS_BYTES = XP_OFF + 256u * 32768u;
constexpr u32 LX = 0, LWA = 49152, LWB = 98304, LBS = 135168, LSCR = 159744;
constexpr u32 lx(int s) { return LX + s * 16384; }
constexpr u32 lwa(int s) { return LWA + s * 16384; }
constexpr u32 lwb(int s) { return LWB + s * 12288; }
constexpr u32 L_WPHI = LSCR, L_WPA = LSCR + 2048, L_RS = LSCR + 3072, L_SEM = LSCR + 3584, L_ZERO = LSCR + 3648;
constexpr u32 L_SP0 = lwb(0) + 8192, L_SP1 = lwb(1) + 8192, L_SP2 = lwb(2) + 8192;
constexpr u32 L_ST = LBS + 16384;
constexpr int AUX = 16;

DI f32x16 mfma(u32x4 a, u32x4 b, f32x16 c) {
  return __builtin_amdgcn_mfma_f32_32x32x16_bf16(__builtin_bit_cast(b16x8, a), __builtin_bit_cast(b16x8, b), c, 0, 0, 0);
}
DI void mma(f32x16& d, u32x4 a, u32x4 b) { asm volatile("v_mfma_f32_32x32x16_bf16 %0, %1, %2, %0" : "+v"(d) : "v"(a), "v"(b)); }
DI void mma0(f32x16& d, u32x4 a, u32x4 b) { asm volatile("v_mfma_f32_32x32x16_bf16 %0, %1, %2, 0" : "=&v"(d) : "v"(a), "v"(b)); }
DI void mma8(f32x16& d, u32x2 a, u32x2 b) { asm volatile("v_mfma_f32_32x32x8_bf16 %0, %1, %2, %0" : "+v"(d) : "v"(a), "v"(b)); }
DI u32x2 trd(u32 addr) { return __builtin_bit_cast(u32x2, __builtin_amdgcn_ds_read_tr16_b64_v4i16((LAS s16x4*)addr)); }
DI u32x4 trd2(u32 a0, u32 a1) {
  u32x2 lo = trd(a0), hi = trd(a1);
  return __builtin_shufflevector(lo, hi, 0, 1, 2, 3);
}
DI u32x4 lds128(u32 addr) { return *(LAS u32x4*)addr; }
DI f32x4 ldsf4(u32 addr) { return *(LAS f32x4*)addr; }
DI u32 pk(float a, float b) {
  f32x2 v = {a, b};
  return __builtin_bit_cast(u32, __builtin_convertvector(v, b16x2));
}
DI u32 pk2(f32x2 v) { return __builtin_bit_cast(u32, __builtin_convertvector(v, b16x2)); }
DI u32 relu2(u32 v) { return __builtin_bit_cast(u32, __builtin_elementwise_max(__builtin_bit_cast(s16x2, v), s16x2{0, 0})); }
extern "C" __device__ float fmul_legacy_(float, float) __asm("llvm.amdgcn.fmul.legacy");
DI float mul1(float a, float b) { return fmul_legacy_(a, b); }
DI float mx(float a, float b) { return __builtin_elementwise_maximum(a, b); }
DI float dot2(u32 a, u32 b, float c) {
  return __builtin_amdgcn_fdot2_f32_bf16(__builtin_bit_cast(b16x2, a), __builtin_bit_cast(b16x2, b), c, false);
}
DI void swap32(float& a, float& b) {
  auto r = __builtin_amdgcn_permlane32_swap(__float_as_uint(a), __float_as_uint(b), false, false);
  a = __uint_as_float(r[0]);
  b = __uint_as_float(r[1]);
}
template <int I> DI f32x2 pr(const f32x16& a) { return __builtin_shufflevector(a, a, I, I + 1); }
template <int I> DI f32x4 q4(const f32x16& a) { return __builtin_shufflevector(a, a, I, I + 1, I + 2, I + 3); }
DI u32x4 make_srd(const void* p, u32 bytes) {
  u64 a = (u64)p;
  u32x4 r = {(u32)a, (u32)(a >> 32) & 0xffffu, bytes, 0x00020000u};
  return r;
}
template <u32 LI, u32 SI>
DI void dma(u32x4 srd, u32 voff, u32 sbase, u32 lbase) {
  u32 t;
  asm volatile("s_add_u32 m0, %4, %6\n\ts_add_u32 %0, %3, %5\n\tbuffer_load_dwordx4 %1, %2, %0 offen lds"
               : "=&s"(t)
               : "v"(voff), "s"(srd), "s"(sbase), "s"(lbase), "i"(SI), "i"(LI)
               : "memory", "scc");
}
DI void st128(u32x4 v, __amdgpu_buffer_rsrc_t rs, u32 voff, u32 soff) {
  __builtin_amdgcn_raw_buffer_store_b128(v, rs, voff, soff, AUX);
  asm volatile("s_nop 1" ::"v"(v) : "memory");
}
DI u32x4 ld128(__amdgpu_buffer_rsrc_t rs, u32 voff, u32 soff) { return __builtin_amdgcn_raw_buffer_load_b128(rs, voff, soff, AUX); }
template <int OFF> DI void gld64(u64& d, u32 voff, const char* base) {
  asm volatile("global_load_dwordx2 %0, %1, %2 offset:%3 sc1" : "=v"(d) : "v"(voff), "s"(base), "i"(OFF) : "memory");
}
template <int OFF> DI void gld128(f32x4& d, u32 voff, const char* base) {
  asm volatile("global_load_dwordx4 %0, %1, %2 offset:%3 sc1" : "=v"(d) : "v"(voff), "s"(base), "i"(OFF) : "memory");
}
template <int OFF> DI void warm1(u32& d, const void* base) { asm volatile("s_load_dword %0, %1, %2" : "+s"(d) : "s"(base), "i"(OFF)); }
template <int N> struct IC { static constexpr int v = N; };
template <int B, int E, class F> DI void sfor(F&& f) {
  if constexpr (B < E) {
    f(IC<B>{});
    sfor<B + 1, E>(f);
  }
}
constexpr u32 ONES2 = 0x3f803f80u;
constexpr float LOG2E = 1.4426950408889634f;

DI void dual_softmax(const f32x16& P, float sl, u32x4& o0, u32x4& o1) {
  float m0 = mx(mx(mx(mx(mx(mx(mx(P[0], P[1]), P[2]), P[3]), P[4]), P[5]), P[6]), P[7]);
  float m1 = mx(mx(mx(mx(mx(mx(mx(P[8], P[9]), P[10]), P[11]), P[12]), P[13]), P[14]), P[15]);
  swap32(m0, m1);
  m0 = mx(m0, m1) * -sl;
  m1 = m0;
  swap32(m0, m1);
  f32x2 e[8];
  sfor<0, 8>([&](auto II) {
    constexpr int I = decltype(II)::v;
    const float m = I < 4 ? m0 : m1;
    e[I] = __builtin_elementwise_fma(pr<2 * I>(P), f32x2{sl, sl}, f32x2{m, m});
  });
#pragma unroll
  for (int i = 0; i < 8; i++) e[i] = f32x2{__builtin_amdgcn_exp2f(e[i][0]), __builtin_amdgcn_exp2f(e[i][1])};
  const f32x2 s0 = (e[0] + e[1]) + (e[2] + e[3]), s1 = (e[4] + e[5]) + (e[6] + e[7]);
  m0 = s0[0] + s0[1];
  OPAQUE(m0);
  m1 = s1[0] + s1[1];
  swap32(m0, m1);
  m0 = __builtin_amdgcn_rcpf(m0 + m1);
  m1 = m0;
  swap32(m0, m1);
#pragma unroll
  for (int i = 0; i < 4; i++) {
    o0[i] = pk2(e[i] * m0);
    o1[i] = pk2(e[4 + i] * m1);
  }
}
DI u32x4 wphi_frag(u32 a, u32 sg) {
  u32x4 f = trd2(a, a + 256);
  f[0] ^= sg; f[1] ^= sg; f[2] ^= sg; f[3] ^= sg;
  return f;
}
DI void dual_softmax1(const f32x16& P, float sl, u32x4& o0, u32x4& o1) {
  float m0 = mx(mx(mx(mx(mx(mx(mx(P[0], P[1]), P[2]), P[3]), P[4]), P[5]), P[6]), P[7]);
  float m1 = mx(mx(mx(mx(mx(mx(mx(P[8], P[9]), P[10]), P[11]), P[12]), P[13]), P[14]), P[15]);
  swap32(m0, m1);
  m0 = mx(m0, m1) * -sl;
  m1 = m0;
  swap32(m0, m1);
  float e[16];
#pragma unroll
  for (int i = 0; i < 16; i++) {
    e[i] = __builtin_fmaf(P[i], sl, i < 8 ? m0 : m1);
    OPAQUE(e[i]);
  }
#pragma unroll
  for (int i = 0; i < 16; i++) e[i] = __builtin_amdgcn_exp2f(e[i]);
  m0 = e[0];
  m1 = e[8];
#pragma unroll
  for (int i = 1; i < 8; i++) {
    m0 += e[i];
    OPAQUE(m0);
    m1 += e[8 + i];
    OPAQUE(m1);
  }
  swap32(m0, m1);
  m0 = __builtin_amdgcn_rcpf(m0 + m1);
  m1 = m0;
  swap32(m0, m1);
#pragma unroll
  for (int i = 0; i < 4; i++) {
    o0[i] = pk(mul1(e[2 * i], m0), mul1(e[2 * i + 1], m0));
    o1[i] = pk(mul1(e[8 + 2 * i], m1), mul1(e[9 + 2 * i], m1));
  }
}
DI void phi_put(u32 apw, u32x4 o0, u32x4 o1) {
  *(LAS u32x2*)(apw) = u32x2{o0[0], o0[1]};
  *(LAS u32x2*)(apw ^ 16) = u32x2{o0[2], o0[3]};
  *(LAS u32x2*)(apw ^ 32) = u32x2{o1[0], o1[1]};
  *(LAS u32x2*)(apw ^ 48) = u32x2{o1[2], o1[3]};
}
DI void phi_store(u32 apw, const f32x16& P, float sl) {
  u32x4 o0, o1;
  dual_softmax(P, sl, o0, o1);
  phi_put(apw, o0, o1);
}
template <int S> DI u32x4 pack8(const f32x16& a) { return u32x4{pk2(pr<8 * S>(a)), pk2(pr<8 * S + 2>(a)), pk2(pr<8 * S + 4>(a)), pk2(pr<8 * S + 6>(a))}; }

constexpr u32 slot_loc(int s, int k) { return k < 2 ? lx(s) + k * 8192 : k < 4 ? lwa(s) + (k - 2) * 8192 : lwb(s); }
constexpr u32 bloc(int n) {
  return n < 5 ? slot_loc(2, n) : n < 10 ? slot_loc(0, n - 5) : n < 15 ? slot_loc(1, n - 10) : n < 40 ? LBS + (n & 1) * 8192 : LBS + (n - 40) * 8192;
}

struct Ctx {
  __amdgpu_buffer_rsrc_t ws;
  char* wsp;
  u32 m, lane;
  u32 ab[3][2];
  u32 par;
  u64 tag;
  u64 own;
  u32 fl;
};
template <int N> DI u32x4 rdb(const Ctx& c, int cb) {
  constexpr u32 W = bloc(N) / 57344;
  const u32 a = c.ab[W][cb] + (bloc(N) - W * 57344);
  return trd2(a, a + 1024);
}
DI void init_ab(Ctx& c) {
  const u32 lane = c.lane, m = c.m, hh = lane >> 5, blk = (lane >> 4) & 1, q = (lane & 15) >> 2, p = lane & 3;
#pragma unroll
  for (int cb = 0; cb < 2; cb++) {
    const u32 t = m * 2048 + (4 * hh + q) * 128 + (((4 * (cb ^ (m & 1)) + 2 * blk + (p >> 1)) ^ ((q >> 1) << 2)) << 4) + 8 * (p & 1);
#pragma unroll
    for (int k = 0; k < 3; k++) {
      c.ab[k][cb] = t + k * 57344;
      OPAQUE(c.ab[k][cb]);
    }
  }
}
DI void load_tag(Ctx& c, u32 tgt, u32 slot) {
  c.fl = MB_OFF + tgt * 1024 + slot * 8;
  asm volatile("global_load_dwordx2 %0, %1, %2 sc1" : "=v"(c.tag) : "v"(EP_OFF + tgt * 8), "s"(c.wsp) : "memory");
}
DI void set_tag(Ctx& c) {
  OPAQUE(c.tag);
  c.tag += 1;
  c.own = ((u64)(u32)__builtin_amdgcn_readlane((u32)(c.tag >> 32), 63) << 32) | (u32)__builtin_amdgcn_readlane((u32)c.tag, 63);
}
DI void publish(const Ctx& c, u64 mask) {
  asm volatile("s_mov_b64 exec, %3\n\tglobal_atomic_swap_x2 %0, %1, %2 sc1\n\ts_mov_b64 exec, -1" ::"v"(c.fl), "v"(c.tag), "s"(c.wsp), "s"(mask) : "memory");
}
DI u64 peek(const Ctx& c, u32 off, int n) {
  u64 v = c.own;
  const u64 z = 0;
  asm volatile("s_mov_b64 exec, %4\n\tglobal_atomic_or_x2 %0, %1, %2, %3 sc0 sc1\n\ts_mov_b64 exec, -1"
               : "+v"(v)
               : "v"(off + c.lane * 8), "v"(z), "s"(c.wsp), "s"((1ull << n) - 1)
               : "memory");
  return v;
}
DI void await(const Ctx& c, u32 off, int n, u64 v) {
  OPAQUE(v);
#pragma unroll 1
  for (int it = 0; it < (1 << 16) && !__all(v == c.own); it++) {
    __builtin_amdgcn_s_sleep(2);
    v = peek(c, off, n);
    asm volatile("s_waitcnt vmcnt(0)" : "+v"(v)::"memory");
  }
}
DI void sem_post(u32 idx) {
  asm volatile("s_mov_b64 exec, 1\n\tds_add_u32 %0, %1\n\ts_mov_b64 exec, -1" ::"v"(L_SEM + idx * 4), "v"(1u) : "memory");
}
DI void sem_wait(u32 idx, u32 n) {
  u32 v, sv, left = 1u << 20;
  asm volatile(
      "1:\n\tds_read_b32 %0, %3\n\ts_sub_u32 %2, %2, 1\n\ts_waitcnt lgkmcnt(0)\n\ts_cbranch_scc1 2f\n\tv_readfirstlane_b32 %1, %0\n\ts_nop 0\n\t"
      "s_cmp_lt_u32 %1, %4\n\ts_cbranch_scc1 1b\n2:"
      : "=&v"(v), "=&s"(sv), "+s"(left)
      : "v"(L_SEM + idx * 4), "s"(n)
      : "memory", "scc");
}
DI u32 sem_peek(u32 idx) { return *(volatile LAS u32*)(L_SEM + idx * 4); }
DI void sem_check(u32 v, u32 idx, u32 n) {
  if (__builtin_amdgcn_readfirstlane(v) < n) sem_wait(idx, n);
  asm volatile("" ::: "memory");
}
constexpr u32 SEM_B = 0, SEM_B2 = 1, SEM_B0 = 2, SEM_7 = 4, SEM_P = 5, SEM_S = 6, SEM_K = 8, SEM_A = 12, SEM_KV = 13, SEM_C = 14, SEM_H = 15;
template <int N> DI u32 aset(const Ctx& c) {
  constexpr int e = N % 7, o = 7 + (e + 1) % 7;
  return c.par ? (N < 7 ? o : e) : (N < 7 ? e : o);
}
struct BSrc {
  u32x4 srd;
  u32 vb, sb, d2;
  DI void init(const Ctx& c, const u16* w, u32 rows, u32 sb_) {
    srd = make_srd(w, rows * 1024);
    vb = (c.lane >> 3) * 1024 + (((c.lane & 7) ^ (((c.lane >> 4) & 1) << 2)) << 4);
    sb = __builtin_amdgcn_readfirstlane(sb_);
    d2 = __builtin_amdgcn_readfirstlane((threadIdx.x & 192) * 32);
  }
  DI void init2(const Ctx& c, const u16* w2) {
    init(c, w2, 1792, ((blockIdx.x & 3) * 448 + (c.m >> 1) * 16) * 1024 + ((blockIdx.x >> 3) & 3) * 256 + (c.m & 1) * 128);
  }
  template <int N, int H> DI void issue1(const Ctx& c) const {
    dma<bloc(N) + H * 1024, H * 8192>(srd, vb, sb + (N < 28 ? aset<N>(c) * 32768 : (N - 28) * 16384), d2);
  }
  template <int N> DI void issue(const Ctx& c) const {
    issue1<N, 0>(c);
    issue1<N, 1>(c);
  }
};

struct Tile {
  int rt, j, ct;
  bool ffn;
  u32 ldw;
  u32 c0, c1, c2, cc;
  u32x4 srd_x, srd_w;
  u32 vx0, vw;
};
DI u32 blk_of(u32 rt, u32 ct) { return (((rt & 7) * 4 + (ct & 3)) << 3) + (rt >> 3) * 4 + (ct >> 2); }

template <int N> DI void put_block(const Ctx& c, const f32x16& a) {
  const u32 p = c.m * 2048 + c.lane * 16 + bloc(N);
  *(LAS u32x4*)p = pack8<0>(a);
  *(LAS u32x4*)(p + 1024) = pack8<1>(a);
}
template <int N> DI void get_block(const Ctx& c, u32 src, u32x4 (&f)[2]) {
  const u32 p = src * 2048 + c.lane * 16 + bloc(N);
  f[0] = lds128(p);
  f[1] = lds128(p + 1024);
}

constexpr int WOFF = 10;
constexpr int QW = 8;
constexpr int PUBU = 16, PKU = 34, CKU = 46;
constexpr int NCP = 8;
constexpr u32 CODE_BYTES = 40960;
DI void helper(Ctx& c, const Tile& t, const u16* x, const u16* wo, const u16* w2, u16* out) {
  constexpr int ND = 10;
  const u32 lane = c.lane, m = c.m;
  const int b = blockIdx.x, i = 2 * t.rt + ((b >> 1) & 1), jc = ((b >> 3) & 3) * 2 + (b & 1);
  const int hd = (b & 7) * 4 + ((b >> 3) & 3), ga = b >> 5;
  const u32 vx1 = t.vx0 ^ 64, vw1 = t.vw + 8 * t.ldw;
  const u32 vc = (lane >> 2) * 3584 + ((lane & 3) << 4);
  const u32 sx = (t.rt * 128 + m * 32) * 1024, sc = m * 16 * 3584 + t.cc;
  const u32 d4 = m * 4096, d2 = m * 2048, d1 = m * 1024;
  u32 wd = 0;
  const char *pb = (const char*)w2 + (b * 4 + m) * 1792, *pc = (const char*)wo + (b * 4 + m) * 512;
  const char* pm = c.wsp + MB_OFF + b * 1024 + m * 256;
  const char* pf = c.wsp + KV_OFF + ((4 * t.j + m) * 16 + t.rt) * 2048 + (t.ffn ? 1024 : 0);
  const char* pz = c.wsp + KZ_OFF + ((4 * t.j + m) * 16 + t.rt) * 128 + (t.ffn ? 64 : 0);
  auto warm = [&](auto KK) {
    constexpr int k = decltype(KK)::v - WOFF;
    if constexpr (k < 0) {
    } else if constexpr (k < 4) warm1<k * 64>(wd, pm);
    else if constexpr (k < 32) warm1<(k - 4) * 64>(wd, pb);
    else if constexpr (k < 40) warm1<(k - 32) * 64>(wd, pc);
    else if constexpr (k == 40) warm1<0>(wd, pz);
    else if constexpr (k < 49) warm1<(k - 41) * 128>(wd, pf);
  };
  auto ringf = [&](auto FF) {
    constexpr int G = decltype(FF)::v / ND, S = G % 3, I = decltype(FF)::v % ND;
    const u32 sg = (m * 16 + G * 64) * t.ldw;
    if constexpr (I == 4) {
      if (t.ffn) dma<lwb(S) + 8192, G * 64 * 3584>(t.srd_w, vc, sc, d1);
    }
    if constexpr (I < 4) dma<lx(S) + I * 1024, I * 8192 + G * 128>(t.srd_x, (I & 1) ? vx1 : t.vx0, sx, d4);
    else if constexpr (I < 6) dma<lwa(S) + (I & 1) * 1024, 0>(t.srd_w, (I & 1) ? vw1 : t.vw, sg + t.c0, d2);
    else if constexpr (I < 8) dma<lwa(S) + 8192 + (I & 1) * 1024, 0>(t.srd_w, (I & 1) ? vw1 : t.vw, sg + t.c1, d2);
    else dma<lwb(S) + (I & 1) * 1024, 0>(t.srd_w, (I & 1) ? vw1 : t.vw, sg + t.c2, d2);
  };
  constexpr auto rend = [](int G) { return G < 0 ? ND + 3 : G == 0 ? 2 * ND + 5 : G == 1 ? 3 * ND + 8 : G < 6 ? (G + 3) * ND : 8 * ND; };
  sfor<ND, rend(-1)>(ringf);
  load_tag(c, lane < 16 ? blk_of(t.rt, lane) : lane < 32 ? blk_of(lane & 15, t.ct) : lane < 40 ? blk_of(2 * ga + (m >> 1), (m & 1) * 8 + (lane & 1) * 4 + ((lane >> 1) & 3)) : b,
           lane < 16 ? 64 + t.ct : lane < 32 ? 80 + t.rt : 32 + hd);
  BSrc b2, bo;
  b2.init2(c, w2);
  bo.init(c, wo, 512, m * 128 * 1024 + jc * 128);
  const u32 hh = lane >> 5, blk = (lane >> 4) & 1, q = (lane & 15) >> 2, p = lane & 3, r = lane & 31;
  init_ab(c);
  u32x4 bf[8][2];
  auto ldb = [&](auto NN) {
    constexpr int N = decltype(NN)::v;
    bf[N - 28][0] = rdb<N>(c, 0);
    bf[N - 28][1] = rdb<N>(c, 1);
  };
  u64 ps = 0, grs = 0;
  f32x4 gc[4];
  __builtin_amdgcn_s_setprio(1);
  WAITV(4);
  BAR();
  constexpr auto wo_in = [](int G) { return G == 3 ? 28 : G == 4 ? 29 : G == 6 ? 30 : G == 7 ? 31 : -1; };
  float sv[4], scs;
  u32 a_cf = L_ST + (m * 64 + (lane & 15) * 4 + (lane >> 4)) * 8, a_fm = L_ST + 4096 + (m * 32 + r) * 8 + hh * 4, a_lr = L_RS + m * 128 + r * 4;
  u32 a_rs = RS_OFF + (t.rt * 128 + m * 32 + r) * 8, a_cs = CS_OFF + t.ct * 16384 + (lane >> 4) * 4096 + m * 256 + (lane & 15) * 16;
  OPAQUE(a_cf); OPAQUE(a_fm); OPAQUE(a_lr); OPAQUE(a_rs); OPAQUE(a_cs);
  sfor<0, 8>([&](auto GG) {
    constexpr int G = decltype(GG)::v;
    constexpr int NRB = G == 6 ? 10 : G == 7 ? 10 : 0;
    if constexpr (G == 5 || G == 6) ldb(IC<23 + G>{});
    if constexpr (G == 1) {
      const u32 cv = *(LAS u32*)(L_ST + m * 2048 + lane * 4), rv = *(LAS u32*)(L_ST + ((lane >> 2) & 3) * 2048 + 1024 + (lane & 3) * 4);
      __builtin_amdgcn_raw_buffer_store_b32(cv, c.ws, lane * 4, CS_OFF + (t.ct * 16 + t.rt) * 1024 + m * 256, AUX);
      asm volatile("s_mov_b64 exec, %3\n\tglobal_store_dword %0, %1, %2 sc1\n\ts_mov_b64 exec, -1" ::"v"(RS_OFF + (t.rt * 128 + t.ct * 8) * 8 + (lane & 15) * 4),
                   "v"(rv), "s"(c.wsp), "s"(m == 0 ? 0xffffull : 0ull)
                   : "memory");
      set_tag(c);
    }
    if constexpr (G == 2) publish(c, 0xffull << (8 * m));
    auto gather = [&]() {
      await(c, MB_OFF + b * 1024 + 512, 32, ps);
      gld64<0>(grs, a_rs, c.wsp);
      gld128<0>(gc[0], a_cs, c.wsp); gld128<1024>(gc[1], a_cs, c.wsp); gld128<2048>(gc[2], a_cs, c.wsp); gld128<3072>(gc[3], a_cs, c.wsp);
    };
    if constexpr (G == 4) ps = peek(c, MB_OFF + b * 1024 + 512, 32);
    if constexpr (G == 5) gather();
    sfor<rend(G - 1), rend(G)>([&](auto FF) {
      ringf(FF);
      warm(IC<decltype(FF)::v - rend(-1)>{});
    });
    if constexpr (G >= 6)
      sfor<(G - 6) * 5, (G - 6) * 5 + NRB / 2>([&](auto NN) {
        constexpr int N = decltype(NN)::v;
        b2.issue<N>(c);
        SB();
        if constexpr (N == 0) {
#pragma unroll
          for (int k = 0; k < 4; k++) OPAQUE(gc[k]);
#pragma unroll
          for (int k = 0; k < 4; k++) {
            sv[k] = gc[0][k];
#pragma unroll
            for (int u = 1; u < 4; u++) {
              sv[k] += gc[u][k];
              OPAQUE(sv[k]);
            }
          }
        } else if constexpr (N == 1) {
          swap32(sv[0], sv[2]);
          swap32(sv[1], sv[3]);
          sv[0] += sv[2];
          OPAQUE(sv[0]);
          sv[1] += sv[3];
          auto sw16 = __builtin_amdgcn_permlane16_swap(__float_as_uint(sv[0]), __float_as_uint(sv[1]), false, false);
          scs = __uint_as_float(sw16[0]) + __uint_as_float(sw16[1]);
          OPAQUE(scs);
        } else if constexpr (N == 2) {
          const u32 h2 = pk(scs, scs);
          const float lo = scs - __uint_as_float(h2 & 0xffff0000u);
          *(LAS u32x2*)a_cf = u32x2{h2, pk(lo, lo)};
        } else if constexpr (N == 3) {
          OPAQUE(grs);
          *(LAS u32*)a_fm = (u32)grs;
          *(LAS u32*)a_lr = (u32)(grs >> 32);
        }
      });
    if constexpr (G >= 6) WAITL0();
    if constexpr (wo_in(G) > 0) bo.issue<wo_in(G)>(c);
    WAITV(G < 3 ? rend(G) - (G + 2) * ND : rend(G) - rend(G - 1) + NRB + (wo_in(G) > 0 ? 2 : 0));
    if constexpr (G < 7) BAR();
    else sem_post(SEM_B0);
  });
  const u32 mb = MB_OFF + b * 1024;
  const u32 fk = mb + 128, fo = mb + 256 + 64 * m, fq = mb + 768 + 16 * m;
  const u32 awp = (4 * hh + q) * 32 + p * 8, sg = blk ? 0x80008000u : 0u;
  asm volatile("" : "+s"(wd));
  u64 pq;
  if (!t.ffn) {
    const u32x4 w0 = wphi_frag(L_WPHI + 1024 + awp, sg), w1 = wphi_frag(L_WPHI + 1536 + awp, sg);
    const float sl = __uint_as_float((u32)(grs >> 32)) * LOG2E;
    sem_wait(SEM_K + m, 1);
    const u32 ak = ((m >> 1) ? L_SP2 : L_SP1) + (m & 1) * 2048 + lane * 16;
    const f32x16 P0 = mfma(w0, lds128(ak), f32x16{}), P1 = mfma(w1, lds128(ak + 1024), f32x16{});
    u32x4 o0, o1;
    dual_softmax1(P0, sl, o0, o1);
    OPAQUE(o0); OPAQUE(o1);
    sem_wait(SEM_7, 4);
    const u32 apw = lwa(1) + m * 2048 + r * 64 + ((hh ^ ((r >> 1) & 7)) << 3);
    phi_put(apw, o0, o1);
    phi_store(apw + 8192, P1, sl);
    sem_post(SEM_P);
  } else {
    sem_wait(SEM_K + m, 1);
  }
  __builtin_amdgcn_s_setprio(2);
  pq = peek(c, fq, 2);
  const u32x4 wp = wphi_frag(L_WPA + (hd & 1) * 512 + awp, sg);
  WAITV(0);
  sem_post(SEM_B);
  await(c, fq, 2, pq);
  u32x4 p00, p01, p10, p11;
  u64 pkv;
  float rs0, rs1;
  {
    u32x4 qf[2];
#pragma unroll
    for (int u = 0; u < 2; u++) qf[u] = ld128(c.ws, lane * 16, QF_OFF + ((8 * ga + 2 * m + u) * 32 + hd) * 1024);
    rs0 = __uint_as_float(__builtin_amdgcn_raw_buffer_load_b32(c.ws, lane * 8 + 4, RS_OFF + (8 * ga + 2 * m) * 256, AUX));
    ldb(IC<30>{});
    ldb(IC<31>{});
    rs1 = rs0;
    swap32(rs0, rs1);
    const f32x16 Q0 = mfma(wp, qf[0], f32x16{}), Q1 = mfma(wp, qf[1], f32x16{});
    dual_softmax1(Q0, rs0 * LOG2E, p00, p01);
    OPAQUE(p00); OPAQUE(p01);
    SB();
    pkv = peek(c, fk, 16);
    dual_softmax1(Q1, rs1 * LOG2E, p10, p11);
    OPAQUE(p10); OPAQUE(p11);
  }
  WAITV(0);
  OPAQUE(pkv);
#pragma unroll 1
  for (int it = 0; it < (1 << 16); it++) {
    if (__all(pkv == c.own)) {
      sem_post(SEM_S);
      break;
    }
    if (__builtin_amdgcn_readfirstlane(sem_peek(SEM_S))) break;
    pkv = peek(c, fk, 16);
    asm volatile("s_waitcnt vmcnt(0)" : "+v"(pkv)::"memory");
  }
  {
    f32x4 part[8];
#pragma unroll
    for (int s = 0; s < 8; s++) part[s] = __builtin_bit_cast(f32x4, ld128(c.ws, hh * 2048 + r * 16, KV_OFF + (hd * 16 + 2 * s) * 2048 + m * 512));
    f32x4 z = __builtin_bit_cast(f32x4, ld128(c.ws, (lane & 15) * 128 + blk * 16, KZ_OFF + hd * 2048 + m * 32));
    SB();
    float v[4] = {part[0][0], part[0][1], part[0][2], part[0][3]};
#pragma unroll
    for (int s = 1; s < 8; s++)
#pragma unroll
      for (int k = 0; k < 4; k++) {
        v[k] += part[s][k];
        OPAQUE(v[k]);
      }
    swap32(v[0], v[2]);
    swap32(v[1], v[3]);
    v[0] += v[2];
    OPAQUE(v[0]);
    *(LAS u32*)(L_SP0 + r * 32 + m * 8 + hh * 4) = pk(v[0], v[1] + v[3]);
    float za = hh ? z[2] : z[0], zb = hh ? z[3] : z[1];
    sfor<0, 4>([&](auto KK) {
      za += __int_as_float(__builtin_amdgcn_update_dpp(0, __float_as_int(za), 0x120 + (8 >> decltype(KK)::v), 0xf, 0xf, false));
      zb += __int_as_float(__builtin_amdgcn_update_dpp(0, __float_as_int(zb), 0x120 + (8 >> decltype(KK)::v), 0xf, 0xf, false));
    });
    *(LAS u32*)(L_SP0 + (32 + blk) * 32 + m * 8 + hh * 4) = pk(za, zb);
    sem_post(SEM_A);
    sem_wait(SEM_A, 4);
    const u32 e = blk == 0 ? hh * 16 + (lane & 15) : 32 + hh;
    const u32x4 k0 = lds128(L_SP0 + e * 32), k1 = lds128(L_SP0 + e * 32 + 16);
    f32x16 Z0 = mfma(k0, p00, f32x16{}), Z1 = mfma(k0, p10, f32x16{});
    Z0 = mfma(k1, p01, Z0);
    Z1 = mfma(k1, p11, Z1);
    const float r0 = __builtin_amdgcn_rcpf(mul1(Z0[8] + 1e-6f, rs0)), r1 = __builtin_amdgcn_rcpf(mul1(Z1[8] + 1e-6f, rs1));
    u32x4 o0, o1;
#pragma unroll
    for (int d = 0; d < 4; d++) {
      o0[d] = pk(mul1(Z0[2 * d], r0), mul1(Z0[2 * d + 1], r0));
      o1[d] = pk(mul1(Z1[2 * d], r1), mul1(Z1[2 * d + 1], r1));
    }
    st128(o0, c.ws, lane * 16, OF_OFF + ((8 * ga + 2 * m) * 32 + hd) * 1024);
    st128(o1, c.ws, lane * 16, OF_OFF + ((8 * ga + 2 * m + 1) * 32 + hd) * 1024);
  }
  bo.issue<32>(c);
  bo.issue<33>(c);
  WAITV(4);
  publish(c, 0xff00000000ull);
  WAITV(1);
  ldb(IC<32>{});
  ldb(IC<33>{});
  WAITL0();
  bo.issue<34>(c);
  bo.issue<35>(c);
  const u32 orow = 64 * i + 32 * (m >> 1) + r, oidx = orow * 512 + 64 * jc + 32 * (m & 1) + 8 * hh;
  u32x4 xr[2] = {*(const GAS u32x4*)(x + oidx), *(const GAS u32x4*)(x + oidx + 16)};
  float frs = __uint_as_float(__builtin_amdgcn_raw_buffer_load_b32(c.ws, orow * 8 + 4, RS_OFF, AUX));
  u64 po = peek(c, fo, 8);
  const u32 fx = mb + 832 + (m >> 1) * 32;
  u32x4 sel[2];
  {
    const u32 one = (((r >> 2) & 1) == hh) ? ((r & 1) ? 0x3f800000u : 0x3f80u) : 0u, dw = ((r >> 3) & 1) * 2 + ((r >> 1) & 1);
#pragma unroll
    for (int d = 0; d < 4; d++) {
      sel[0][d] = (dw == (u32)d && blk == 0) ? one : 0u;
      sel[1][d] = (dw == (u32)d && blk == 1) ? one : 0u;
    }
    OPAQUE(sel[0]); OPAQUE(sel[1]);
  }
  WAITV(0);
  OPAQUE(xr[0]); OPAQUE(xr[1]); OPAQUE(frs);
  ldb(IC<34>{});
  ldb(IC<35>{});
  await(c, fo, 8, po);
  u64 px = peek(c, fx, 4);
  u32x4 af[8][2];
#pragma unroll
  for (int n = 0; n < 8; n++)
#pragma unroll
    for (int u = 0; u < 2; u++) af[n][u] = ld128(c.ws, lane * 16, OF_OFF + ((2 * i + ((m >> 1) ^ u)) * 32 + 8 * m + n) * 1024);
  WAITV(16);
  OPAQUE(px);
  auto finish = [&](auto EE) {
  constexpr bool EARLY = decltype(EE)::v;
  u32x4 f[7][2];
  auto ldx = [&]() {
#pragma unroll
    for (int k = 0; k < 4; k++)
#pragma unroll
      for (int u = 0; u < 2; u++) f[3 + k][u] = ld128(c.ws, lane * 16, XP_OFF + b * 32768 + k * 8192 + m * 2048 + u * 1024);
    SB();
  };
  if constexpr (EARLY) ldx();
  f32x16 acc[4];
  constexpr int NSM = 4;
  sfor<0, NSM>([&](auto NN) {
    constexpr int N = decltype(NN)::v;
    if constexpr (N == 0) {
      mma0(acc[0], bf[N][0], af[N][0]); mma0(acc[1], bf[N][1], af[N][0]); mma0(acc[2], bf[N][0], af[N][1]); mma0(acc[3], bf[N][1], af[N][1]);
    } else {
      mma(acc[0], bf[N][0], af[N][0]); mma(acc[1], bf[N][1], af[N][0]); mma(acc[2], bf[N][0], af[N][1]); mma(acc[3], bf[N][1], af[N][1]);
    }
  });

  sfor<0, 4>([&](auto KK) {
    constexpr int k = 3 - decltype(KK)::v;
    sfor<NSM, 8>([&](auto NN) {
      constexpr int N = decltype(NN)::v;
      mma(acc[k], bf[N][k & 1], af[N][k >> 1]);
      if constexpr (N == NSM + 1 && k < 3) {
        SB();
        put_block<40 + k>(c, acc[k + 1]);
        if constexpr (k == 0) sem_post(SEM_H);
        SB();
      }
    });
  });
  {
    if constexpr (!EARLY) {
      await(c, fx, 4, px);
      ldx();
    }
    sem_wait(SEM_H, 4);
    get_block<40>(c, m ^ 1, f[0]);
    get_block<41>(c, m ^ 2, f[1]);
    get_block<42>(c, m ^ 3, f[2]);
    sem_wait(SEM_C, 4);
#pragma unroll
    for (int k = 0; k < 7; k++) { mma(acc[0], sel[0], f[k][0]); mma(acc[0], sel[1], f[k][1]); }
  }
  if (threadIdx.x == 319) __hip_atomic_store((GAS u64*)(c.wsp + EP_OFF + b * 8), c.tag, __ATOMIC_RELAXED, __HIP_MEMORY_SCOPE_AGENT);
  asm volatile("s_nop 15" : "+v"(acc[0]));
#pragma unroll
  for (int h = 0; h < 2; h++) {
    float e[8];
#pragma unroll
    for (int k = 0; k < 8; k++) e[k] = acc[0][8 * h + k];
#pragma unroll
    for (int k = 0; k < 4; k++) swap32(e[k], e[4 + k]);
    u32x4 o;
#pragma unroll
    for (int d = 0; d < 4; d++)
      o[d] = pk(__builtin_fmaf(e[2 * d], frs, __uint_as_float(xr[h][d] << 16)), __builtin_fmaf(e[2 * d + 1], frs, __uint_as_float(xr[h][d] & 0xffff0000u)));
    *(GAS u32x4*)(out + oidx + 16 * h) = o;
  }
  };
  if (__all(px == c.own)) finish(IC<1>{});
  else finish(IC<0>{});
}

template <bool FFN> struct Slots {
  static constexpr int NBK = FFN ? 7 : 6, N7 = 28 * NBK, NS = N7 + 5 * NBK;
  static constexpr int blk(int n) {
    if (n < N7) return n % NBK;
    const int k = (n - N7) / 5;
    return (!FFN && k < 4) ? (k + 1) & 3 : k;
  }
  static constexpr int step(int n) { return n < N7 ? n / NBK : ((n - N7) % 5 == 0 ? -1 : 27 + (n - N7) % 5); }
  static constexpr int bar(int G) { return (G < 7 ? (4 * G + 3) * NBK : NS - NBK) + 1; }
};
template <bool FFN>
DI void compute1(Ctx& c, const Tile& t, const u16* w2) {
  u32x4 xa, xb;
  using SL = Slots<FFN>;
  constexpr int NBK = SL::NBK;
  const u32 lane = c.lane, m = c.m;
  const int rt = t.rt, j = t.j;
  const u32 hh = lane >> 5, blk = (lane >> 4) & 1, q = (lane & 15) >> 2, p = lane & 3, r = lane & 31;
  u32 ax[4], aw[2][2], ac;
#pragma unroll
  for (int s = 0; s < 4; s++) ax[s] = m * 4096 + r * 128 + (((2 * s + hh) ^ ((r >> 1) & 7)) << 4);
  const u32 at = (((2 * blk + (p >> 1)) ^ ((q >> 1) << 2)) << 4) + 8 * (p & 1);
  {
    const u32 a = (8 * hh + q) * 128 + at;
    aw[0][0] = a + LWA; aw[0][1] = (a ^ 64) + LWA; aw[1][0] = a + LWB; aw[1][1] = (a ^ 64) + LWB;
    ac = (8 * hh + q) * 64 + 32 * blk + 8 * p + LWB + 8192;
    OPAQUE(aw[0][0]); OPAQUE(aw[0][1]); OPAQUE(aw[1][0]); OPAQUE(aw[1][1]); OPAQUE(ac);
  }
  u32 acs = L_ST + r * 8;
  OPAQUE(acs);
  auto rdw = [&](auto NN) -> u32x4 {
    constexpr int T = SL::step(decltype(NN)::v), M = SL::blk(decltype(NN)::v);
    if constexpr (T < 0) {
      const u32x2 v = *(LAS u32x2*)(acs + M * 256);
      return __builtin_shufflevector(v, v, 0, 1, -1, -1);
    }
    else {
      constexpr int S = (T >> 2) % 3, s = T & 3;
      if constexpr (M < 6) {
        const u32 a = (M < 4 ? aw[0][M & 1] + S * 16384 + (M >> 1) * 8192 : aw[1][M & 1] + S * 12288) + s * 2048;
        return trd2(a, a + 512);
      } else {
        const u32 a = ac + S * 12288 + s * 1024;
        return trd2(a, a + 256);
      }
    }
  };
  u32x4 xf[4], wf[2 * NBK];
  u32x2 fm;
  f32x16 acc[NBK], P0, P1;
  float rstd;
  u32x2 sv[8];
  u32x4 eo, af[28];
  float sa;
  const u32 tb = rt * 4 + m;
  const u32 obase = FFN ? HF_OFF + tb * 112 * 1024 : QF_OFF + (tb * 32 + 4 * j) * 1024;
  WAITL0();
  WAITV(NCP);
  BAR();
  xf[0] = lds128(ax[0]);
  sfor<0, NBK>([&](auto NN) { wf[decltype(NN)::v] = rdw(NN); });
  SB();
  auto filler = [&](auto NN) {
    constexpr int n = decltype(NN)::v;
    if constexpr (n == SL::bar(0) - 5) {
      WAITV(0);
      xa = lds128(L_ST + m * 2048 + lane * 16);
      xb = lds128(L_ST + m * 2048 + 1024 + lane * 16);
      const u32 a = LBS + m * 4096 + (4 * hh + q) * 128 + at;
#pragma unroll
      for (int k = 0; k < 8; k++) sv[k] = trd((a ^ ((k >> 2) * 64)) + (k & 3) * 1024);
      set_tag(c);
    }
    if constexpr (n == SL::bar(0) - 2) {
      u32 ones = ONES2;
      asm volatile("" : "+s"(ones));
      float st[4] = {0.f, 0.f, 0.f, 0.f}, cs[2] = {0.f, 0.f};
#pragma unroll
      for (int d = 0; d < 4; d++) {
        st[0] = dot2(ones, xa[d], st[0]);
        st[1] = dot2(xa[d], xa[d], st[1]);
        st[2] = dot2(ones, xb[d], st[2]);
        st[3] = dot2(xb[d], xb[d], st[3]);
      }
#pragma unroll
      for (int d = 0; d < 8; d++) {
        cs[0] = dot2(ones, sv[d >> 1][d & 1], cs[0]);
        cs[1] = dot2(ones, sv[4 + (d >> 1)][d & 1], cs[1]);
      }
      swap32(st[0], st[1]);
      swap32(st[2], st[3]);
      swap32(cs[0], cs[1]);
      *(LAS float*)(L_ST + m * 2048 + lane * 4) = cs[0] + cs[1];
      st[0] += st[1];
      OPAQUE(st[0]);
      st[2] += st[3];
      auto sw16 = __builtin_amdgcn_permlane16_swap(__float_as_uint(st[0]), __float_as_uint(st[2]), false, false);
      sa = __uint_as_float(sw16[0]) + __uint_as_float(sw16[1]);
      OPAQUE(sa);
    }
    if constexpr (n == SL::bar(0) - 1) {
      sfor<0, 4>([&](auto KK) { sa += __int_as_float(__builtin_amdgcn_update_dpp(0, __float_as_int(sa), 0x120 + (8 >> decltype(KK)::v), 0xf, 0xf, false)); });
      float mean = sa * (1.f / 512.f), ms = mean;
      swap32(mean, ms);
      ms = __builtin_amdgcn_rsqf(mx(__builtin_fmaf(-mean, mean, ms), 0.f) + 1e-5f);
      const u32 mh = pk(-mean, -mean);
      const float ml = -mean - __uint_as_float(mh & 0xffff0000u);
      *(LAS u32x2*)(L_ST + m * 2048 + 1024 + (((lane >> 4) & 1) | ((lane & 15) << 1) | (lane & 32)) * 8) = u32x2{(mh & 0xffffu) | (pk(ml, ml) & 0xffff0000u), __float_as_uint(ms)};
    }
    if constexpr (n >= SL::N7 + 5) {
      constexpr int g = (n - SL::N7) % 5, M = SL::blk(n - 5);
      if constexpr (g >= 1) {
        constexpr int s = (g - 1) >> 1, h = (g - 1) & 1;
        if constexpr (FFN || M < 4) {
          eo[2 * h] = pk2(pr<8 * s + 4 * h>(acc[M]));
          eo[2 * h + 1] = pk2(pr<8 * s + 4 * h + 2>(acc[M]));
          if constexpr (FFN) {
            eo[2 * h] = relu2(eo[2 * h]);
            eo[2 * h + 1] = relu2(eo[2 * h + 1]);
          }
          if constexpr (h == 0) OPAQUE(eo);
        }
        if constexpr (h == 1) {
          if constexpr (FFN) {
            af[2 * M + s] = eo;
            st128(eo, c.ws, lane * 16, obase + ((t.c0 >> 5) + 2 * M + s) * 1024);
          }
          else if constexpr (M == 1) {
            *(LAS u32x4*)(((m >> 1) ? L_SP2 : L_SP1) + (m & 1) * 2048 + s * 1024 + lane * 16) = eo;
            if constexpr (s == 1) sem_post(SEM_K + m);
          } else if constexpr (M == 0) {
            const u32x4 wp = wphi_frag(L_WPHI + s * 512 + (4 * hh + q) * 32 + p * 8, blk ? 0x80008000u : 0u);
            asm volatile("s_nop 1\n\tv_mfma_f32_32x32x16_bf16 %0, %1, %2, 0" : "=&v"(s ? P1 : P0) : "v"(wp), "v"(eo));
          } else if constexpr (M < 4) st128(eo, c.ws, lane * 16, obase + (2 * (M - 2) + s) * 1024);
        }
      }
    }
  };
  sfor<0, SL::NS>([&](auto NN) {
    constexpr int n = decltype(NN)::v, T = SL::step(n), M = SL::blk(n);
    constexpr bool TR = !FFN && M >= 4;
    const u32x4 xo = xf[T & 3], wo_ = wf[n % (2 * NBK)];
    if constexpr (T < 0) {
      const u32x2 cf = __builtin_shufflevector(wo_, wo_, 0, 1);
      mma8(acc[M], TR ? fm : cf, TR ? cf : fm);
    } else if constexpr (T == 0) mma0(acc[M], TR ? xo : wo_, TR ? wo_ : xo);
    else mma(acc[M], TR ? xo : wo_, TR ? wo_ : xo);
    constexpr int G = n < SL::N7 ? n / (4 * NBK) : 7;
    if constexpr (n == SL::bar(G) && G < 7) {
      WAITL0();
      BAR();
    }
    if constexpr (n + NBK < SL::NS && n != SL::bar(G) - 1) {
      if constexpr (n < SL::N7 && (n == SL::bar(G) || (M == 0 && (T & 3) != 3))) {
        if constexpr (T == 27) {
          fm = *(LAS u32x2*)(hh ? L_ZERO : L_ST + 4096 + (m * 32 + r) * 8);
          xf[0] = lds128(ax[0] + 16384); xf[1] = lds128(ax[1] + 16384); xf[2] = lds128(ax[2] + 16384);
        } else xf[(T + 1) & 3] = lds128(ax[(T + 1) & 3] + (((T + 1) >> 2) % 3) * 16384);
      }
      if constexpr (n == SL::bar(G)) wf[(n - 1 + NBK) % (2 * NBK)] = rdw(IC<n - 1 + NBK>{});
      wf[(n + NBK) % (2 * NBK)] = rdw(IC<n + NBK>{});
      if constexpr (n == SL::N7) {
        xf[3] = lds128(ax[3] + 16384);
        if constexpr (!FFN) rstd = *(LAS float*)(L_RS + m * 128 + r * 4);
      }
    }
    if constexpr (n == SL::N7) __builtin_amdgcn_s_setprio(2);
    if constexpr (n == SL::NS - NBK - 1) sem_post(SEM_7);
    if constexpr (FFN && n == SL::N7 + 14) sem_post(SEM_K + m);
    filler(NN);
    SB();
  });
  __builtin_amdgcn_s_setprio(3);
  u32 ab[3][2];
#pragma unroll
  for (int k = 0; k < 6; k++) {
    ab[k >> 1][k & 1] = (((4 * hh + q) * 128 + at) ^ ((k & 1) * 64)) + (k >> 1) * 57344;
    OPAQUE(ab[k >> 1][k & 1]);
  }
  constexpr auto stp = [](int u) { return u < 96 ? u >> 2 : 24 + (u & 3); };
  constexpr auto blo = [](int u) { return u < 96 ? u & 3 : (u - 96) >> 2; };
  auto rd2 = [&](auto UU) -> u32x4 {
    constexpr int n = stp(decltype(UU)::v), k = blo(decltype(UU)::v);
    constexpr u32 L = bloc(n >> 1), W = L / 57344;
    const u32 a = ab[W][k & 1] + (L - W * 57344) + ((n & 1) * 2 + (k >> 1)) * 2048;
    return trd2(a, a + 1024);
  };
  u32x4 w2f[8];
  if constexpr (FFN) {
    sem_wait(SEM_B0, 4);
    sfor<0, 4>([&](auto UU) { w2f[decltype(UU)::v] = rd2(UU); });
    asm volatile("s_nop 15" : "+v"(acc[6]));
    sfor<0, 2>([&](auto SS) {
      u32x4 o = pack8<decltype(SS)::v>(acc[6]);
#pragma unroll
      for (int d = 0; d < 4; d++) o[d] = relu2(o[d]);
      af[12 + decltype(SS)::v] = o;
      st128(o, c.ws, lane * 16, obase + ((t.cc >> 5) + decltype(SS)::v) * 1024);
    });
  } else {
    const u32 apw = lx(1) + m * 2048 + r * 64 + ((hh ^ ((r >> 1) & 7)) << 3);
    WAITV(0);
    publish(c, 0xf00ull);
    sem_wait(SEM_7, 4);
    phi_store(apw, P0, rstd * LOG2E);
    SB();
    const u32 av0 = L_ST + m * 2048 + lane * 16, av1 = lwb(1) + m * 2048 + lane * 16;
    sfor<0, 2>([&](auto TT) {
      constexpr int tt = decltype(TT)::v;
      const f32x4 ra = ldsf4(L_RS + m * 128 + (16 * tt + 4 * hh) * 4), rb = ldsf4(L_RS + m * 128 + (16 * tt + 8 + 4 * hh) * 4);
      const f32x4 a0 = q4<8 * tt>(acc[4]) * ra, b0 = q4<8 * tt + 4>(acc[4]) * rb, a1 = q4<8 * tt>(acc[5]) * ra, b1 = q4<8 * tt + 4>(acc[5]) * rb;
      *(LAS u32x4*)(av0 + tt * 1024) = u32x4{pk(a0[0], a0[1]), pk(a0[2], a0[3]), pk(b0[0], b0[1]), pk(b0[2], b0[3])};
      *(LAS u32x4*)(av1 + tt * 1024) = u32x4{pk(a1[0], a1[1]), pk(a1[2], a1[3]), pk(b1[0], b1[1]), pk(b1[2], b1[3])};
    });
    phi_store(apw + 8192, P1, rstd * LOG2E);
    sem_post(SEM_P);
    sem_wait(SEM_P, 8);
    const u32 t0 = 4 * hh + q;
    const u32 apr = ((m >> 1) ? lwa(1) : lx(1)) + (m & 1) * 8192 + t0 * 64 + (((4 * blk + p) ^ (t0 >> 1)) << 3);
    const u32 avr = ((m >> 1) ? lwb(1) : L_ST) + lane * 16;
    const bool isv = blk == (m & 1);
    u32x4 pa[8], vb[8];
#pragma unroll
    for (int k = 0; k < 8; k++) {
      pa[k] = trd2(apr + k * 1024, (apr ^ 32) + k * 1024 + 512);
      vb[k] = lds128(avr + k * 1024);
    }
    sem_post(SEM_KV);
    f32x16 X = f32x16{};
#pragma unroll
    for (int k = 0; k < 8; k++) {
#pragma unroll
      for (int d = 0; d < 4; d++) vb[k][d] = isv ? vb[k][d] : ONES2;
      X = mfma(pa[k], vb[k], X);
    }
    const u32 pi = (4 * j + m) * 16 + rt;
    const u32 ve = isv ? KV_OFF + pi * 2048 + (hh * 16 + (lane & 15)) * 16 : KZ_OFF + pi * 128 + hh * 16, vs = isv ? 512 : 32;
    st128(__builtin_bit_cast(u32x4, q4<0>(X)), c.ws, ve, 0);
    st128(__builtin_bit_cast(u32x4, q4<4>(X)), c.ws, ve + vs, 0);
    st128(__builtin_bit_cast(u32x4, q4<8>(X)), c.ws, ve + 2 * vs, 0);
    st128(__builtin_bit_cast(u32x4, q4<12>(X)), c.ws, ve + 3 * vs, 0);
  }
  __builtin_amdgcn_s_setprio(0);
  const int b = blockIdx.x;
  BSrc b2;
  b2.init2(c, w2);
  const u32 fh = MB_OFF + b * 1024 + 16 * m + (FFN ? 8 - 8 * c.par : 0);
  const u32 ah = HF_OFF + (tb * 112 + 28 * (b & 3)) * 1024;
  auto lda = [&](auto NN) {
    constexpr int N = decltype(NN)::v;
    af[N] = ld128(c.ws, lane * 16, ah + (2 * aset<(N >> 1)>(c) + (N & 1)) * 1024);
  };
  u64 ph;
  if constexpr (!FFN) {
    ph = peek(c, fh, 2);
    WAITV(1);
    publish(c, 0xffull);
    sem_wait(SEM_KV, 4);
    sfor<10, 14>([&](auto NN) { b2.issue<decltype(NN)::v>(c); });
    sfor<0, 4>([&](auto UU) { w2f[decltype(UU)::v] = rd2(UU); });
    WAITV(9);
    await(c, fh, 2, ph);
    sfor<0, QW>(lda);
    SB();
  } else sem_wait(SEM_7, 4);
  const u32 xb_ = XP_OFF + (b & 3) * 8192 + (m & 1) * 4096;
  const u32 xo0 = xb_ + blk_of(rt, (m >> 1) * 8 + ((b >> 3) & 3)) * 32768, xo1 = xb_ + blk_of(rt, (m >> 1) * 8 + 4 + ((b >> 3) & 3)) * 32768;
  auto send = [&](const f32x16& a, int k) {
    st128(pack8<0>(a), c.ws, lane * 16, (k >> 1 ? xo1 : xo0) + (k & 1) * 2048);
    st128(pack8<1>(a), c.ws, lane * 16, (k >> 1 ? xo1 : xo0) + (k & 1) * 2048 + 1024);
  };
  f32x16 ac2[4];
  u32 sp = 0;
  SB();
  sfor<0, 112>([&](auto UU) {
    constexpr int u = decltype(UU)::v, n = stp(u), k = blo(u);
    if constexpr (u < 4) mma0(ac2[k], w2f[u & 7], af[n]);
    else mma(ac2[k], w2f[u & 7], af[n]);
    SB();
    if constexpr (u == 32 || u == 72) sp = sem_peek(u == 32 ? SEM_B : SEM_B2);
    if constexpr (u == 35 || u == 75) sem_check(sp, u == 35 ? SEM_B : SEM_B2, 4);
    if constexpr (u + 4 < 112) w2f[(u + 4) & 7] = rd2(IC<(u + 4 < 112 ? u + 4 : 0)>{});
    if constexpr (FFN) {
      if constexpr (u < 8) b2.template issue1<10 + (u < 8 ? u >> 1 : 0), (u & 1)>(c);
      if constexpr (u == PUBU) {
        WAITV(8);
        publish(c, 0xfull);
      }
      if constexpr (u == PKU) ph = peek(c, fh, 1);
      if constexpr (u == CKU) {
        WAITV(0);
        sem_post(SEM_B2);
        await(c, fh, 1, ph);
        sfor<14, 28>(lda);
      }
    } else {
      if constexpr (u == 0) sem_post(SEM_B2);
      if constexpr ((u & 3) == 1 && (u >> 2) + QW < 28) lda(IC<((u >> 2) + QW < 28 ? (u >> 2) + QW : 0)>{});
    }
    if constexpr (u >= 100 && (u & 3) == 1) send(ac2[k - 1], k - 1);
    SB();
  });
  asm volatile("s_nop 15" : "+v"(ac2[3]));
  send(ac2[3], 3);
  WAITV(0);
  publish(c, 0x3000ull);
  sem_post(SEM_C);
}

__global__ __launch_bounds__(512, 2) void fused_k(const u16* x, const u16* wqkv, const u16* wphi, const u16* wo, const u16* w1, const u16* w2, u16* out,
                                                  char* ws) {
  __shared__ __attribute__((aligned(16))) char smem[163840];
  asm volatile("" ::"v"((LAS char*)smem));
  const int b = blockIdx.x, xcd = b & 7, r = b >> 3;
  u64 pc0;
  asm volatile("s_getpc_b64 %0" : "=s"(pc0));
  {
    u32 off = ((r * 8 + (threadIdx.x >> 6)) * 3 + (threadIdx.x & 3)) * 64;
    off = off < CODE_BYTES ? off : 0;
    asm volatile("s_mov_b32 m0, %2\n\ts_mov_b64 exec, 7\n\tglobal_load_lds_dword %0, %1\n\ts_mov_b64 exec, -1" ::"v"(off), "s"(pc0), "i"(L_RS) : "memory");
  }
  Ctx c;
  c.lane = threadIdx.x & 63;
  const u32 w = __builtin_amdgcn_readfirstlane(threadIdx.x >> 6);
  c.m = w & 3;
  c.ws = __builtin_amdgcn_make_buffer_rsrc(ws, 0, WS_BYTES, 0x00020000);
  c.wsp = ws;
  c.par = (b >> 4) & 1;
  const int rt = (xcd >> 2) * 8 + (r >> 2), ct = (xcd & 3) * 4 + (r & 3), j = ct >> 1;
  Tile t;
  t.rt = rt;
  t.j = j;
  t.ct = ct;
  t.ffn = ct & 1;
  t.ldw = t.ffn ? 3584 : 3072;
  const u32 lane = c.lane, m = c.m;
  t.srd_x = make_srd(x, 2048 * 512 * 2);
  t.srd_w = make_srd(t.ffn ? w1 : wqkv, 512 * t.ldw);
  t.vx0 = (lane >> 3) * 1024 + (((lane & 7) ^ (lane >> 4)) << 4);
  {
    const u32 sx = (rt * 128 + m * 32 + (w >> 2) * 16) * 1024, dx = m * 4096 + (w >> 2) * 2048;
    dma<LX, 0>(t.srd_x, t.vx0, sx, dx);
    dma<LX + 1024, 8192>(t.srd_x, t.vx0 ^ 64, sx, dx);
    t.vw = (lane >> 3) * t.ldw + (((lane & 7) ^ (((lane >> 4) & 1) << 2)) << 4);
    t.c0 = t.ffn ? 448 * j + (j & 1) * 64 : 1024 + 128 * j;
    t.c1 = t.ffn ? t.c0 + 128 : 128 * j;
    t.c2 = t.ffn ? t.c0 + 256 : 2048 + 128 * j;
    t.cc = t.ffn ? 448 * j + ((j & 1) ? 0 : 384) : 0;
    dma<LWA, 0>(t.srd_w, t.vw, w * 8 * t.ldw + t.c0, w * 1024);
    dma<LWA + 8192, 0>(t.srd_w, t.vw, w * 8 * t.ldw + t.c1, w * 1024);
    dma<LWB, 0>(t.srd_w, t.vw, w * 8 * t.ldw + t.c2, w * 1024);
    if (t.ffn && w >= 4) dma<LWB + 8192, 0>(t.srd_w, (lane >> 2) * 3584 + ((lane & 3) << 4), m * 16 * 3584 + t.cc, m * 1024);
    BAR();
    SB();
  }
  if (__builtin_expect(w >= 4, 1)) {
    helper(c, t, x, wo, w2, out);
  } else {
    {
      const u32 vs = m < 3 ? t.vw : (lane >> 3) * t.ldw + ((lane & 3) << 4);
      const u32 so = rt * 32 * t.ldw + (m == 0 ? t.c0 : m == 1 ? t.c1 : m == 2 ? t.c2 : t.cc);
      dma<LBS, 0>(t.srd_w, vs, so, m * 4096);
      dma<LBS + 1024, 0>(t.srd_w, vs, so + 8 * t.ldw, m * 4096);
      dma<LBS + 2048, 0>(t.srd_w, vs, so + 16 * t.ldw, m * 4096);
      dma<LBS + 3072, 0>(t.srd_w, vs, so + 24 * t.ldw, m * 4096);
      dma<L_ST, 0>(t.srd_x, lane * 16, (rt * 128 + ct * 8 + m * 2) * 1024, m * 2048);
      dma<L_ST + 1024, 1024>(t.srd_x, lane * 16, (rt * 128 + ct * 8 + m * 2) * 1024, m * 2048);
      dma<L_WPHI, 0>(make_srd(wphi, 32 * 512), lane * 16, m < 2 ? (u32)(j * 2048 + m * 1024) : (u32)(((b & 7) * 2 + ((b >> 4) & 1)) * 1024), (m < 2 ? m : 2) * 1024);
      const u32 t1 = t.ffn ? blk_of(rt, (ct & 12) + (lane & 3)) : (lane & 7) * 32 + m * 8 + j, f1 = t.ffn ? m * 2 + (j & 1) : 16 + rt;
      load_tag(c, lane < 8 ? t1 : lane < 12 ? (rt >> 1) * 32 + (lane & 3) * 8 + j : lane < 14 ? blk_of(rt, (m >> 1) * 8 + (lane & 1) * 4 + (ct & 3)) : b,
               lane < 8 ? f1 : lane < 12 ? 96 + (rt & 1) * 4 + m : 104 + (m & 1) * 4 + (ct >> 2));
      *(LAS u32*)(L_SEM + (lane & 31) * 4) = 0;
    }
    if (t.ffn) compute1<true>(c, t, w2);
    else compute1<false>(c, t, w2);
  }
}

extern "C" void kernel_launch(void* const* d_in, const int* in_sizes, int n_in, void* d_out, int out_size, void* d_ws, size_t ws_size,
                              hipStream_t stream) {
  const u16 *x = (const u16*)d_in[0], *wqkv = (const u16*)d_in[1], *wphi = (const u16*)d_in[2], *wo = (const u16*)d_in[3];
  const u16 *w1 = (const u16*)d_in[5], *w2 = (const u16*)d_in[6];
  fused_k<<<256, 512, 0, stream>>>(x, wqkv, wphi, wo, w1, w2, (u16*)d_out, (char*)d_ws);
}
```
